```python
import torch

def _softmax(x, axis=-1):
    return torch.softmax(x, dim=axis)
def _gelu(x):
    return torch.nn.functional.gelu(x, approximate="tanh")
def _silu(x):
    return torch.nn.functional.silu(x)
def _sigmoid(x):
    return torch.sigmoid(x)
def _layernorm(x, axis=-1, eps=1e-5):
    m = x.mean(dim=axis, keepdim=True)
    v = x.var(dim=axis, keepdim=True, unbiased=False)
    return (x - m) / torch.sqrt(v + eps)
def _scatter_add(tgt, idx, src, axis=0):
    return tgt.clone().index_add_(axis, idx, src)
def _segment_sum(src, seg, n):
    out = torch.zeros((n,) + src.shape[1:], dtype=src.dtype)
    return out.index_add_(0, seg, src)
def _pad(x, pw):
    flat = tuple(v for pair in reversed(pw) for v in pair)
    return torch.nn.functional.pad(x, flat)

kernel_name = 'parallel_sum__mha_block__glu_gated_reglu__prompt'

def setup_inputs():

    _w = (lambda a: (lambda t: t.to(torch.bfloat16) if t.is_floating_point() else t)(torch.as_tensor(a)))
    def _A_setup():
        B, S, D, H = 1, 2048, 640, 32
        return {
            "x": _w((lambda *s: torch.randn(*s, dtype=torch.bfloat16) * 0.05)(B, S, D)),
            "wqkv": _w((lambda *s: torch.randn(*s, dtype=torch.bfloat16) * 0.05)(D, 3 * D)),
            "wo": _w((lambda *s: torch.randn(*s, dtype=torch.bfloat16) * 0.05)(D, D)),
        }
    _A_in = _A_setup()

    def _B_setup():
        B, S, D, F = 1, 2048, 640, 1536
        return {
            "x": _w((lambda *s: torch.randn(*s, dtype=torch.bfloat16) * 0.05)(B, S, D)),
            "W_gate": _w((lambda *s: torch.randn(*s, dtype=torch.bfloat16) * 0.4)(D, F) * (2.0 / D) ** 0.5),
            "W_up": _w((lambda *s: torch.randn(*s, dtype=torch.bfloat16) * 0.4)(D, F) * (2.0 / D) ** 0.5),
            "W_down": _w((lambda *s: torch.randn(*s, dtype=torch.bfloat16) * 0.4)(F, D) * (1.0 / F) ** 0.5),
        }
    _B_in = _B_setup()

    return {
        "A_x": _A_in["x"],
        "A_wqkv": _A_in["wqkv"],
        "A_wo": _A_in["wo"],
        "B_x": _B_in["x"],
        "B_W_gate": _B_in["W_gate"],
        "B_W_up": _B_in["W_up"],
        "B_W_down": _B_in["W_down"],
    }

def reference(A_x, A_wqkv, A_wo, B_x, B_W_gate, B_W_up, B_W_down):
    def _A_ref(x, wqkv, wo):
        B, S, D = x.shape
        H = 32
        Dh = D // H
        qkv = (lambda x, *d: x.permute(*d))((x @ wqkv).reshape(B, S, 3, H, Dh), 2, 0, 3, 1, 4)
        q, k, v = qkv[0], qkv[1], qkv[2]
        scores = torch.einsum("bhqd,bhkd->bhqk", q, k) * (Dh ** -0.5)
        p = _softmax(scores, axis=-1)
        o = torch.einsum("bhqk,bhkd->bhqd", p, v)
        return (lambda x, *d: x.permute(*d))(o, 0, 2, 1, 3).reshape(B, S, D) @ wo

    def _B_ref(x, W_gate, W_up, W_down):
        gate = torch.einsum("bsd,df->bsf", x, W_gate)
        up = torch.einsum("bsd,df->bsf", x, W_up)
        gate = gate * (lambda x, dt: x.to(dt))(gate > 0, gate.dtype)
        h = gate * up
        return torch.einsum("bsf,fd->bsd", h, W_down)

    x = A_x
    xn = _layernorm(x, axis=-1)
    return x + _A_ref(xn, A_wqkv, A_wo) + _B_ref(xn, B_W_gate, B_W_up, B_W_down)
```

```cpp
#include <hip/hip_runtime.h>
#include <hip/hip_bf16.h>
#include <stdint.h>

typedef __attribute__((ext_vector_type(8))) short bf16x8;
typedef __attribute__((ext_vector_type(4))) short bf16x4;
typedef __attribute__((ext_vector_type(16))) float f32x16;
typedef __attribute__((ext_vector_type(4))) float f32x4;
typedef __attribute__((ext_vector_type(2))) float f32x2;
typedef __attribute__((ext_vector_type(4))) unsigned u32x4;
typedef __attribute__((ext_vector_type(2))) unsigned u32x2;
typedef __attribute__((ext_vector_type(2))) __bf16 bf2;
#define LAS __attribute__((address_space(3)))
#define DI __device__ __forceinline__

constexpr int S_ = 2048, D_ = 640, H_ = 32, F_ = 1536;
constexpr size_t WS_Q = 0;
constexpr size_t WS_KV = WS_Q + 32 * 64 * 1280;
constexpr size_t WS_XN = WS_KV + 32 * 64 * 3072;
constexpr size_t WS_H = WS_XN + 64 * 40 * 1024;
constexpr size_t WS_O = WS_H + 64 * 96 * 1024;
constexpr size_t WS_Y = WS_O + 64 * 40 * 1024;

#define MFMA16(a, b, c) __builtin_amdgcn_mfma_f32_32x32x16_bf16((a), (b), (c), 0, 0, 0)
#define MFMA8(a, b, c) __builtin_amdgcn_mfma_f32_32x32x8bf16_1k((a), (b), (c), 0, 0, 0)
#define MFMA16X16(a, b, c) __builtin_amdgcn_mfma_f32_16x16x32_bf16((a), (b), (c), 0, 0, 0)
#define WAIT_BAR(N) asm volatile("s_waitcnt vmcnt(" #N ") lgkmcnt(0)\n\ts_barrier" ::: "memory")
#define BAR_L() asm volatile("s_waitcnt lgkmcnt(0)\n\ts_barrier" ::: "memory")

DI void glds16(unsigned voff, const void* sbase, unsigned lds) {
    unsigned sv;
    asm volatile("s_mov_b32 %0, m0\n\ts_mov_b32 m0, %3\n\ts_nop 0\n\tglobal_load_lds_dwordx4 %1, %2\n\ts_mov_b32 m0, %0"
                 : "=&s"(sv) : "v"(voff), "s"(sbase), "s"(lds) : "memory");
}
DI void glds16x3(unsigned voff, const void* sbase, unsigned lds) {
    unsigned sv;
    asm volatile("s_mov_b32 %0, m0\n\ts_mov_b32 m0, %3\n\ts_nop 0\n\tglobal_load_lds_dwordx4 %1, %2\n\t"
                 "global_load_lds_dwordx4 %1, %2 offset:1024\n\tglobal_load_lds_dwordx4 %1, %2 offset:2048\n\ts_mov_b32 m0, %0"
                 : "=&s"(sv) : "v"(voff), "s"(sbase), "s"(lds) : "memory");
}
DI bf16x8 lds128(LAS char* p) { return *(LAS bf16x8*)p; }
DI bf16x4 lds64(LAS char* p) { return *(LAS bf16x4*)p; }
DI bf16x4 ldstr(LAS char* p) { return __builtin_amdgcn_ds_read_tr16_b64_v4i16((LAS bf16x4*)p); }
DI bf16x8 cat(bf16x4 a, bf16x4 b) { return __builtin_shufflevector(a, b, 0, 1, 2, 3, 4, 5, 6, 7); }
DI unsigned pk(float a, float b) { f32x2 f = {a, b}; return __builtin_bit_cast(unsigned, __builtin_convertvector(f, bf2)); }
DI float bflo(unsigned u) { return __uint_as_float(u << 16); }
DI float bfhi(unsigned u) { return __uint_as_float(u & 0xffff0000u); }
DI float dot2(unsigned a, unsigned b, float c) { return __builtin_amdgcn_fdot2_f32_bf16(__builtin_bit_cast(bf2, a), __builtin_bit_cast(bf2, b), c, false); }
DI float swap32_add(float v) { auto r = __builtin_amdgcn_permlane32_swap(__float_as_uint(v), __float_as_uint(v), false, false); return __uint_as_float(r[0]) + __uint_as_float(r[1]); }
DI float swap32_max(float v) { auto r = __builtin_amdgcn_permlane32_swap(__float_as_uint(v), __float_as_uint(v), false, false); return __builtin_fmaxf(__uint_as_float(r[0]), __uint_as_float(r[1])); }

DI void ln_role(const char* x, char* ws, int rb) {
    const int lane = threadIdx.x & 63, w = threadIdx.x >> 6;
    const int row = rb * 128 + w * 16 + (lane & 15), part = lane >> 4;
    const char* src = x + (size_t)row * 1280 + part * 16;
    u32x4 v[20];
#pragma unroll
    for (int i = 0; i < 20; ++i) v[i] = *(const u32x4*)(src + i * 64);
    float s = 0.f;
#pragma unroll
    for (int i = 0; i < 20; ++i)
#pragma unroll
        for (int e = 0; e < 4; ++e) s = dot2(v[i][e], 0x3F803F80u, s);
    s += __shfl_xor(s, 16); s += __shfl_xor(s, 32);
    const float mu = s * (1.f / 640.f);
    float q = 0.f;
#pragma unroll
    for (int i = 0; i < 20; ++i)
#pragma unroll
        for (int e = 0; e < 4; ++e) { const float a = bflo(v[i][e]) - mu, b = bfhi(v[i][e]) - mu; q = __builtin_fmaf(a, a, q); q = __builtin_fmaf(b, b, q); }
    q += __shfl_xor(q, 16); q += __shfl_xor(q, 32);
    const float rs = __builtin_amdgcn_rsqf(q * (1.f / 640.f) + 1e-5f), nb = -mu * rs;
    char* dst = ws + WS_XN + (size_t)(row >> 5) * 40960 + (row & 31) * 16 + (part >> 1) * 1024 + (part & 1) * 512;
#pragma unroll
    for (int i = 0; i < 20; ++i) {
        u32x4 o;
#pragma unroll
        for (int e = 0; e < 4; ++e) o[e] = pk(__builtin_fmaf(bflo(v[i][e]), rs, nb), __builtin_fmaf(bfhi(v[i][e]), rs, nb));
        *(u32x4*)(dst + i * 2048) = o;
    }
}

template <bool VT>
DI void qkv_tile(const char* x, const char* wqkv, char* ws, LAS char* lds, int rb, int cb) {
    const int lane = threadIdx.x & 63, w = __builtin_amdgcn_readfirstlane(threadIdx.x >> 6);
    const int wm = w >> 2, wn = w & 3, r = lane & 31, h = lane >> 5;
    const unsigned lds0 = (unsigned)(uintptr_t)lds;
    const int ra0 = 16 * w + (lane >> 3), ra1 = ra0 + 8;
    const unsigned voffA0 = ra0 * 1280 + (((lane & 7) ^ ((ra0 >> 1) & 7)) << 4);
    const unsigned voffA1 = ra1 * 1280 + (((lane & 7) ^ ((ra1 >> 1) & 7)) << 4);
    const unsigned voffW = (8 * w + ((lane >> 2) & 3)) * 3840 + (lane >> 4) * 64 + (lane & 3) * 16;
    const char* xa = x + (size_t)rb * 128 * 1280;
    const char* wb = wqkv + cb * 256;
#define K1_STAGE(kt) do { const unsigned lb_ = lds0 + ((kt) & 3) * 32768 + w * 2048; \
        glds16(voffA0, xa + (kt) * 128, lb_); glds16(voffA1, xa + (kt) * 128, lb_ + 1024); \
        glds16(voffW, wb + (kt) * 245760, lb_ + 16384); glds16(voffW, wb + (kt) * 245760 + 15360, lb_ + 16384 + 1024); } while (0)
    K1_STAGE(0); K1_STAGE(1); K1_STAGE(2);
    const int rot = wn ^ (2 * wm);
    const int xrow = wm * 64 + r;
    const unsigned ax0 = xrow * 128 + (((2 * rot + h) ^ ((xrow >> 1) & 7)) << 4);
    const unsigned aw0 = 16384 + (4 * rot + 2 * h) * 1024 + wn * 256 + ((lane >> 2) & 3) * 64 + ((lane >> 4) & 1) * 32 + (lane & 3) * 8;
    f32x16 acc0 = {}, acc1 = {};
    float sx0 = 0.f, sq0 = 0.f, sx1 = 0.f, sq1 = 0.f, cw = 0.f;
    for (int kt = 0; kt < 10; ++kt) {
        if (kt <= 7) { WAIT_BAR(8); } else if (kt == 8) { WAIT_BAR(4); } else { WAIT_BAR(0); }
        if (kt < 7) K1_STAGE(kt + 3);
        LAS char* b = lds + (kt & 3) * 32768;
#pragma unroll
        for (int p = 0; p < 4; ++p) {
            const bf16x8 x0 = lds128(b + (ax0 ^ (p * 32))), x1 = lds128(b + (ax0 ^ (p * 32)) + 4096);
            const bf16x8 wf = cat(ldstr(b + (aw0 ^ (p << 12))), ldstr(b + (aw0 ^ (p << 12)) + 1024));
            if (VT) { acc0 = MFMA16(x0, wf, acc0); acc1 = MFMA16(x1, wf, acc1); }
            else { acc0 = MFMA16(wf, x0, acc0); acc1 = MFMA16(wf, x1, acc1); }
            if (p == 0) {
                const u32x4 u0 = __builtin_bit_cast(u32x4, x0), u1 = __builtin_bit_cast(u32x4, x1);
#pragma unroll
                for (int e = 0; e < 4; ++e) { sx0 = dot2(u0[e], 0x3F803F80u, sx0); sq0 = dot2(u0[e], u0[e], sq0); sx1 = dot2(u1[e], 0x3F803F80u, sx1); sq1 = dot2(u1[e], u1[e], sq1); }
            }
            if (p < 2) {
                const u32x4 uw = __builtin_bit_cast(u32x4, wf);
#pragma unroll
                for (int e = 0; e < 4; ++e) cw = dot2(uw[e], 0x3F803F80u, cw);
            }
        }
    }
#undef K1_STAGE
    sx0 = swap32_add(sx0); sq0 = swap32_add(sq0); sx1 = swap32_add(sx1); sq1 = swap32_add(sq1); cw = swap32_add(cw);
    BAR_L();
    LAS float* ps = (LAS float*)lds;
    LAS float* pc = (LAS float*)(lds + 4096);
    LAS float* fa = (LAS float*)(lds + 8192);
    LAS float* fb = (LAS float*)(lds + 8704);
    LAS float* fc = (LAS float*)(lds + 9216);
    { const f32x2 t = {h ? sx1 : sx0, h ? sq1 : sq0}; *(LAS f32x2*)(ps + (wn * 128 + wm * 64 + lane) * 2) = t; }
    if (h == 0) pc[wm * 128 + wn * 32 + r] = cw;
    BAR_L();
    if (threadIdx.x < 128) {
        const int t = threadIdx.x;
        float s = 0.f, q = 0.f;
#pragma unroll
        for (int i = 0; i < 4; ++i) { const f32x2 v = *(LAS f32x2*)(ps + (i * 128 + t) * 2); s += v[0]; q += v[1]; }
        const float mu = s * (1.f / 640.f);
        const float var = __builtin_fmaxf(__builtin_fmaf(-mu, mu, q * (1.f / 640.f)), 0.f);
        float rs = __builtin_amdgcn_rsqf(var + 1e-5f);
        if (cb < 5) rs *= 0.22360679774997896f * 1.4426950408889634f;
        fa[t] = rs; fb[t] = -mu * rs;
    } else if (threadIdx.x < 256) {
        const int t = threadIdx.x - 128;
        fc[t] = pc[t] + pc[128 + t];
    }
    BAR_L();
    if (!VT) {
        const bool isq = cb < 5;
        char* base = ws + (isq ? WS_Q : WS_KV);
        const int rec = isq ? 1280 : 3072;
        const int nb = (cb % 5) * 128 + wn * 32 + 4 * h;
        const float a0 = fa[xrow], b0 = fb[xrow], a1 = fa[xrow + 32], b1 = fb[xrow + 32];
        const int tile = rb * 4 + wm * 2;
#pragma unroll
        for (int g = 0; g < 4; ++g) {
            const f32x4 cs = *(LAS f32x4*)(fc + wn * 32 + 8 * g + 4 * h);
            const int n0 = nb + 8 * g, hd = n0 / 20, c = (n0 - hd * 20) >> 2;
            const unsigned off = (unsigned)(hd * 64 + tile) * rec + (c < 4 ? (c >> 1) * 512 + r * 16 + (c & 1) * 8 : 1024 + r * 8);
            u32x2 o0, o1;
            o0[0] = pk(a0 * acc0[4 * g] + b0 * cs[0], a0 * acc0[4 * g + 1] + b0 * cs[1]); o0[1] = pk(a0 * acc0[4 * g + 2] + b0 * cs[2], a0 * acc0[4 * g + 3] + b0 * cs[3]);
            o1[0] = pk(a1 * acc1[4 * g] + b1 * cs[0], a1 * acc1[4 * g + 1] + b1 * cs[1]); o1[1] = pk(a1 * acc1[4 * g + 2] + b1 * cs[2], a1 * acc1[4 * g + 3] + b1 * cs[3]);
            *(u32x2*)(base + off) = o0; *(u32x2*)(base + off + rec) = o1;
            if (!isq && c == 4) { const u32x2 one = {0x3F80u, 0u}; *(u32x2*)(base + off + 256) = one; *(u32x2*)(base + off + rec + 256) = one; }
        }
    } else {
        const int n = (cb - 10) * 128 + wn * 32 + r, hd = n / 20, d = n - hd * 20;
        const float cs = fc[wn * 32 + r];
        char* base = ws + WS_KV + (unsigned)(hd * 64 + rb * 4 + wm * 2) * 3072 + 1536 + h * 384 + d * 16;
#pragma unroll
        for (int i = 0; i < 2; ++i) {
#pragma unroll
            for (int s = 0; s < 2; ++s) {
                u32x4 o;
#pragma unroll
                for (int gg = 0; gg < 2; ++gg) {
                    const int g = 2 * s + gg;
                    const f32x4 a = *(LAS f32x4*)(fa + wm * 64 + i * 32 + 8 * g + 4 * h), bb = *(LAS f32x4*)(fb + wm * 64 + i * 32 + 8 * g + 4 * h);
                    const f32x16& acc = i ? acc1 : acc0;
                    o[2 * gg] = pk(a[0] * acc[4 * g] + bb[0] * cs, a[1] * acc[4 * g + 1] + bb[1] * cs);
                    o[2 * gg + 1] = pk(a[2] * acc[4 * g + 2] + bb[2] * cs, a[3] * acc[4 * g + 3] + bb[3] * cs);
                }
                *(u32x4*)(base + i * 3072 + s * 768) = o;
                if (d == 19) { const u32x4 one = {0x3F803F80u, 0x3F803F80u, 0x3F803F80u, 0x3F803F80u}; *(u32x4*)(base + i * 3072 + s * 768 + 16) = one; }
            }
        }
    }
}

__global__ __launch_bounds__(512, 2) void k_qkv(const char* __restrict__ x, const char* __restrict__ wqkv, char* __restrict__ ws) {
    extern __shared__ __attribute__((aligned(16))) char smem[];
    const int b = blockIdx.x, j = b & 7, i = b >> 3, rbg = j >> 1;
    int cb;
    if (j & 1) { if (i >= 28) { ln_role(x, ws, 4 * rbg + (i - 28)); return; } cb = 8 + (i >> 2); } else cb = i >> 2;
    const int rb = 4 * rbg + (i & 3);
    if (cb < 10) qkv_tile<false>(x, wqkv, ws, (LAS char*)smem, rb, cb);
    else qkv_tile<true>(x, wqkv, ws, (LAS char*)smem, rb, cb);
}

DI float max16(const f32x16& s) {
    float a = __builtin_fmaxf(__builtin_fmaxf(s[0], s[1]), s[2]), b = __builtin_fmaxf(__builtin_fmaxf(s[3], s[4]), s[5]);
    a = __builtin_fmaxf(__builtin_fmaxf(a, s[6]), s[7]); b = __builtin_fmaxf(__builtin_fmaxf(b, s[8]), s[9]);
    a = __builtin_fmaxf(__builtin_fmaxf(a, s[10]), s[11]); b = __builtin_fmaxf(__builtin_fmaxf(b, s[12]), s[13]);
    a = __builtin_fmaxf(__builtin_fmaxf(a, s[14]), s[15]);
    return swap32_max(__builtin_fmaxf(a, b));
}
DI void exp_pack(const f32x16& s, bf16x8& p0, bf16x8& p1) {
    u32x4 a, b;
#pragma unroll
    for (int e = 0; e < 4; ++e) {
        a[e] = pk(__builtin_amdgcn_exp2f(s[2 * e]), __builtin_amdgcn_exp2f(s[2 * e + 1]));
        b[e] = pk(__builtin_amdgcn_exp2f(s[8 + 2 * e]), __builtin_amdgcn_exp2f(s[8 + 2 * e + 1]));
    }
    p0 = __builtin_bit_cast(bf16x8, a); p1 = __builtin_bit_cast(bf16x8, b);
}

DI void attn_role(char* ws, LAS char* lds, int hd, int qb) {
    const int lane = threadIdx.x & 63, w = __builtin_amdgcn_readfirstlane(threadIdx.x >> 6);
    const int r = lane & 31, h = lane >> 5;
    const unsigned lds0 = (unsigned)(uintptr_t)lds;
    const char* kv = ws + WS_KV + (size_t)hd * 196608;
    const unsigned voff = lane * 16;
    glds16x3(voff, kv + w * 3072, lds0 + w * 3072);
    const int qt = qb * 8 + w;
    const char* qp = ws + WS_Q + (size_t)(hd * 64 + qt) * 1280;
    const bf16x8 q1 = *(const bf16x8*)(qp + lane * 16);
    bf16x4 q2 = *(const bf16x4*)(qp + 1024 + r * 8);
    if (h) q2 = bf16x4{0, 0, 0, 0};
    glds16x3(voff, kv + 24576 + w * 3072, lds0 + 24576 + w * 3072);
    const unsigned aK = lane * 16, aK2 = 1024 + lane * 8, aV = 1536 + h * 384 + (r < 21 ? r : r - 16) * 16;
    const f32x16 zero = {};
    WAIT_BAR(3);
    bf16x8 k1 = lds128(lds + aK); bf16x4 k2 = lds64(lds + aK2);
    f32x16 sc = MFMA8(k2, q2, MFMA16(k1, q1, zero));
    const bf16x4 q2raw = q2;
    { const float m = max16(sc); if (h) q2[0] = (short)(pk(-m, 0.f) & 0xffffu); }
    sc = MFMA8(k2, q2, MFMA16(k1, q1, zero));
    f32x16 o = {};
    int cur = 0;
    for (int s = 0; s < 8; ++s) {
        LAS char* b = lds + cur;
        const int nxt = cur == 49152 ? 0 : cur + 24576;
#pragma unroll
        for (int t = 0; t < 8; ++t) {
            if (t == 7) {
                WAIT_BAR(0);
                if (s < 6) { const int nn = nxt == 49152 ? 0 : nxt + 24576; glds16x3(voff, kv + (s + 2) * 24576 + w * 3072, lds0 + nn + w * 3072); }
            }
            LAS char* bn = t == 7 ? lds + nxt : b + (t + 1) * 3072;
            k1 = lds128(bn + aK); k2 = lds64(bn + aK2);
            const bf16x8 v0 = lds128(b + t * 3072 + aV), v1 = lds128(b + t * 3072 + aV + 768);
            const f32x16 sn = MFMA8(k2, q2, MFMA16(k1, q1, zero));
            bf16x8 p0, p1;
            exp_pack(sc, p0, p1);
            o = MFMA16(v0, p0, o); o = MFMA16(v1, p1, o);
            sc = sn;
        }
        cur = nxt;
    }
    float l;
    { auto rr = __builtin_amdgcn_permlane32_swap(__float_as_uint(o[8]), __float_as_uint(o[8]), false, false); l = __uint_as_float(rr[1]); }
    float am = 0.f;
#pragma unroll
    for (int i = 0; i < 12; ++i) am = __builtin_fmaxf(am, __builtin_fabsf(i >= 8 && h ? 0.f : o[i]));
    const bool ok = (l > 5.4e-20f) && (l < 1.8e19f) && (am < 3.0e38f);
    if (__builtin_expect(!__all(ok), 0)) {
        float m = -INFINITY;
        o = zero;
        for (int t = 0; t < 64; ++t) {
            const char* rec = kv + t * 3072;
            const bf16x8 fk1 = *(const bf16x8*)(rec + aK); const bf16x4 fk2 = *(const bf16x4*)(rec + aK2);
            const bf16x8 fv0 = *(const bf16x8*)(rec + aV), fv1 = *(const bf16x8*)(rec + aV + 768);
            f32x16 sf = MFMA8(fk2, q2raw, MFMA16(fk1, q1, zero));
            const float mn = __builtin_fmaxf(m, max16(sf));
            const float al = __builtin_amdgcn_exp2f(m - mn);
            m = mn;
#pragma unroll
            for (int i = 0; i < 16; ++i) { sf[i] -= mn; o[i] *= al; }
            bf16x8 p0, p1;
            exp_pack(sf, p0, p1);
            o = MFMA16(fv0, p0, o); o = MFMA16(fv1, p1, o);
        }
        auto rr = __builtin_amdgcn_permlane32_swap(__float_as_uint(o[8]), __float_as_uint(o[8]), false, false); l = __uint_as_float(rr[1]);
    }
    const float inv = 1.f / l;
    char* op = ws + WS_O + (size_t)qt * 40960;
    u32x4 oa;
#pragma unroll
    for (int e = 0; e < 4; ++e) oa[e] = pk(o[2 * e] * inv, o[2 * e + 1] * inv);
    *(u32x4*)(op + hd * 1024 + lane * 16) = oa;
    if (h == 0) {
        const u32x2 ob = {pk(o[8] * inv, o[9] * inv), pk(o[10] * inv, o[11] * inv)};
        *(u32x2*)(op + (32 + (hd >> 2)) * 1024 + ((hd >> 1) & 1) * 512 + r * 16 + (hd & 1) * 8) = ob;
    }
}

DI void gu_role(const char* wg, const char* wu, char* ws, LAS char* lds, int rb, int fb) {
    const int lane = threadIdx.x & 63, w = __builtin_amdgcn_readfirstlane(threadIdx.x >> 6);
    const int h = lane >> 5, tt = w & 3, fh = w >> 2;
    const unsigned lds0 = (unsigned)(uintptr_t)lds;
    const int q = (lane >> 2) & 3, c = lane & 3, gi = (lane >> 4) & 1;
    const char* xs = ws + WS_XN + (size_t)((rb * 4 + (w >> 1)) * 40 + (w & 1)) * 1024;
    const unsigned voffX = lane * 16;
    const int m0 = w >= 6, i0 = m0 ? w - 6 : w, st0 = 4 * i0 + (lane >> 4);
    const unsigned voffW0 = (4 * (st0 / 3) + q) * 3072 + (st0 % 3) * 64 + ((m0 ? c ^ 2 : c) << 4);
    const int st1 = 4 * (2 + (w & 3)) + (lane >> 4);
    const unsigned voffW1 = (4 * (st1 / 3) + q) * 3072 + (st1 % 3) * 64 + ((c ^ 2) << 4);
    const char* wb0 = (m0 ? wu : wg) + fb * 192;
    const char* wb1 = wu + fb * 192;
#define GU_STAGE(kt) do { const unsigned lb_ = lds0 + ((kt) & 3) * 20480 + w * 1024; \
        glds16(voffX, xs + (kt) * 2048, lb_); glds16(voffW0, wb0 + (kt) * 98304, lb_ + 8192); \
        if (w < 4) glds16(voffW1, wb1 + (kt) * 98304, lb_ + 16384); } while (0)
    GU_STAGE(0); GU_STAGE(1); GU_STAGE(2);
    const unsigned ax = tt * 2048 + lane * 16;
    const int sw = ((c & 1) << 1) | (c >> 1);
    unsigned aw[3];
#pragma unroll
    for (int a = 0; a < 3; ++a) { const int nt = 3 * fh + a; aw[a] = 8192 + gi * 6144 + (2 * h * 3 + (nt >> 1)) * 256 + q * 64 + (((nt & 1) ^ gi) << 5) + sw * 8; }
    f32x16 acc[3] = {};
    for (int kt = 0; kt < 20; ++kt) {
        if (w < 4) { if (kt <= 17) { WAIT_BAR(6); } else if (kt == 18) { WAIT_BAR(3); } else { WAIT_BAR(0); } }
        else { if (kt <= 17) { WAIT_BAR(4); } else if (kt == 18) { WAIT_BAR(2); } else { WAIT_BAR(0); } }
        if (kt < 17) GU_STAGE(kt + 3);
        LAS char* b = lds + (kt & 3) * 20480;
#pragma unroll
        for (int ks = 0; ks < 2; ++ks) {
            const bf16x8 xf = lds128(b + ax + ks * 1024);
#pragma unroll
            for (int a = 0; a < 3; ++a) {
                const bf16x8 wf = cat(ldstr(b + aw[a] + ks * 3072), ldstr(b + aw[a] + ks * 3072 + 768));
                acc[a] = MFMA16(wf, xf, acc[a]);
            }
        }
    }
#undef GU_STAGE
    char* hp = ws + WS_H + (size_t)((rb * 4 + tt) * 96 + fb * 6 + 3 * fh) * 1024 + lane * 16;
#pragma unroll
    for (int a = 0; a < 3; ++a) {
        u32x4 o;
#pragma unroll
        for (int e = 0; e < 4; ++e)
            o[e] = pk(__builtin_fmaxf(acc[a][2 * e], 0.f) * acc[a][8 + 2 * e], __builtin_fmaxf(acc[a][2 * e + 1], 0.f) * acc[a][8 + 2 * e + 1]);
        *(u32x4*)(hp + a * 1024) = o;
    }
}

DI void down_role(const char* x, const char* wd, char* ws, LAS char* lds, int rb, int cb, int kh) {
    const int lane = threadIdx.x & 63, w = __builtin_amdgcn_readfirstlane(threadIdx.x >> 6);
    const int r = lane & 31, h = lane >> 5, tt = w & 1, kq = w >> 1;
    const unsigned lds0 = (unsigned)(uintptr_t)lds;
    const int q = (lane >> 2) & 3, c = lane & 3, gi = (lane >> 4) & 1;
    const int ks0 = kh * 48 + kq * 12;
    unsigned vw[3];
#pragma unroll
    for (int i = 0; i < 3; ++i) { const int st = 4 * (tt * 3 + i) + (lane >> 4); vw[i] = (4 * (st / 5) + q) * 1280 + (st % 5) * 64 + c * 16; }
    const char* wb = wd + (size_t)ks0 * 20480 + cb * 320;
    const char* hb = ws + WS_H + (size_t)((rb * 2 + tt) * 96 + ks0) * 1024;
    const unsigned voffH = lane * 16;
    bf16x8 hf0, hf1, hf2;
#define DN_STAGE(t, slot, HF) do { const unsigned lb_ = lds0 + (slot) * 20480 + kq * 5120 + tt * 3072; \
        glds16(vw[0], wb + (t) * 20480, lb_); glds16(vw[1], wb + (t) * 20480, lb_ + 1024); \
        if (tt == 0) glds16(vw[2], wb + (t) * 20480, lb_ + 2048); \
        asm volatile("global_load_dwordx4 %0, %1, %2" : "=v"(HF) : "v"(voffH), "s"(hb + (t) * 1024) : "memory"); } while (0)
#define DN_WAIT(N0, N1, HF) asm volatile("s_cmp_eq_u32 %1, 0\n\ts_cbranch_scc1 1f\n\ts_waitcnt vmcnt(" #N1 ")\n1:\n\ts_waitcnt vmcnt(" #N0 ") lgkmcnt(0)\n\ts_barrier" \
        : "+v"(HF) : "s"(tt) : "memory", "scc")
#define DN_COMP(slot, HF) do { _Pragma("unroll") for (int nt = 0; nt < 5; ++nt) { \
        const bf16x8 wf = cat(ldstr(lds + (slot) * 20480 + aw + nt * 256), ldstr(lds + (slot) * 20480 + aw + nt * 256 + 1280)); \
        acc[nt] = MFMA16(wf, HF, acc[nt]); } } while (0)
    DN_STAGE(0, 0, hf0); DN_STAGE(1, 1, hf1);
    const unsigned aw = kq * 5120 + 2 * h * 1280 + q * 64 + (4 * (c & 1) + 2 * gi + (c >> 1)) * 8;
    f32x16 acc[5] = {};
    for (int t = 0; t < 9; t += 3) {
        DN_WAIT(4, 3, hf0); DN_STAGE(t + 2, 2, hf2); DN_COMP(0, hf0);
        DN_WAIT(4, 3, hf1); DN_STAGE(t + 3, 0, hf0); DN_COMP(1, hf1);
        DN_WAIT(4, 3, hf2); DN_STAGE(t + 4, 1, hf1); DN_COMP(2, hf2);
    }
    DN_WAIT(4, 3, hf0); DN_STAGE(11, 2, hf2); DN_COMP(0, hf0);
    DN_WAIT(4, 3, hf1); DN_COMP(1, hf1);
    DN_WAIT(0, 0, hf2); DN_COMP(2, hf2);
#undef DN_STAGE
#undef DN_WAIT
#undef DN_COMP
    BAR_L();
    if (kq >= 2) {
#pragma unroll
        for (int nt = 0; nt < 5; ++nt)
#pragma unroll
            for (int g = 0; g < 4; ++g) *(LAS f32x4*)(lds + (w - 4) * 20480 + (nt * 4 + g) * 1024 + lane * 16) = f32x4{acc[nt][4 * g], acc[nt][4 * g + 1], acc[nt][4 * g + 2], acc[nt][4 * g + 3]};
    }
    BAR_L();
    if (kq < 2) {
#pragma unroll
        for (int nt = 0; nt < 5; ++nt)
#pragma unroll
            for (int g = 0; g < 4; ++g) { const f32x4 v = *(LAS f32x4*)(lds + w * 20480 + (nt * 4 + g) * 1024 + lane * 16);
                acc[nt][4 * g] += v[0]; acc[nt][4 * g + 1] += v[1]; acc[nt][4 * g + 2] += v[2]; acc[nt][4 * g + 3] += v[3]; }
    }
    BAR_L();
    if (kq == 1) {
#pragma unroll
        for (int nt = 0; nt < 5; ++nt)
#pragma unroll
            for (int g = 0; g < 4; ++g) *(LAS f32x4*)(lds + tt * 20480 + (nt * 4 + g) * 1024 + lane * 16) = f32x4{acc[nt][4 * g], acc[nt][4 * g + 1], acc[nt][4 * g + 2], acc[nt][4 * g + 3]};
    }
    BAR_L();
    if (kq == 0) {
        const size_t go = (size_t)(rb * 64 + tt * 32 + r) * 1280 + cb * 320 + h * 32;
        char* yp = ws + WS_Y + (size_t)kh * (S_ * D_ * 2) + go;
#pragma unroll
        for (int nt = 0; nt < 5; ++nt) {
            float v[16];
#pragma unroll
            for (int g = 0; g < 4; ++g) { const f32x4 t = *(LAS f32x4*)(lds + tt * 20480 + (nt * 4 + g) * 1024 + lane * 16);
#pragma unroll
                for (int e = 0; e < 4; ++e) v[4 * g + e] = acc[nt][4 * g + e] + t[e]; }
            if (kh == 0) {
                const u32x4 xa = *(const u32x4*)(x + go + nt * 64), xb = *(const u32x4*)(x + go + nt * 64 + 16);
#pragma unroll
                for (int e = 0; e < 4; ++e) { v[2 * e] += bflo(xa[e]); v[2 * e + 1] += bfhi(xa[e]); v[8 + 2 * e] += bflo(xb[e]); v[8 + 2 * e + 1] += bfhi(xb[e]); }
            }
            u32x4 oa, ob;
#pragma unroll
            for (int e = 0; e < 4; ++e) { oa[e] = pk(v[2 * e], v[2 * e + 1]); ob[e] = pk(v[8 + 2 * e], v[8 + 2 * e + 1]); }
            *(u32x4*)(yp + nt * 64) = oa; *(u32x4*)(yp + nt * 64 + 16) = ob;
        }
    }
}

__global__ __launch_bounds__(512, 4) void k_attn(char* __restrict__ ws) {
    extern __shared__ __attribute__((aligned(16))) char smem[];
    const int b = blockIdx.x, j = b & 7, i = b >> 3;
    attn_role(ws, (LAS char*)smem, j * 4 + (i >> 3), i & 7);
}
__global__ __launch_bounds__(512, 4) void k_gu(const char* __restrict__ wg, const char* __restrict__ wu, char* __restrict__ ws) {
    extern __shared__ __attribute__((aligned(16))) char smem[];
    const int b = blockIdx.x, j = b & 7, i = b >> 3;
    gu_role(wg, wu, ws, (LAS char*)smem, (j >> 1) * 4 + (i & 3), (j & 1) * 8 + (i >> 2));
}
__global__ __launch_bounds__(512, 4) void k_down(const char* __restrict__ x, const char* __restrict__ wd, char* __restrict__ ws) {
    extern __shared__ __attribute__((aligned(16))) char smem[];
    const int b = blockIdx.x, j = b & 7, i = b >> 3;
    down_role(x, wd, ws, (LAS char*)smem, (j >> 1) * 8 + (i & 7), i >> 3, j & 1);
}

__global__ __launch_bounds__(512, 2) void k_out(const char* __restrict__ wo, const char* __restrict__ ws, char* __restrict__ out) {
    extern __shared__ __attribute__((aligned(16))) char smem[];
    LAS char* lds = (LAS char*)smem;
    const int b = blockIdx.x, j = b & 7, i = b >> 3, rb = j * 4 + (i & 3), cb = i >> 2;
    const int lane = threadIdx.x & 63, w = __builtin_amdgcn_readfirstlane(threadIdx.x >> 6);
    const unsigned lds0 = (unsigned)(uintptr_t)lds;
    const int t16 = w & 3, kh = w >> 2, r16 = lane & 15, q4 = lane >> 4;
    const char* wb = wo + cb * 160;
#pragma unroll
    for (int ii = 0; ii < 5; ++ii) {
        const int P = ii * 64 + lane, rho = P / 10, c = P - rho * 10, u = rho >> 4, qq = (rho >> 2) & 3, e = rho & 3;
        const unsigned va = (20 * (qq >> 1) + 4 * (qq & 1) + 8 * u + e) * 1280 + c * 16;
        glds16(va, wb + w * 51200, lds0 + w * 5120 + ii * 1024);
        glds16(va, wb + (w + 8) * 51200, lds0 + (w + 8) * 5120 + ii * 1024);
        if (w < 4) {
            const unsigned vb = (20 * (4 * (qq >> 1) + 2 * (qq & 1) + u) + 16 + e) * 1280 + c * 16;
            glds16(vb, wb + w * 204800, lds0 + (w + 16) * 5120 + ii * 1024);
        }
    }
    const char* op = ws + WS_O + (size_t)((rb * 2 + (t16 >> 1)) * 40 + kh * 20 + (q4 >> 1)) * 1024 + (q4 & 1) * 512 + ((t16 & 1) * 16 + r16) * 16;
    bf16x8 of[10];
#pragma unroll
    for (int s = 0; s < 10; ++s) of[s] = *(const bf16x8*)(op + s * 2048);
    const size_t go = (size_t)(rb * 64 + t16 * 16 + r16) * 1280 + cb * 160 + q4 * 40;
    u32x2 yv[5];
#pragma unroll
    for (int nt = 0; nt < 5; ++nt) yv[nt] = *(const u32x2*)(ws + WS_Y + (size_t)kh * (S_ * D_ * 2) + go + nt * 8);
    WAIT_BAR(0);
    f32x4 acc[5];
#pragma unroll
    for (int nt = 0; nt < 5; ++nt) acc[nt] = f32x4{bflo(yv[nt][0]), bfhi(yv[nt][0]), bflo(yv[nt][1]), bfhi(yv[nt][1])};
    const unsigned aw = kh * 51200 + (4 * q4 + ((lane >> 2) & 3)) * 160 + (lane & 3) * 40;
#pragma unroll
    for (int s = 0; s < 10; ++s)
#pragma unroll
        for (int nt = 0; nt < 5; ++nt) {
            const bf16x8 wf = cat(ldstr(lds + aw + s * 5120 + nt * 8), ldstr(lds + aw + s * 5120 + nt * 8 + 2560));
            acc[nt] = MFMA16X16(wf, of[s], acc[nt]);
        }
    BAR_L();
    if (kh == 1) {
#pragma unroll
        for (int nt = 0; nt < 5; ++nt) *(LAS f32x4*)(lds + (t16 * 5 + nt) * 1024 + lane * 16) = acc[nt];
    }
    BAR_L();
    if (kh == 0) {
#pragma unroll
        for (int nt = 0; nt < 5; ++nt) {
            const f32x4 t = *(LAS f32x4*)(lds + (t16 * 5 + nt) * 1024 + lane * 16);
            const u32x2 o = {pk(acc[nt][0] + t[0], acc[nt][1] + t[1]), pk(acc[nt][2] + t[2], acc[nt][3] + t[3])};
            *(u32x2*)(out + go + nt * 8) = o;
        }
    }
}

extern "C" void kernel_launch(void* const* d_in, const int* in_sizes, int n_in, void* d_out, int out_size, void* d_ws, size_t ws_size,
                              hipStream_t stream) {
    const char* x = (const char*)d_in[0];
    const char* wqkv = (const char*)d_in[1];
    const char* wo = (const char*)d_in[2];
    const char* wg = (const char*)d_in[4];
    const char* wu = (const char*)d_in[5];
    const char* wd = (const char*)d_in[6];
    char* ws = (char*)d_ws;
    (void)hipFuncSetAttribute((const void*)k_qkv, hipFuncAttributeMaxDynamicSharedMemorySize, 131072);
    (void)hipFuncSetAttribute((const void*)k_attn, hipFuncAttributeMaxDynamicSharedMemorySize, 81920);
    (void)hipFuncSetAttribute((const void*)k_gu, hipFuncAttributeMaxDynamicSharedMemorySize, 81920);
    (void)hipFuncSetAttribute((const void*)k_down, hipFuncAttributeMaxDynamicSharedMemorySize, 81920);
    (void)hipFuncSetAttribute((const void*)k_out, hipFuncAttributeMaxDynamicSharedMemorySize, 102400);
    k_qkv<<<256, 512, 131072, stream>>>(x, wqkv, ws);
    k_gu<<<256, 512, 81920, stream>>>(wg, wu, ws);
    k_attn<<<256, 512, 81920, stream>>>(ws);
    k_down<<<256, 512, 81920, stream>>>(x, wd, ws);
    k_out<<<256, 512, 102400, stream>>>(wo, ws, (char*)d_out);
}
```

```cpp
#include <hip/hip_runtime.h>
#include <hip/hip_bf16.h>
#include <stdint.h>

typedef __attribute__((ext_vector_type(8))) short bf16x8;
typedef __attribute__((ext_vector_type(4))) short bf16x4;
typedef __attribute__((ext_vector_type(16))) float f32x16;
typedef __attribute__((ext_vector_type(4))) float f32x4;
typedef __attribute__((ext_vector_type(2))) float f32x2;
typedef __attribute__((ext_vector_type(4))) unsigned u32x4;
typedef __attribute__((ext_vector_type(2))) unsigned u32x2;
typedef __attribute__((ext_vector_type(2))) __bf16 bf2;
#define LAS __attribute__((address_space(3)))
#define DI __device__ __forceinline__

constexpr int S_ = 2048, D_ = 640, H_ = 32, F_ = 1536;
constexpr size_t WS_Q = 0;
constexpr size_t WS_KV = WS_Q + 32 * 64 * 1280;
constexpr size_t WS_XN = WS_KV + 32 * 64 * 3072;
constexpr size_t WS_H = WS_XN + 64 * 40 * 1024;
constexpr size_t WS_O = WS_H + 64 * 96 * 1024;
constexpr size_t WS_Y = WS_O + 64 * 40 * 1024;

constexpr size_t WS_FLAG = WS_Y + 2 * 2048 * 640 * 2;
constexpr size_t WS_DBG = 64u << 20;
#define STAMP(kid, i) do {} while (0)
#define MFMA16(a, b, c) __builtin_amdgcn_mfma_f32_32x32x16_bf16((a), (b), (c), 0, 0, 0)
#define MFMA8(a, b, c) __builtin_amdgcn_mfma_f32_32x32x8bf16_1k((a), (b), (c), 0, 0, 0)
#define MFMA16X16(a, b, c) __builtin_amdgcn_mfma_f32_16x16x32_bf16((a), (b), (c), 0, 0, 0)
#define LP(a) ((LAS char*)(uintptr_t)(a))
#define SBAR() __builtin_amdgcn_sched_barrier(0)
#define PIN(x) asm volatile("" : "+v"(x))
#define WAIT_BAR(N) asm volatile("s_waitcnt vmcnt(" #N ") lgkmcnt(0)\n\ts_barrier" ::: "memory")
#define BAR_L() do { asm volatile("" ::: "memory"); __builtin_amdgcn_s_waitcnt(0xC07F); asm volatile("s_barrier" ::: "memory"); } while (0)

DI void glds16(unsigned voff, const void* sbase, unsigned lds) {
    unsigned sv;
    asm volatile("s_mov_b32 %0, m0\n\ts_mov_b32 m0, %3\n\ts_nop 0\n\tglobal_load_lds_dwordx4 %1, %2\n\ts_mov_b32 m0, %0"
                 : "=&s"(sv) : "v"(voff), "s"(sbase), "s"(lds) : "memory");
}
DI void glds16x3(unsigned voff, const void* sbase, unsigned lds) {
    unsigned sv;
    asm volatile("s_mov_b32 %0, m0\n\ts_mov_b32 m0, %3\n\ts_nop 0\n\tglobal_load_lds_dwordx4 %1, %2\n\t"
                 "global_load_lds_dwordx4 %1, %2 offset:1024\n\tglobal_load_lds_dwordx4 %1, %2 offset:2048\n\ts_mov_b32 m0, %0"
                 : "=&s"(sv) : "v"(voff), "s"(sbase), "s"(lds) : "memory");
}
DI bf16x8 lds128(LAS char* p) { return *(LAS bf16x8*)p; }
DI bf16x4 lds64(LAS char* p) { return *(LAS bf16x4*)p; }
DI bf16x4 ldstr(LAS char* p) { return __builtin_amdgcn_ds_read_tr16_b64_v4i16((LAS bf16x4*)p); }
DI bf16x8 cat(bf16x4 a, bf16x4 b) { return __builtin_shufflevector(a, b, 0, 1, 2, 3, 4, 5, 6, 7); }
DI unsigned pk(float a, float b) { f32x2 f = {a, b}; return __builtin_bit_cast(unsigned, __builtin_convertvector(f, bf2)); }
DI float bflo(unsigned u) { return __uint_as_float(u << 16); }
DI float bfhi(unsigned u) { return __uint_as_float(u & 0xffff0000u); }
DI float dot2(unsigned a, unsigned b, float c) {
    return __builtin_amdgcn_fdot2_f32_bf16(__builtin_bit_cast(bf2, a), __builtin_bit_cast(bf2, b), c, false); }
DI float swap32_add(float v) { auto r = __builtin_amdgcn_permlane32_swap(__float_as_uint(v), __float_as_uint(v), false, false); return __uint_as_float(r[0]) + __uint_as_float(r[1]); }
DI float swap32_max(float v) { auto r = __builtin_amdgcn_permlane32_swap(__float_as_uint(v), __float_as_uint(v), false, false); return __builtin_fmaxf(__uint_as_float(r[0]), __uint_as_float(r[1])); }

DI void ln_role(const char* x, char* ws, int rb) {
    const int lane = threadIdx.x & 63, w = threadIdx.x >> 6;
    const int row = rb * 128 + w * 16 + (lane & 15), part = lane >> 4;
    const char* src = x + (size_t)row * 1280 + part * 16;
    u32x4 v[20];
#pragma unroll
    for (int i = 0; i < 20; ++i) v[i] = *(const u32x4*)(src + i * 64);
    float s = 0.f, q = 0.f, s2 = 0.f, q2 = 0.f;
#pragma unroll
    for (int i = 0; i < 20; ++i)
#pragma unroll
        for (int e = 0; e < 4; e += 2) {
            s = dot2(v[i][e], 0x3F803F80u, s); q = dot2(v[i][e], v[i][e], q);
            s2 = dot2(v[i][e + 1], 0x3F803F80u, s2); q2 = dot2(v[i][e + 1], v[i][e + 1], q2);
        }
    s += s2; q += q2;
    s += __shfl_xor(s, 16); q += __shfl_xor(q, 16);
    s = swap32_add(s); q = swap32_add(q);
    const float mu = s * (1.f / 640.f);
    const float var = __builtin_fmaxf(__builtin_fmaf(-mu, mu, q * (1.f / 640.f)), 0.f);
    const float rs = __builtin_amdgcn_rsqf(var + 1e-5f), nb = -mu * rs;
    char* dst = ws + WS_XN + (size_t)(row >> 5) * 40960 + (row & 31) * 16 + (part >> 1) * 1024 + (part & 1) * 512;
#pragma unroll
    for (int i = 0; i < 20; ++i) {
        u32x4 o;
#pragma unroll
        for (int e = 0; e < 4; ++e) o[e] = pk(__builtin_fmaf(bflo(v[i][e]), rs, nb), __builtin_fmaf(bfhi(v[i][e]), rs, nb));
        *(u32x4*)(dst + i * 2048) = o;
        SBAR();
    }
}

DI void set_m0(unsigned lds) { asm volatile("s_mov_b32 m0, %0\n\ts_nop 0" :: "s"(lds) : "memory"); }
#define GLDS_OFF(voff, sbase, OFF) asm volatile("global_load_lds_dwordx4 %0, %1 offset:" #OFF :: "v"(voff), "s"(sbase) : "memory")

DI void qkv_loader(const char* x, const char* wqkv, LAS char* lds, int rb, int cb, int lw) {
    const int lane = threadIdx.x & 63;
    const unsigned lds0 = (unsigned)(uintptr_t)lds + lw * 8192;
    const int ra0 = lane >> 3, ra1 = ra0 + 8;
    const unsigned voffA0 = ra0 * 1280 + (((lane & 7) ^ ((ra0 >> 1) & 7)) << 4);
    const unsigned voffA1 = ra1 * 1280 + (((lane & 7) ^ ((ra1 >> 1) & 7)) << 4) - 1024;
    const unsigned voffW0 = ((lane >> 2) & 3) * 3840 + (lane >> 4) * 64 + (lane & 3) * 16;
    const unsigned voffW1 = voffW0 + 15360 - 1024;
    const char* xa = x + (size_t)(rb * 128 + lw * 32) * 1280;
    const char* wb = wqkv + (size_t)lw * 16 * 3840 + cb * 256 - 2048;
#define K1_STAGE(kt) do { \
        set_m0(lds0 + ((kt) & 3) * 32768); \
        GLDS_OFF(voffA0, xa + (kt) * 128, 0); GLDS_OFF(voffW0, wb + (kt) * 245760, 2048); \
        GLDS_OFF(voffA1, xa + (kt) * 128, 1024); GLDS_OFF(voffW1, wb + (kt) * 245760, 3072); \
        set_m0(lds0 + ((kt) & 3) * 32768 + 4096); \
        GLDS_OFF(voffA0, xa + (kt) * 128 + 20480, 0); GLDS_OFF(voffW0, wb + (kt) * 245760 + 30720, 2048); \
        GLDS_OFF(voffA1, xa + (kt) * 128 + 20480, 1024); GLDS_OFF(voffW1, wb + (kt) * 245760 + 30720, 3072); } while (0)
    K1_STAGE(0); K1_STAGE(1);
    asm volatile("s_waitcnt vmcnt(8)\n\ts_barrier" ::: "memory");
    K1_STAGE(2);
#pragma unroll 1
    for (int j = 1; j < 8; ++j) {
        K1_STAGE(j + 2);
        asm volatile("s_waitcnt vmcnt(16)\n\ts_barrier" ::: "memory");
    }
    asm volatile("s_waitcnt vmcnt(8)\n\ts_barrier\n\ts_waitcnt vmcnt(0)\n\ts_barrier" ::: "memory");
#undef K1_STAGE
}

template <bool VT>
DI void qkv_tile(char* ws, LAS char* lds, int rb, int cb, int w) {
    const int lane = threadIdx.x & 63;
    const int wm = w >> 2, wn = w & 3, r = lane & 31, h = lane >> 5;
    STAMP(0, 0);
    const int rot = wn ^ (2 * wm);
    const int xrow = wm * 64 + r;
    unsigned ax[4], aw[4];
#pragma unroll
    for (int p = 0; p < 4; ++p) {
        ax[p] = ((xrow >> 4) * 4096 + (xrow & 15) * 128 + (((2 * rot + h) ^ ((xrow >> 1) & 7)) << 4)) ^ (p * 32);
        aw[p] = ((2 * rot + h) * 4096 + 2048 + wn * 256 + ((lane >> 2) & 3) * 64 + ((lane >> 4) & 1) * 32 + (lane & 3) * 8) ^ (p << 13);
    }
    unsigned eoff[4]; bool eone[4];
    if (!VT) {
        const int rec = cb < 5 ? 1280 : 3072, nb = (cb % 5) * 128 + wn * 32 + 4 * h, tile = rb * 4 + wm * 2;
#pragma unroll
        for (int g = 0; g < 4; ++g) {
            const int n0 = nb + 8 * g, hd = n0 / 20, c = (n0 - hd * 20) >> 2;
            eoff[g] = (unsigned)(hd * 64 + tile) * rec + (c < 4 ? (c >> 1) * 512 + r * 16 + (c & 1) * 8 : 1024 + r * 8);
            eone[g] = cb >= 5 && c == 4;
            PIN(eoff[g]);
        }
    } else {
        const int n = (cb - 10) * 128 + wn * 32 + r, hd = n / 20, d = n - hd * 20;
        eoff[0] = (unsigned)(hd * 64 + rb * 4 + wm * 2) * 3072 + 1536 + h * 384 + d * 16;
        eone[0] = d == 19;
        PIN(eoff[0]);
    }
    f32x16 acc0 = {}, acc1 = {};
    float sx0 = 0.f, sq0 = 0.f, sx1 = 0.f, sq1 = 0.f, cw = 0.f, cx = 0.f;
    bf16x8 xA0[4], xA1[4], wA[4], xB0[4], xB1[4], wB[4];
#define K1_READ(X0, X1, W, p, OFF) do { X0[p] = lds128(LP(ax[p]) + (OFF)); X1[p] = lds128(LP(ax[p]) + (OFF) + 8192); \
        W[p] = cat(ldstr(LP(aw[p]) + (OFF)), ldstr(LP(aw[p]) + (OFF) + 1024)); } while (0)
#define K1_XSTAT(U, SX, SQ) do { const float lo_ = bflo(U), hi_ = bfhi(U); SX += lo_; SQ = __builtin_fmaf(lo_, lo_, SQ); SX += hi_; SQ = __builtin_fmaf(hi_, hi_, SQ); PIN(SX); PIN(SQ); } while (0)
#define K1_MMA(X0, X1, W, p) do { \
        acc0 = VT ? MFMA16(X0[p], W[p], acc0) : MFMA16(W[p], X0[p], acc0); PIN(acc0); SBAR(); \
        K1_XSTAT(__builtin_bit_cast(u32x4, X0[0])[p], sx0, sq0); SBAR(); \
        acc1 = VT ? MFMA16(X1[p], W[p], acc1) : MFMA16(W[p], X1[p], acc1); PIN(acc1); SBAR(); \
        K1_XSTAT(__builtin_bit_cast(u32x4, X1[0])[p], sx1, sq1); SBAR(); } while (0)
#define K1_STEP(X0, X1, W, NX0, NX1, NW, OFF) do { \
        BAR_L(); \
        _Pragma("unroll") for (int p = 0; p < 4; ++p) { K1_READ(NX0, NX1, NW, p, OFF); SBAR(); K1_MMA(X0, X1, W, p); } \
        _Pragma("unroll") for (int e = 0; e < 4; ++e) { cw = dot2(__builtin_bit_cast(u32x4, W[0])[e], 0x3F803F80u, cw); cx = dot2(__builtin_bit_cast(u32x4, W[1])[e], 0x3F803F80u, cx); } \
        PIN(cw); PIN(cx); SBAR(); } while (0)
    asm volatile("s_barrier" ::: "memory");
    STAMP(0, 1);
#pragma unroll
    for (int p = 0; p < 4; ++p) K1_READ(xA0, xA1, wA, p, 0);
#pragma unroll 1
    for (int kt = 0; kt < 8; kt += 2) {
        K1_STEP(xA0, xA1, wA, xB0, xB1, wB, 32768);
#pragma unroll
        for (int p = 0; p < 4; ++p) { ax[p] ^= 65536; aw[p] ^= 65536; }
        K1_STEP(xB0, xB1, wB, xA0, xA1, wA, 0);
    }
    K1_STEP(xA0, xA1, wA, xB0, xB1, wB, 32768);
#pragma unroll
    for (int p = 0; p < 4; ++p) K1_MMA(xB0, xB1, wB, p);
#pragma unroll
    for (int e = 0; e < 4; ++e) { cw = dot2(__builtin_bit_cast(u32x4, wB[0])[e], 0x3F803F80u, cw); cx = dot2(__builtin_bit_cast(u32x4, wB[1])[e], 0x3F803F80u, cx); }
#undef K1_READ
#undef K1_XSTAT
#undef K1_MMA
#undef K1_STEP
    STAMP(0, 3);
    sx0 = swap32_add(sx0); sq0 = swap32_add(sq0); sx1 = swap32_add(sx1); sq1 = swap32_add(sq1); cw = swap32_add(cw + cx);
    LAS float* ps = (LAS float*)(lds + 131072);
    LAS float* pc = (LAS float*)(lds + 135168);
    LAS float* fa = (LAS float*)(lds + 136192 + w * 512);
    { const f32x2 t = {h ? sx1 : sx0, h ? sq1 : sq0}; *(LAS f32x2*)(ps + (wn * 128 + wm * 64 + lane) * 2) = t; }
    pc[wm * 128 + wn * 32 + r] = cw;
    BAR_L();
    float ra, rbb;
    {
        f32x2 sq = *(LAS f32x2*)(ps + (wm * 64 + lane) * 2);
#pragma unroll
        for (int i = 1; i < 4; ++i) sq += *(LAS f32x2*)(ps + (i * 128 + wm * 64 + lane) * 2);
        const float mu = sq[0] * (1.f / 640.f);
        const float var = __builtin_fmaxf(__builtin_fmaf(-mu, mu, sq[1] * (1.f / 640.f)), 0.f);
        ra = __builtin_amdgcn_rsqf(var + 1e-5f);
        if (cb < 5) ra *= 0.22360679774997896f * 1.4426950408889634f;
        rbb = -mu;
    }
    if (!VT) {
        char* base = ws + (cb < 5 ? WS_Q : WS_KV);
        const int rec = cb < 5 ? 1280 : 3072;
        const auto sa = __builtin_amdgcn_permlane32_swap(__float_as_uint(ra), __float_as_uint(ra), false, false);
        const auto sb = __builtin_amdgcn_permlane32_swap(__float_as_uint(rbb), __float_as_uint(rbb), false, false);
        const float a0 = __uint_as_float(sa[0]), a1 = __uint_as_float(sa[1]), b0 = __uint_as_float(sb[0]), b1 = __uint_as_float(sb[1]);
#pragma unroll
        for (int g = 0; g < 4; ++g) {
            const f32x4 cs = *(LAS f32x4*)(pc + wn * 32 + 8 * g + 4 * h) + *(LAS f32x4*)(pc + 128 + wn * 32 + 8 * g + 4 * h);
            const f32x4 v0 = (f32x4{acc0[4 * g], acc0[4 * g + 1], acc0[4 * g + 2], acc0[4 * g + 3]} + cs * b0) * a0;
            const f32x4 v1 = (f32x4{acc1[4 * g], acc1[4 * g + 1], acc1[4 * g + 2], acc1[4 * g + 3]} + cs * b1) * a1;
            const u32x2 o0 = {pk(v0[0], v0[1]), pk(v0[2], v0[3])}, o1 = {pk(v1[0], v1[1]), pk(v1[2], v1[3])};
            *(u32x2*)(base + eoff[g]) = o0; *(u32x2*)(base + eoff[g] + rec) = o1;
            if (eone[g]) { const u32x2 one = {0x3F80u, 0u}; *(u32x2*)(base + eoff[g] + 256) = one; *(u32x2*)(base + eoff[g] + 3072 + 256) = one; }
        }
    } else {
        fa[lane] = ra; fa[64 + lane] = rbb;
        const float cs = pc[wn * 32 + r] + pc[128 + wn * 32 + r];
        char* base = ws + WS_KV + eoff[0];
#pragma unroll
        for (int i = 0; i < 2; ++i) {
#pragma unroll
            for (int s = 0; s < 2; ++s) {
                u32x4 o;
#pragma unroll
                for (int gg = 0; gg < 2; ++gg) {
                    const int g = 2 * s + gg;
                    const f32x4 a = *(LAS f32x4*)(fa + i * 32 + 8 * g + 4 * h), bb = *(LAS f32x4*)(fa + 64 + i * 32 + 8 * g + 4 * h);
                    const f32x16& acc = i ? acc1 : acc0;
                    const f32x4 v = (f32x4{acc[4 * g], acc[4 * g + 1], acc[4 * g + 2], acc[4 * g + 3]} + bb * cs) * a;
                    o[2 * gg] = pk(v[0], v[1]); o[2 * gg + 1] = pk(v[2], v[3]);
                }
                *(u32x4*)(base + i * 3072 + s * 768) = o;
            }
        }
        if (eone[0]) {
            const u32x4 one = {0x3F803F80u, 0x3F803F80u, 0x3F803F80u, 0x3F803F80u}, zr = {0u, 0u, 0u, 0u};
#pragma unroll
            for (int i = 0; i < 4; ++i) { *(u32x4*)(base + (i >> 1) * 3072 + (i & 1) * 768 + 16) = one; *(u32x4*)(base + (i >> 1) * 3072 + (i & 1) * 768 + 32) = zr; }
        }
    }
}

__global__ __launch_bounds__(768, 3) void k_qkv(const char* __restrict__ x, const char* __restrict__ wqkv, char* __restrict__ ws) {
    extern __shared__ __attribute__((aligned(16))) char smem[];
    const int b = blockIdx.x, j = b & 7, i = b >> 3, rbg = j >> 1;
    const int w = __builtin_amdgcn_readfirstlane(threadIdx.x >> 6);
    int cb;
    if (j & 1) { if (i >= 28) { if (b == 255 && w == 8 && threadIdx.x < 512 + 32) *(unsigned*)(ws + WS_FLAG + (threadIdx.x - 512) * 256) = 0u; if (w < 8) { STAMP(0, 0); ln_role(x, ws, 4 * rbg + (i - 28)); STAMP(0, 4); } return; } cb = 8 + (i >> 2); } else cb = i >> 2;
    const int rb = 4 * rbg + (i & 3);
    if (w >= 8) { qkv_loader(x, wqkv, (LAS char*)smem, rb, cb, w - 8); return; }
    if (cb < 10) qkv_tile<false>(ws, (LAS char*)smem, rb, cb, w);
    else qkv_tile<true>(ws, (LAS char*)smem, rb, cb, w);
    STAMP(0, 4);
    STAMP(0, 5);
}

DI float max16(const f32x16& s) {
    float a = __builtin_fmaxf(__builtin_fmaxf(s[0], s[1]), s[2]), b = __builtin_fmaxf(__builtin_fmaxf(s[3], s[4]), s[5]);
    a = __builtin_fmaxf(__builtin_fmaxf(a, s[6]), s[7]); b = __builtin_fmaxf(__builtin_fmaxf(b, s[8]), s[9]);
    a = __builtin_fmaxf(__builtin_fmaxf(a, s[10]), s[11]); b = __builtin_fmaxf(__builtin_fmaxf(b, s[12]), s[13]);
    a = __builtin_fmaxf(__builtin_fmaxf(a, s[14]), s[15]);
    return swap32_max(__builtin_fmaxf(a, b));
}
DI void exp_pack(const f32x16& s, bf16x8& p0, bf16x8& p1) {
    u32x4 a, b;
#pragma unroll
    for (int e = 0; e < 4; ++e) {
        a[e] = pk(__builtin_amdgcn_exp2f(s[2 * e]), __builtin_amdgcn_exp2f(s[2 * e + 1]));
        b[e] = pk(__builtin_amdgcn_exp2f(s[8 + 2 * e]), __builtin_amdgcn_exp2f(s[8 + 2 * e + 1]));
    }
    p0 = __builtin_bit_cast(bf16x8, a); p1 = __builtin_bit_cast(bf16x8, b);
}

DI void attn_role(char* ws, LAS char* lds, int hd, int qb) {
    const int lane = threadIdx.x & 63, w = __builtin_amdgcn_readfirstlane(threadIdx.x >> 6);
    const int r = lane & 31, h = lane >> 5;
    const unsigned lds0 = (unsigned)(uintptr_t)lds;
    const char* kv = ws + WS_KV + (size_t)hd * 196608;
    const unsigned voff = lane * 16;
    STAMP(2, 0);
    glds16x3(voff, kv + w * 3072, lds0 + w * 3072);
    const int qt = qb * 8 + w;
    const char* qp = ws + WS_Q + (size_t)(hd * 64 + qt) * 1280;
    const bf16x8 q1 = *(const bf16x8*)(qp + lane * 16);
    bf16x4 q2 = *(const bf16x4*)(qp + 1024 + r * 8);
    if (h) q2 = bf16x4{0, 0, 0, 0};
    glds16x3(voff, kv + 24576 + w * 3072, lds0 + 24576 + w * 3072);
    const unsigned aK = lane * 16, aK2 = 1024 + lane * 8, aV = 1536 + h * 384 + (r < 21 ? r : 21) * 16;
    const f32x16 zero = {};
    WAIT_BAR(3);
    STAMP(2, 1);
    bf16x8 k1 = lds128(lds + aK); bf16x4 k2 = lds64(lds + aK2);
    f32x16 sA = MFMA8(k2, q2, MFMA16(k1, q1, zero));
    const bf16x4 q2raw = q2;
    { const float m = max16(sA); if (h) q2[0] = (short)(pk(-m, 0.f) & 0xffffu); }
    sA = MFMA8(k2, q2, MFMA16(k1, q1, zero));
    k1 = lds128(lds + 3072 + aK); k2 = lds64(lds + 3072 + aK2);
    bf16x8 v0 = lds128(lds + aV), v1 = v0;
    f32x16 sB, o = {};
    u32x4 p0, p1 = {0u, 0u, 0u, 0u};
    float e12 = 0.f, e13 = 0.f, e14 = 0.f, e15 = 0.f;
#define EX(v) __builtin_amdgcn_exp2f(v)
#define ATT_STEP(SC, SN, KB, VB, VN) do { \
        SN = MFMA16(k1, q1, zero); SBAR(); \
        k1 = lds128((KB) + aK); \
        p1[2] = pk(e12, e13); p1[3] = pk(e14, e15); PIN(p1); \
        float e0 = EX(SC[0]), e1 = EX(SC[1]), e2 = EX(SC[2]), e3 = EX(SC[3]); PIN(e0); PIN(e1); PIN(e2); PIN(e3); SBAR(); \
        SN = MFMA8(k2, q2, SN); SBAR(); \
        k2 = lds64((KB) + aK2); \
        p0[0] = pk(e0, e1); p0[1] = pk(e2, e3); PIN(p0); \
        float e4 = EX(SC[4]), e5 = EX(SC[5]), e6 = EX(SC[6]), e7 = EX(SC[7]); PIN(e4); PIN(e5); PIN(e6); PIN(e7); SBAR(); \
        o = MFMA16(v1, __builtin_bit_cast(bf16x8, p1), o); SBAR(); \
        v1 = lds128((VB) + aV + 768); \
        p0[2] = pk(e4, e5); p0[3] = pk(e6, e7); PIN(p0); \
        float e8 = EX(SC[8]), e9 = EX(SC[9]), e10 = EX(SC[10]), e11 = EX(SC[11]); PIN(e8); PIN(e9); PIN(e10); PIN(e11); SBAR(); \
        o = MFMA16(v0, __builtin_bit_cast(bf16x8, p0), o); SBAR(); \
        v0 = lds128((VN) + aV); \
        p1[0] = pk(e8, e9); p1[1] = pk(e10, e11); PIN(p1); \
        e12 = EX(SC[12]); e13 = EX(SC[13]); e14 = EX(SC[14]); e15 = EX(SC[15]); PIN(e12); PIN(e13); PIN(e14); PIN(e15); SBAR(); \
    } while (0)
    int cur = 0;
#pragma unroll 1
    for (int s = 0; s < 8; ++s) {
        LAS char* b = lds + cur;
        const int nxt = cur == 49152 ? 0 : cur + 24576;
        LAS char* bn = lds + nxt;
        ATT_STEP(sA, sB, b + 2 * 3072, b, b + 3072);
        ATT_STEP(sB, sA, b + 3 * 3072, b + 3072, b + 2 * 3072);
        ATT_STEP(sA, sB, b + 4 * 3072, b + 2 * 3072, b + 3 * 3072);
        ATT_STEP(sB, sA, b + 5 * 3072, b + 3 * 3072, b + 4 * 3072);
        ATT_STEP(sA, sB, b + 6 * 3072, b + 4 * 3072, b + 5 * 3072);
        ATT_STEP(sB, sA, b + 7 * 3072, b + 5 * 3072, b + 6 * 3072);
        asm volatile("s_waitcnt vmcnt(0)\n\ts_barrier" ::: "memory");
        if (s < 6) { const int nn = nxt == 49152 ? 0 : nxt + 24576; glds16x3(voff, kv + (s + 2) * 24576 + w * 3072, lds0 + nn + w * 3072); }
        ATT_STEP(sA, sB, bn, b + 6 * 3072, b + 7 * 3072);
        ATT_STEP(sB, sA, bn + 3072, b + 7 * 3072, bn);
        cur = nxt;
    }
    p1[2] = pk(e12, e13); p1[3] = pk(e14, e15);
    o = MFMA16(v1, __builtin_bit_cast(bf16x8, p1), o);
#undef ATT_STEP
    STAMP(2, 3);
    float l;
    { auto rr = __builtin_amdgcn_permlane32_swap(__float_as_uint(o[8]), __float_as_uint(o[8]), false, false); l = __uint_as_float(rr[1]); }
    float am = 0.f;
#pragma unroll
    for (int i = 0; i < 12; ++i) am = __builtin_fmaxf(am, __builtin_fabsf(i >= 8 && h ? 0.f : o[i]));
    const bool ok = (l > 5.4e-20f) && (l < 1.8e19f) && (am < 3.0e38f);
    if (__builtin_expect(!__all(ok), 0)) {
        float m = -INFINITY;
        o = zero;
        for (int t = 0; t < 64; ++t) {
            const char* rec = kv + t * 3072;
            const bf16x8 fk1 = *(const bf16x8*)(rec + aK); const bf16x4 fk2 = *(const bf16x4*)(rec + aK2);
            const bf16x8 fv0 = *(const bf16x8*)(rec + aV), fv1 = *(const bf16x8*)(rec + aV + 768);
            f32x16 sf = MFMA8(fk2, q2raw, MFMA16(fk1, q1, zero));
            const float mn = __builtin_fmaxf(m, max16(sf));
            const float al = __builtin_amdgcn_exp2f(m - mn);
            m = mn;
#pragma unroll
            for (int i = 0; i < 16; ++i) { sf[i] -= mn; o[i] *= al; }
            bf16x8 p0, p1;
            exp_pack(sf, p0, p1);
            o = MFMA16(fv0, p0, o); o = MFMA16(fv1, p1, o);
        }
        auto rr = __builtin_amdgcn_permlane32_swap(__float_as_uint(o[8]), __float_as_uint(o[8]), false, false); l = __uint_as_float(rr[1]);
    }
    const float inv = 1.f / l;
    char* op = ws + WS_O + (size_t)qt * 40960;
    u32x4 oa;
#pragma unroll
    for (int e = 0; e < 4; ++e) oa[e] = pk(o[2 * e] * inv, o[2 * e + 1] * inv);
    *(u32x4*)(op + hd * 1024 + lane * 16) = oa;
    if (h == 0) {
        const u32x2 ob = {pk(o[8] * inv, o[9] * inv), pk(o[10] * inv, o[11] * inv)};
        *(u32x2*)(op + (32 + (hd >> 2)) * 1024 + ((hd >> 1) & 1) * 512 + r * 16 + (hd & 1) * 8) = ob;
    }
}

#define VM_BAR2(sel, NA, NB) asm volatile("s_cmp_eq_u32 %0, 0\n\ts_cbranch_scc1 1f\n\ts_waitcnt vmcnt(" #NB ")\n1:\n\ts_waitcnt vmcnt(" #NA ")\n\ts_barrier" :: "s"(sel) : "memory", "scc")
DI void gu_role(const char* wg, const char* wu, char* ws, int rb, int fb) {
    const int lane = threadIdx.x & 63, w = __builtin_amdgcn_readfirstlane(threadIdx.x >> 6);
    const int h = lane >> 5, tt = w & 3, fh = w >> 2;
    const int q = (lane >> 2) & 3, c = lane & 3, gi = (lane >> 4) & 1;
    const char* xs = ws + WS_XN + (size_t)((rb * 4 + (w >> 1)) * 40 + (w & 1)) * 1024;
    const unsigned voffX = lane * 16;
    const int m0 = w >= 6, i0 = m0 ? w - 6 : w, st0 = 4 * i0 + (lane >> 4);
    const unsigned voffW0 = (4 * (st0 / 3) + q) * 3072 + (st0 % 3) * 64 + ((m0 ? c ^ 2 : c) << 4);
    const int st1 = 4 * (2 + (w & 3)) + (lane >> 4);
    const unsigned voffW1 = (4 * (st1 / 3) + q) * 3072 + (st1 % 3) * 64 + ((c ^ 2) << 4);
    const char* wb0 = (m0 ? wu : wg) + fb * 192;
    const char* wb1 = wu + fb * 192;
#define GU_P0(kt) do { set_m0(((kt) & 3) * 20480 + w * 1024); GLDS_OFF(voffX, xs + (kt) * 2048, 0); } while (0)
#define GU_P1(kt) do { set_m0(((kt) & 3) * 20480 + w * 1024 + 8192); GLDS_OFF(voffW0, wb0 + (kt) * 98304, 0); } while (0)
#define GU_P2(kt) do { if (fh == 0) { set_m0(((kt) & 3) * 20480 + w * 1024 + 16384); GLDS_OFF(voffW1, wb1 + (kt) * 98304, 0); } } while (0)
    STAMP(1, 0);
    GU_P0(0); GU_P1(0); GU_P2(0); GU_P0(1); GU_P1(1); GU_P2(1); GU_P0(2); GU_P1(2); GU_P2(2);
    unsigned ax = tt * 2048 + lane * 16;
    const int sw = ((c & 1) << 1) | (c >> 1);
    unsigned aw[3];
#pragma unroll
    for (int a = 0; a < 3; ++a) { const int nt = 3 * fh + a; aw[a] = 8192 + gi * 6144 + (2 * h * 3 + (nt >> 1)) * 256 + q * 64 + (((nt & 1) ^ gi) << 5) + sw * 8; }
    f32x16 acc[3] = {};
    bf16x8 xf[2], wf[2][3];
#define GU_RW(ks, a, OFF) wf[ks][a] = cat(ldstr(LP(aw[a]) + (OFF) + (ks) * 3072), ldstr(LP(aw[a]) + (OFF) + (ks) * 3072 + 768))
#define GU_MM(ks, a, OFF) do { acc[a] = MFMA16(wf[ks][a], xf[ks], acc[a]); PIN(acc[a]); SBAR(); GU_RW(ks, a, OFF); SBAR(); } while (0)
#define GU_STEP(kt, OFF, DMA) do { \
        __builtin_amdgcn_s_waitcnt(0xC07F); \
        if (DMA) VM_BAR2(fh, 3, 2); else asm volatile("s_waitcnt vmcnt(0)\n\ts_barrier" ::: "memory"); \
        GU_MM(0, 0, OFF); if (DMA) GU_P0((kt) + 3); GU_MM(0, 1, OFF); GU_MM(0, 2, OFF); xf[0] = lds128(LP(ax) + (OFF)); if (DMA) GU_P1((kt) + 3); \
        GU_MM(1, 0, OFF); GU_MM(1, 1, OFF); if (DMA) GU_P2((kt) + 3); GU_MM(1, 2, OFF); xf[1] = lds128(LP(ax) + (OFF) + 1024); SBAR(); } while (0)
    VM_BAR2(fh, 6, 4);
    STAMP(1, 1);
#pragma unroll
    for (int ks = 0; ks < 2; ++ks) { xf[ks] = lds128(LP(ax) + ks * 1024); GU_RW(ks, 0, 0); GU_RW(ks, 1, 0); GU_RW(ks, 2, 0); }
    __builtin_amdgcn_s_setprio(0);
#pragma unroll 1
    for (int kt = 0; kt < 16; kt += 4) {
        GU_STEP(kt, 20480, true); GU_STEP(kt + 1, 40960, true);
        ax += 61440; aw[0] += 61440; aw[1] += 61440; aw[2] += 61440;
        GU_STEP(kt + 2, 0, true);
        ax -= 61440; aw[0] -= 61440; aw[1] -= 61440; aw[2] -= 61440;
        GU_STEP(kt + 3, 0, true);
    }
    GU_STEP(16, 20480, true); GU_STEP(17, 40960, false);
    ax += 61440; aw[0] += 61440; aw[1] += 61440; aw[2] += 61440;
    GU_STEP(18, 0, false);
#pragma unroll
    for (int ks = 0; ks < 2; ++ks)
#pragma unroll
        for (int a = 0; a < 3; ++a) acc[a] = MFMA16(wf[ks][a], xf[ks], acc[a]);
#undef GU_P0
#undef GU_P1
#undef GU_P2
#undef GU_RW
#undef GU_MM
#undef GU_STEP
    __builtin_amdgcn_s_setprio(3);
    STAMP(1, 3);
    const char* hp = ws + WS_H + (size_t)((rb * 4 + tt) * 96 + fb * 6 + 3 * fh) * 1024;
#pragma unroll
    for (int a = 0; a < 3; ++a) {
        u32x4 o;
#pragma unroll
        for (int e = 0; e < 4; ++e)
            o[e] = pk(__builtin_fmaxf(acc[a][2 * e], 0.f) * acc[a][8 + 2 * e], __builtin_fmaxf(acc[a][2 * e + 1], 0.f) * acc[a][8 + 2 * e + 1]);
        asm volatile("global_store_dwordx4 %0, %1, %2 sc1\n\ts_nop 1" :: "v"(voffX), "v"(o), "s"(hp + a * 1024) : "memory");
    }
}

typedef __attribute__((address_space(1))) unsigned gu32;
DI void down_role(const char* x, const char* wd, char* ws, int rb, int cb, int kh, gu32* flag) {
    const int lane = threadIdx.x & 63, w = __builtin_amdgcn_readfirstlane(threadIdx.x >> 6);
    const int r = lane & 31, h = lane >> 5, tt = w & 1, kq = w >> 1;
    const int q = (lane >> 2) & 3, c = lane & 3, gi = (lane >> 4) & 1;
    const int ks0 = kh * 48 + kq * 12;
    unsigned vw[3];
#pragma unroll
    for (int i = 0; i < 3; ++i) { const int st = 4 * (tt * 3 + i) + (lane >> 4); vw[i] = (4 * (st / 5) + q) * 1280 + (st % 5) * 64 + c * 16 + 2048 - i * 1024; }
    const char* wb = wd + (size_t)ks0 * 20480 + cb * 320 - 2048;
    const char* hb = ws + WS_H + (size_t)((rb * 2 + tt) * 96 + ks0) * 1024;
    const unsigned voffH = lane * 16;
    bf16x8 hf0, hf1, hf2, wf[5];
#define DN_W(t) do { set_m0(((t) & 3) * 20480 + kq * 5120 + tt * 3072); GLDS_OFF(vw[0], wb + (t) * 20480, 0); GLDS_OFF(vw[1], wb + (t) * 20480, 1024); \
        if (tt == 0) GLDS_OFF(vw[2], wb + (t) * 20480, 2048); } while (0)
#define DN_H(t, HF) asm volatile("global_load_dwordx4 %0, %1, %2 sc1" : "=v"(HF) : "v"(voffH), "s"(hb + (t) * 1024) : "memory")
#define DN_WAIT(N0, N1, HF) asm volatile("s_cmp_eq_u32 %1, 0\n\ts_cbranch_scc1 1f\n\ts_waitcnt vmcnt(" #N1 ")\n1:\n\ts_waitcnt vmcnt(" #N0 ")\n\ts_barrier" \
        : "+v"(HF) : "s"(tt) : "memory", "scc")
#define DN_RW(nt, OFF) wf[nt] = cat(ldstr(LP(aw) + (OFF) + (nt) * 256), ldstr(LP(aw) + (OFF) + (nt) * 256 + 1280))
#define DN_MM(nt, OFF, HF) do { acc[nt] = MFMA16(wf[nt], HF, acc[nt]); PIN(acc[nt]); SBAR(); DN_RW(nt, OFF); SBAR(); } while (0)
    STAMP(3, 0);
    DN_W(0); DN_W(1); DN_W(2);
    if (w == 0) {
        for (int spin = 0; spin < (1 << 20) && __builtin_amdgcn_readfirstlane(__hip_atomic_load(flag, __ATOMIC_RELAXED, __HIP_MEMORY_SCOPE_AGENT)) != 8u; ++spin)
            __builtin_amdgcn_s_sleep(8);
    }
    asm volatile("s_waitcnt vmcnt(0)\n\ts_barrier" ::: "memory");
    STAMP(3, 1);
    DN_H(0, hf0); DN_H(1, hf1);
    unsigned aw = kq * 5120 + 2 * h * 1280 + q * 64 + (4 * (c & 1) + 2 * gi + (c >> 1)) * 8;
    f32x16 acc[5] = {};
#pragma unroll
    for (int nt = 0; nt < 5; ++nt) DN_RW(nt, 0);
#define DN_STEP(t, OFF, HF, HN, N0, N1, DH, DW) do { \
        __builtin_amdgcn_s_waitcnt(0xC07F); DN_WAIT(N0, N1, HF); \
        DN_MM(0, OFF, HF); if (DH) DN_H((t) + 2, HN); DN_MM(1, OFF, HF); DN_MM(2, OFF, HF); if (DW) DN_W((t) + 3); DN_MM(3, OFF, HF); DN_MM(4, OFF, HF); } while (0)
    __builtin_amdgcn_s_setprio(0);
    DN_STEP(0, 20480, hf0, hf2, 1, 1, true, true);
    DN_STEP(1, 40960, hf1, hf0, 4, 3, true, true);
    aw += 61440;
    DN_STEP(2, 0, hf2, hf1, 4, 3, true, true);
    aw -= 61440;
    DN_STEP(3, 0, hf0, hf2, 4, 3, true, true);
    DN_STEP(4, 20480, hf1, hf0, 4, 3, true, true);
    DN_STEP(5, 40960, hf2, hf1, 4, 3, true, true);
    aw += 61440;
    DN_STEP(6, 0, hf0, hf2, 4, 3, true, true);
    aw -= 61440;
    DN_STEP(7, 0, hf1, hf0, 4, 3, true, true);
    DN_STEP(8, 20480, hf2, hf1, 4, 3, true, true);
    DN_STEP(9, 40960, hf0, hf2, 4, 3, true, false);
    aw += 61440;
    DN_STEP(10, 0, hf1, hf0, 1, 1, false, false);
    asm volatile("s_waitcnt vmcnt(0)" : "+v"(hf2) :: "memory");
#pragma unroll
    for (int nt = 0; nt < 5; ++nt) acc[nt] = MFMA16(wf[nt], hf2, acc[nt]);
#undef DN_W
#undef DN_H
#undef DN_WAIT
#undef DN_RW
#undef DN_MM
#undef DN_STEP
    __builtin_amdgcn_s_setprio(3);
    STAMP(3, 3);
    const size_t go = (size_t)(rb * 64 + tt * 32 + r) * 1280 + cb * 320 + h * 32;
#define AQ(Q) f32x4{acc[(Q) >> 2][4 * ((Q) & 3)], acc[(Q) >> 2][4 * ((Q) & 3) + 1], acc[(Q) >> 2][4 * ((Q) & 3) + 2], acc[(Q) >> 2][4 * ((Q) & 3) + 3]}
    LAS char* mine = LP(w * 10240 + lane * 16);
    LAS char* oth2 = LP((w ^ 4) * 10240 + lane * 16);
    LAS char* oth1 = LP((w ^ 6) * 10240 + lane * 16);
    f32x4 k1[10], k2[5];
    BAR_L();
    if (kq & 2) {
#pragma unroll
        for (int i = 0; i < 10; ++i) *(LAS f32x4*)(mine + i * 1024) = AQ(i);
        BAR_L();
#pragma unroll
        for (int i = 0; i < 10; ++i) k1[i] = AQ(10 + i) + *(LAS f32x4*)(oth2 + i * 1024);
    } else {
#pragma unroll
        for (int i = 0; i < 10; ++i) *(LAS f32x4*)(mine + i * 1024) = AQ(10 + i);
        BAR_L();
#pragma unroll
        for (int i = 0; i < 10; ++i) k1[i] = AQ(i) + *(LAS f32x4*)(oth2 + i * 1024);
    }
#undef AQ
    SBAR();
    u32x2 xv[5];
#pragma unroll
    for (int i = 0; i < 5; ++i) { const int Q = 5 * kq + i; xv[i] = *(const u32x2*)(x + go + (Q >> 2) * 64 + (Q & 3) * 8); }
    const unsigned xm = kh == 0 ? 0xffffffffu : 0u;
    if (kq & 1) {
#pragma unroll
        for (int i = 0; i < 5; ++i) *(LAS f32x4*)(oth2 + i * 1024) = k1[i];
        BAR_L();
#pragma unroll
        for (int i = 0; i < 5; ++i) k2[i] = k1[5 + i] + *(LAS f32x4*)(oth1 + i * 1024);
    } else {
#pragma unroll
        for (int i = 0; i < 5; ++i) *(LAS f32x4*)(oth2 + i * 1024) = k1[5 + i];
        BAR_L();
#pragma unroll
        for (int i = 0; i < 5; ++i) k2[i] = k1[i] + *(LAS f32x4*)(oth1 + i * 1024);
    }
    char* yp = ws + WS_Y + (size_t)kh * (S_ * D_ * 2) + go;
#pragma unroll
    for (int i = 0; i < 5; ++i) {
        const int Q = 5 * kq + i;
        xv[i][0] &= xm; xv[i][1] &= xm;
        const u32x2 o = {pk(k2[i][0] + bflo(xv[i][0]), k2[i][1] + bfhi(xv[i][0])), pk(k2[i][2] + bflo(xv[i][1]), k2[i][3] + bfhi(xv[i][1]))};
        *(u32x2*)(yp + (Q >> 2) * 64 + (Q & 3) * 8) = o;
    }
}

__global__ __launch_bounds__(512, 4) void k_mid(const char* __restrict__ x, const char* __restrict__ wg, const char* __restrict__ wu,
                                                const char* __restrict__ wd, char* __restrict__ ws, int mode) {
    extern __shared__ __attribute__((aligned(16))) char smem[];
    const int b = (blockIdx.x + mode) & 255, j = b & 7, i = b >> 3;
    if (blockIdx.x + mode < 256) {
        attn_role(ws, (LAS char*)smem, j * 4 + (i >> 3), i & 7);
        STAMP(2, 4);
    } else {
        __builtin_amdgcn_s_setprio(3);
        gu32* flags = (gu32*)(ws + WS_FLAG);
        gu_role(wg, wu, ws, (j >> 1) * 4 + (i & 3), (j & 1) * 8 + (i >> 2));
        asm volatile("s_waitcnt vmcnt(0) lgkmcnt(0)\n\ts_barrier" ::: "memory");
        if (threadIdx.x == 0) __hip_atomic_fetch_add(flags + (((j >> 1) * 4 + (i & 3)) * 2 + (j & 1)) * 64, 1u, __ATOMIC_RELAXED, __HIP_MEMORY_SCOPE_AGENT);
        STAMP(1, 4);
        down_role(x, wd, ws, (j >> 1) * 8 + (i & 7), i >> 3, j & 1, flags + ((((j >> 1) * 8 + (i & 7)) >> 1) * 2 + (j & 1)) * 64);
        STAMP(3, 4);
    }
}

__global__ __launch_bounds__(512, 2) void k_out(const char* __restrict__ wo, char* __restrict__ ws, char* __restrict__ out) {
    extern __shared__ __attribute__((aligned(16))) char smem[];
    LAS char* lds = (LAS char*)smem;
    const int b = blockIdx.x, j = b & 7, i = b >> 3, rb = j * 4 + (i & 3), cb = i >> 2;
    const int lane = threadIdx.x & 63, w = __builtin_amdgcn_readfirstlane(threadIdx.x >> 6);
    const unsigned lds0 = (unsigned)(uintptr_t)lds;
    const int t16 = w & 3, kh = w >> 2, r16 = lane & 15, q4 = lane >> 4;
    const char* wb = wo + cb * 160;
    STAMP(4, 0);
#pragma unroll
    for (int ii = 0; ii < 5; ++ii) {
        const int P = ii * 64 + lane, rho = P / 10, c = P - rho * 10, u = rho >> 4, qq = (rho >> 2) & 3, e = rho & 3;
        const unsigned va = (20 * (qq >> 1) + 4 * (qq & 1) + 8 * u + e) * 1280 + c * 16;
        glds16(va, wb + w * 51200, lds0 + w * 5120 + ii * 1024);
        glds16(va, wb + (w + 8) * 51200, lds0 + (w + 8) * 5120 + ii * 1024);
        if (w < 4) {
            const unsigned vb = (20 * (4 * (qq >> 1) + 2 * (qq & 1) + u) + 16 + e) * 1280 + c * 16;
            glds16(vb, wb + w * 204800, lds0 + (w + 16) * 5120 + ii * 1024);
        }
    }
    const char* op = ws + WS_O + (size_t)((rb * 2 + (t16 >> 1)) * 40 + kh * 20 + (q4 >> 1)) * 1024 + (q4 & 1) * 512 + ((t16 & 1) * 16 + r16) * 16;
    bf16x8 of[10];
#pragma unroll
    for (int s = 0; s < 10; ++s) of[s] = *(const bf16x8*)(op + s * 2048);
    const size_t go = (size_t)(rb * 64 + t16 * 16 + r16) * 1280 + cb * 160 + q4 * 40;
    u32x2 yv[5];
#pragma unroll
    for (int nt = 0; nt < 5; ++nt) yv[nt] = *(const u32x2*)(ws + WS_Y + (size_t)kh * (S_ * D_ * 2) + go + nt * 8);
    WAIT_BAR(0);
    STAMP(4, 1);
    f32x4 acc[5];
#pragma unroll
    for (int nt = 0; nt < 5; ++nt) acc[nt] = f32x4{bflo(yv[nt][0]), bfhi(yv[nt][0]), bflo(yv[nt][1]), bfhi(yv[nt][1])};
    const unsigned aw = kh * 51200 + (4 * q4 + ((lane >> 2) & 3)) * 160 + (lane & 3) * 40;
#pragma unroll
    for (int s = 0; s < 10; ++s)
#pragma unroll
        for (int nt = 0; nt < 5; ++nt) {
            const bf16x8 wf = cat(ldstr(lds + aw + s * 5120 + nt * 8), ldstr(lds + aw + s * 5120 + nt * 8 + 2560));
            acc[nt] = MFMA16X16(wf, of[s], acc[nt]);
        }
    STAMP(4, 3);
    BAR_L();
    if (kh == 1) {
#pragma unroll
        for (int nt = 0; nt < 5; ++nt) *(LAS f32x4*)(lds + (t16 * 5 + nt) * 1024 + lane * 16) = acc[nt];
    }
    BAR_L();
    if (kh == 0) {
#pragma unroll
        for (int nt = 0; nt < 5; ++nt) {
            const f32x4 t = *(LAS f32x4*)(lds + (t16 * 5 + nt) * 1024 + lane * 16);
            const u32x2 o = {pk(acc[nt][0] + t[0], acc[nt][1] + t[1]), pk(acc[nt][2] + t[2], acc[nt][3] + t[3])};
            *(u32x2*)(out + go + nt * 8) = o;
        }
    }
    STAMP(4, 4);
}

extern "C" void kernel_launch(void* const* d_in, const int* in_sizes, int n_in, void* d_out, int out_size, void* d_ws, size_t ws_size,
                              hipStream_t stream) {
    const char* x = (const char*)d_in[0];
    const char* wqkv = (const char*)d_in[1];
    const char* wo = (const char*)d_in[2];
    const char* wg = (const char*)d_in[4];
    const char* wu = (const char*)d_in[5];
    const char* wd = (const char*)d_in[6];
    char* ws = (char*)d_ws;
    (void)hipFuncSetAttribute((const void*)k_qkv, hipFuncAttributeMaxDynamicSharedMemorySize, 140288);
    (void)hipFuncSetAttribute((const void*)k_mid, hipFuncAttributeMaxDynamicSharedMemorySize, 81920);
    (void)hipFuncSetAttribute((const void*)k_out, hipFuncAttributeMaxDynamicSharedMemorySize, 102400);
    k_qkv<<<256, 768, 140288, stream>>>(x, wqkv, ws);
    k_mid<<<512, 512, 81920, stream>>>(x, wg, wu, wd, ws, 0);
    k_out<<<256, 512, 102400, stream>>>(wo, ws, (char*)d_out);
}
```

```cpp
#include <hip/hip_runtime.h>
#include <hip/hip_bf16.h>
#include <stdint.h>

typedef __attribute__((ext_vector_type(8))) short bf16x8;
typedef __attribute__((ext_vector_type(4))) short bf16x4;
typedef __attribute__((ext_vector_type(16))) float f32x16;
typedef __attribute__((ext_vector_type(4))) float f32x4;
typedef __attribute__((ext_vector_type(2))) float f32x2;
typedef __attribute__((ext_vector_type(4))) unsigned u32x4;
typedef __attribute__((ext_vector_type(2))) unsigned u32x2;
typedef __attribute__((ext_vector_type(2))) __bf16 bf2;
typedef __attribute__((address_space(1))) unsigned gu32;
typedef __amdgpu_buffer_rsrc_t rsrc_t;
#define LAS __attribute__((address_space(3)))
#define DI __device__ __forceinline__

constexpr unsigned WS_Q = 0;
constexpr unsigned WS_KV = WS_Q + 32 * 64 * 1536;
constexpr unsigned WS_XN = WS_KV + 32 * 64 * 3072;
constexpr unsigned WS_H = WS_XN + 64 * 40 * 1024;
constexpr unsigned WS_O = WS_H + 64 * 96 * 1024;
constexpr unsigned WS_Y = WS_O + 64 * 40 * 1024;
constexpr unsigned WS_ST = WS_Y + 2 * 64 * 4 * 20 * 1024;
constexpr unsigned WS_CS = WS_ST + 2048 * 16;
constexpr unsigned WS_FLAG = WS_CS + 8 * 1920 * 8;
constexpr unsigned WS_END = WS_FLAG + 65536;
constexpr int F_GEN = 0;
constexpr int F_ACK = 64;
constexpr int F_LN = 1024;
constexpr int F_H = 2048;
constexpr int F_O = 3072;
constexpr int F_Y = 4096;
constexpr int F_QKV = 8192;
constexpr size_t WS_DBG = 64u << 20;
#define STAMP(kid, i) do {} while (0)

#define MFMA16(a, b, c) __builtin_amdgcn_mfma_f32_32x32x16_bf16((a), (b), (c), 0, 0, 0)
#define MFMA8(a, b, c) __builtin_amdgcn_mfma_f32_32x32x8bf16_1k((a), (b), (c), 0, 0, 0)
#define MFMA16X16(a, b, c) __builtin_amdgcn_mfma_f32_16x16x32_bf16((a), (b), (c), 0, 0, 0)
#define LP(a) ((LAS char*)(uintptr_t)(a))
#define SBAR() __builtin_amdgcn_sched_barrier(0)
#define PIN(x) asm volatile("" : "+v"(x))
#define LGKM0() __builtin_amdgcn_s_waitcnt(0xC07F)
#define BAR() asm volatile("s_barrier" ::: "memory")
#define BAR_L() do { asm volatile("" ::: "memory"); LGKM0(); BAR(); } while (0)
#define VM_BAR(N) asm volatile("s_waitcnt vmcnt(" #N ")\n\ts_barrier" ::: "memory")
#define DRAIN_BAR() asm volatile("s_waitcnt vmcnt(0) lgkmcnt(0)\n\ts_barrier" ::: "memory")
#define VM_BAR2(sel, NA, NB) asm volatile("s_cmp_eq_u32 %0, 0\n\ts_cbranch_scc1 1f\n\ts_waitcnt vmcnt(" #NB ")\n1:\n\ts_waitcnt vmcnt(" #NA ")\n\ts_barrier" :: "s"(sel) : "memory", "scc")

DI void set_m0(unsigned lds) { asm volatile("s_mov_b32 m0, %0\n\ts_nop 0" :: "s"(lds) : "memory"); }
#define GLDS(voff, sbase, OFF) asm volatile("global_load_lds_dwordx4 %0, %1 offset:" #OFF :: "v"(voff), "s"(sbase) : "memory")
#define GLDS_SC1(voff, sbase, OFF) asm volatile("global_load_lds_dwordx4 %0, %1 offset:" #OFF " sc1" :: "v"(voff), "s"(sbase) : "memory")

DI int lane_id() { int l; asm volatile("v_mbcnt_lo_u32_b32 %0, -1, 0\n\tv_mbcnt_hi_u32_b32 %0, -1, %0" : "=v"(l)); return l; }
DI bf16x8 lds128(LAS char* p) { return *(LAS bf16x8*)p; }
DI bf16x4 lds64(LAS char* p) { return *(LAS bf16x4*)p; }
DI bf16x4 ldstr(LAS char* p) { return __builtin_amdgcn_ds_read_tr16_b64_v4i16((LAS bf16x4*)p); }
DI bf16x8 cat(bf16x4 a, bf16x4 b) { return __builtin_shufflevector(a, b, 0, 1, 2, 3, 4, 5, 6, 7); }
DI unsigned pk(float a, float b) { f32x2 f = {a, b}; return __builtin_bit_cast(unsigned, __builtin_convertvector(f, bf2)); }
DI float bflo(unsigned u) { return __uint_as_float(u << 16); }
DI float bfhi(unsigned u) { return __uint_as_float(u & 0xffff0000u); }
DI float swap32_add(float v) { auto r = __builtin_amdgcn_permlane32_swap(__float_as_uint(v), __float_as_uint(v), false, false); return __uint_as_float(r[0]) + __uint_as_float(r[1]); }
DI float swap32_max(float v) { auto r = __builtin_amdgcn_permlane32_swap(__float_as_uint(v), __float_as_uint(v), false, false); return __builtin_fmaxf(__uint_as_float(r[0]), __uint_as_float(r[1])); }
DI float addf(float a, float b) { float r; asm("v_add_f32 %0, %1, %2" : "=v"(r) : "v"(a), "v"(b)); return r; }
DI float mulf(float a, float b) { float r; asm("v_mul_f32 %0, %1, %2" : "=v"(r) : "v"(a), "v"(b)); return r; }
DI float relu(float a) { float r; asm("v_max_f32 %0, 0, %1" : "=v"(r) : "v"(a)); return r; }
DI f32x4 add4(f32x4 a, f32x4 b) { return f32x4{addf(a[0], b[0]), addf(a[1], b[1]), addf(a[2], b[2]), addf(a[3], b[3])}; }
DI void wst128(rsrc_t rs, unsigned off, u32x4 v) { __builtin_amdgcn_raw_buffer_store_b128(v, rs, off, 0, 16); }
DI void wst64(rsrc_t rs, unsigned off, u32x2 v) { __builtin_amdgcn_raw_buffer_store_b64(v, rs, off, 0, 16); }
DI void wst32(rsrc_t rs, unsigned off, unsigned v) { __builtin_amdgcn_raw_buffer_store_b32(v, rs, off, 0, 16); }
DI u32x4 wld128(rsrc_t rs, unsigned off) { return __builtin_amdgcn_raw_buffer_load_b128(rs, off, 0, 16); }
DI u32x2 wld64(rsrc_t rs, unsigned off) { return __builtin_amdgcn_raw_buffer_load_b64(rs, off, 0, 16); }
DI unsigned wld32(rsrc_t rs, unsigned off) { return __builtin_amdgcn_raw_buffer_load_b32(rs, off, 0, 16); }
#define SPIN_ST (1 << 18)
#define PRIO_EPI 1
#define PRIO_B0 3
#define PRIO_A0 2
#define POLL_SLEEP 8
DI void wait_tags(gu32* flags, int word, unsigned tag) {
    for (int spin = 0; spin < (1 << 18); ++spin) {
        const unsigned v = __hip_atomic_load(flags + word, __ATOMIC_RELAXED, __HIP_MEMORY_SCOPE_AGENT);
        if (__all(v == tag)) break;
        __builtin_amdgcn_s_sleep(POLL_SLEEP);
    }
    asm volatile("" ::: "memory");
}
DI void wait_tags2(gu32* flags, int word0, int word1, unsigned tag) {
    for (int spin = 0; spin < (1 << 18); ++spin) {
        const unsigned v0 = __hip_atomic_load(flags + word0, __ATOMIC_RELAXED, __HIP_MEMORY_SCOPE_AGENT);
        const unsigned v1 = __hip_atomic_load(flags + word1, __ATOMIC_RELAXED, __HIP_MEMORY_SCOPE_AGENT);
        if (__all(v0 == tag && v1 == tag)) break;
        __builtin_amdgcn_s_sleep(POLL_SLEEP);
    }
    asm volatile("" ::: "memory");
}
DI void publish(gu32* flags, int word, bool on, unsigned tag) { if (on) __hip_atomic_store(flags + word, tag, __ATOMIC_RELAXED, __HIP_MEMORY_SCOPE_AGENT); }
#define TAG_OF(gen) ((unsigned)__builtin_amdgcn_readfirstlane(gen) + 1u)

DI unsigned ln_role(const char* x, const char* wqkv, rsrc_t rs, unsigned gen, int n, int w) {
    const int lane = lane_id(), r = lane & 31, h = lane >> 5;
    const char* src = x + (size_t)(n * 8 + w) * 1280;
    const u32x4 v0 = *(const u32x4*)(src + lane * 16);
    u32x4 v1 = *(const u32x4*)(src + 1024 + (lane & 15) * 16);
    unsigned cv[5];
    const int nc = n < 240 ? n : 0;
    const char* wsrc = wqkv + (size_t)((nc / 30) * 80 + w * 10 + h) * 3840 + (nc % 30) * 128 + r * 4;
#pragma unroll
    for (int i = 0; i < 5; ++i) cv[i] = *(const unsigned*)(wsrc + i * 7680);
    SBAR();
    const unsigned tag = TAG_OF(gen);
    if (lane >= 16) v1 = u32x4{0u, 0u, 0u, 0u};
    float s = 0.f;
#pragma unroll
    for (int e = 0; e < 4; ++e) s += (bflo(v0[e]) + bfhi(v0[e])) + (bflo(v1[e]) + bfhi(v1[e]));
#pragma unroll
    for (int m = 1; m < 32; m <<= 1) s += __shfl_xor(s, m);
    const float mu = swap32_add(s) * (1.f / 640.f);
    float a0[8], a1[8], q = 0.f, q1 = 0.f;
#pragma unroll
    for (int e = 0; e < 4; ++e) {
        a0[2 * e] = bflo(v0[e]) - mu; a0[2 * e + 1] = bfhi(v0[e]) - mu; a1[2 * e] = bflo(v1[e]) - mu; a1[2 * e + 1] = bfhi(v1[e]) - mu;
        q = __builtin_fmaf(a0[2 * e], a0[2 * e], q); q = __builtin_fmaf(a0[2 * e + 1], a0[2 * e + 1], q);
        q1 = __builtin_fmaf(a1[2 * e], a1[2 * e], q1); q1 = __builtin_fmaf(a1[2 * e + 1], a1[2 * e + 1], q1);
    }
    if (lane < 16) q += q1;
#pragma unroll
    for (int m = 1; m < 32; m <<= 1) q += __shfl_xor(q, m);
    const float rstd = __builtin_amdgcn_rsqf(swap32_add(q) * (1.f / 640.f) + 1e-5f);
    if (lane < 2) wst64(rs, WS_ST + (n * 8 + w) * 16 + lane * 8, u32x2{__float_as_uint(lane ? -mu : rstd), tag});
    u32x4 o0, o1;
#pragma unroll
    for (int e = 0; e < 4; ++e) { o0[e] = pk(a0[2 * e] * rstd, a0[2 * e + 1] * rstd); o1[e] = pk(a1[2 * e] * rstd, a1[2 * e + 1] * rstd); }
    *(LAS u32x4*)LP(lane * 128 + w * 16) = o0;
    if (lane < 16) *(LAS u32x4*)LP(8192 + lane * 128 + w * 16) = o1;
    float c0 = 0.f, c1 = 0.f;
#pragma unroll
    for (int i = 0; i < 5; ++i) { c0 += bflo(cv[i]); c1 += bfhi(cv[i]); }
    c0 = swap32_add(c0); c1 = swap32_add(c1);
    if (h == 0) *(LAS f32x2*)LP(10496 + w * 256 + r * 8) = f32x2{c0, c1};
    BAR_L();
    const int t = w * 64 + lane;
    const unsigned xb = WS_XN + (n >> 2) * 40960 + ((n & 3) * 8 + (t & 7)) * 16;
    wst128(rs, xb + (t >> 3) * 512, *(LAS u32x4*)LP(t * 16));
    if (t < 128) wst128(rs, xb + (64 + (t >> 3)) * 512, *(LAS u32x4*)LP(8192 + t * 16));
    else if (t < 192 && n < 240) {
        float c = *(LAS float*)LP(10496 + lane * 4);
#pragma unroll
        for (int i = 1; i < 8; ++i) c += *(LAS float*)LP(10496 + i * 256 + lane * 4);
        wst64(rs, WS_CS + ((n / 30) * 1920 + (n % 30) * 64 + lane) * 8, u32x2{__float_as_uint(c), tag});
    }
    return tag;
}

DI void qkv_loader(const char* x, const char* wqkv, rsrc_t rs, unsigned gen, int rb, int cb, int lw) {
    const int lane = lane_id();
    const int ra0 = lane >> 3, ra1 = ra0 + 8;
    const unsigned vA0 = ra0 * 1280 + (((lane & 7) ^ ((ra0 >> 1) & 7)) << 4);
    const unsigned vA1 = ra1 * 1280 + (((lane & 7) ^ ((ra1 >> 1) & 7)) << 4) - 1024;
    const unsigned vW0 = ((lane >> 2) & 3) * 3840 + (lane >> 4) * 64 + (lane & 3) * 16;
    const unsigned vW1 = vW0 + 15360 - 1024;
    const char* xa = x + (size_t)(rb * 128 + lw * 16) * 1280;
    const char* wb = wqkv + (size_t)lw * 8 * 3840 + cb * 256;
#define QX(u, slot) do { set_m0((slot) * 16384 + ((u) & 1) * 8192 + lw * 2048); const char* p_ = xa + ((u) >> 1) * 128 + ((u) & 1) * 81920; GLDS(vA0, p_, 0); GLDS(vA1, p_, 1024); } while (0)
#define QW(u) do { set_m0(49152 + ((u) & 3) * 8192 + lw * 2048); const char* p_ = wb + (size_t)(u) * 122880; GLDS(vW0, p_, 0); GLDS(vW1, p_, 1024); } while (0)
    QX(0, 0); QW(0); QX(1, 0); QW(1); QX(2, 1);
    VM_BAR(4);
    QW(2); QX(3, 1);
    int slot = 2;
#pragma unroll 1
    for (int u = 3; u < 19; u += 2) {
        QW(u); QX(u + 1, slot);
        VM_BAR(8);
        QW(u + 1); QX(u + 2, slot);
        VM_BAR(8);
        slot = slot == 2 ? 0 : slot + 1;
    }
    QW(19);
    u32x4 st0, st1; u32x2 cp[8];
    const unsigned so = WS_ST + (rb * 128 + lane) * 16, co = WS_CS + (cb * 128 + (lw & 1) * 64 + lane) * 8;
    if (lw == 0) { st0 = wld128(rs, so); st1 = wld128(rs, so + 1024); VM_BAR(8); VM_BAR(4); VM_BAR(2); }
    else if (lw < 3) {
#pragma unroll
        for (int i = 0; i < 8; ++i) cp[i] = wld64(rs, co + i * 15360);
        VM_BAR(14); VM_BAR(10); VM_BAR(8);
    } else { VM_BAR(6); VM_BAR(2); VM_BAR(0); }
    const unsigned tag = TAG_OF(gen);
#undef QX
#undef QW
    if (lw == 0) {
        for (int spin = 0; spin < SPIN_ST && !__all(st0[1] == tag && st0[3] == tag && st1[1] == tag && st1[3] == tag); ++spin) {
            __builtin_amdgcn_s_sleep(4); st0 = wld128(rs, so); st1 = wld128(rs, so + 1024);
        }
        const float sc = cb < 5 ? 0.22360679774997896f * 1.4426950408889634f : 1.f;
        *(LAS float*)LP(16384 + lane * 4) = __uint_as_float(st0[0]) * sc; *(LAS float*)LP(16640 + lane * 4) = __uint_as_float(st1[0]) * sc;
        *(LAS unsigned*)LP(16896 + lane * 4) = st0[2]; *(LAS unsigned*)LP(17152 + lane * 4) = st1[2];
    } else if (lw < 3) {
        for (int spin = 0; spin < SPIN_ST; ++spin) {
            bool ok = true;
#pragma unroll
            for (int i = 0; i < 8; ++i) ok = ok && cp[i][1] == tag;
            if (__all(ok)) break;
            __builtin_amdgcn_s_sleep(4);
#pragma unroll
            for (int i = 0; i < 8; ++i) cp[i] = wld64(rs, co + i * 15360);
        }
        float c = 0.f;
#pragma unroll
        for (int i = 0; i < 8; ++i) c += __uint_as_float(cp[i][0]);
        *(LAS float*)LP(17408 + ((lw & 1) * 64 + lane) * 4) = c;
    }
    BAR_L();
}

template <bool VT>
DI void qkv_tile(char* ws, rsrc_t rs, int rb, int cb, int w) {
    const int lane = lane_id();
    const int wm = w >> 1, wn = w & 1, r = lane & 31, h = lane >> 5;
    const int xrow = wm * 64 + r;
    unsigned ax[4];
#pragma unroll
    for (int p = 0; p < 4; ++p) ax[p] = xrow * 128 + (((2 * p + h) ^ ((xrow >> 1) & 7)) << 4);
    const int c4 = lane & 3, cq = VT ? c4 : (((c4 & 1) << 1) | (c4 >> 1));
    const unsigned aw = 49152 + 2 * h * 1024 + wn * 512 + ((lane >> 2) & 3) * 64 + ((lane >> 4) & 1) * 32 + cq * 8;
    f32x16 acc[2][2] = {};
    bf16x8 x0[2], w0[2], w10, x1A[2], w11A, x1B[2], w11B;
#define QRX(i, A) lds128(LP(A) + (i) * 4096)
#define QRW(ks, n, A) cat(ldstr(LP(A) + (ks) * 4096 + (n) * 256), ldstr(LP(A) + (ks) * 4096 + (n) * 256 + 1024))
#define QMM(W, X, n, i) do { acc[n][i] = VT ? MFMA16(X, W, acc[n][i]) : MFMA16(W, X, acc[n][i]); PIN(acc[n][i]); SBAR(); } while (0)
#define QSTEP(AX0, AX1, AW, X1, W11, X1N, W11N) do { BAR_L(); \
        QMM(w10, X1[0], 0, 0); X1N[0] = QRX(0, AX1); X1N[1] = QRX(1, AX1); SBAR(); QMM(w10, X1[1], 0, 1); w10 = QRW(1, 0, AW); SBAR(); \
        QMM(w0[0], x0[0], 0, 0); W11N = QRW(1, 1, AW); SBAR(); QMM(w0[0], x0[1], 0, 1); w0[0] = QRW(0, 0, AW); SBAR(); \
        QMM(w0[1], x0[0], 1, 0); x0[0] = QRX(0, AX0); SBAR(); QMM(w0[1], x0[1], 1, 1); w0[1] = QRW(0, 1, AW); x0[1] = QRX(1, AX0); SBAR(); \
        QMM(W11, X1[0], 1, 0); QMM(W11, X1[1], 1, 1); } while (0)
#define QLAST(X1, W11) do { QMM(w10, X1[0], 0, 0); QMM(w10, X1[1], 0, 1); QMM(w0[0], x0[0], 0, 0); QMM(w0[0], x0[1], 0, 1); \
        QMM(w0[1], x0[0], 1, 0); QMM(w0[1], x0[1], 1, 1); QMM(W11, X1[0], 1, 0); QMM(W11, X1[1], 1, 1); } while (0)
    BAR();
    STAMP(0, 1);
    x1A[0] = QRX(0, ax[1]); x1A[1] = QRX(1, ax[1]); w10 = QRW(1, 0, aw); w0[0] = QRW(0, 0, aw); x0[0] = QRX(0, ax[0]); x0[1] = QRX(1, ax[0]);
    w0[1] = QRW(0, 1, aw); w11A = QRW(1, 1, aw);
    unsigned xs = 0, wo = 8192;
#pragma unroll 1
    for (int u = 1; u < 19; u += 2) {
        { const unsigned a0 = ax[2] + xs, a1 = ax[3] + xs, a2 = aw + wo; QSTEP(a0, a1, a2, x1A, w11A, x1B, w11B); }
        xs = xs == 32768 ? 0 : xs + 16384; wo = (wo + 8192) & 24576;
        { const unsigned a0 = ax[0] + xs, a1 = ax[1] + xs, a2 = aw + wo; QSTEP(a0, a1, a2, x1B, w11B, x1A, w11A); }
        wo = (wo + 8192) & 24576;
        if (u == 15) STAMP(0, 2);
    }
    STAMP(0, 5);
    { const unsigned a0 = ax[2] + xs, a1 = ax[3] + xs, a2 = aw + wo; QSTEP(a0, a1, a2, x1A, w11A, x1B, w11B); }
    QLAST(x1B, w11B);
    BAR();
    if (!VT) {
        float a[2], b[2];
#pragma unroll
        for (int i = 0; i < 2; ++i) { a[i] = *(LAS float*)LP(16384 + (xrow + 32 * i) * 4); b[i] = *(LAS float*)LP(16896 + (xrow + 32 * i) * 4); }
        f32x4 cs[2][2][2];
#pragma unroll
        for (int n = 0; n < 2; ++n)
#pragma unroll
            for (int o = 0; o < 2; ++o)
#pragma unroll
                for (int e = 0; e < 2; ++e) cs[n][o][e] = *(LAS f32x4*)LP(17408 + (wn * 64 + n * 32 + o * 16 + h * 8 + e * 4) * 4);
        SBAR();
        const unsigned rec = cb < 5 ? 1536 : 3072;
        const unsigned base = (cb < 5 ? WS_Q : WS_KV) + (rb * 4 + wm * 2) * rec + r * 16;
#pragma unroll
        for (int n = 0; n < 2; ++n)
#pragma unroll
            for (int o = 0; o < 2; ++o) {
                const int G = (cb % 5) * 16 + wn * 8 + n * 4 + o * 2 + h, P = G / 5, gp = G - P * 5;
                const unsigned off = base + (2 * P + (gp > 2)) * 64 * rec + (gp > 2 ? gp - 3 : gp) * 512;
#pragma unroll
                for (int i = 0; i < 2; ++i) {
                    const f32x16& c = acc[n][i];
                    const f32x4 v0 = (f32x4{c[8 * o], c[8 * o + 1], c[8 * o + 2], c[8 * o + 3]} + cs[n][o][0] * b[i]) * a[i];
                    const f32x4 v1 = (f32x4{c[8 * o + 4], c[8 * o + 5], c[8 * o + 6], c[8 * o + 7]} + cs[n][o][1] * b[i]) * a[i];
                    const u32x4 ov = {pk(v0[0], v0[1]), pk(v0[2], v0[3]), pk(v1[0], v1[1]), pk(v1[2], v1[3])};
                    wst128(rs, off + i * rec, ov);
                    if (gp == 2) wst128(rs, off + i * rec + 64 * rec, ov);
                }
            }
    } else {
        float cs[2];
#pragma unroll
        for (int n = 0; n < 2; ++n) cs[n] = *(LAS float*)LP(17408 + (wn * 64 + n * 32 + r) * 4);
#pragma unroll
        for (int n = 0; n < 2; ++n) {
            const int col = (cb - 10) * 128 + wn * 64 + n * 32 + r, hd = col / 20, d = col - hd * 20;
            const unsigned off = WS_KV + (hd * 64 + rb * 4 + wm * 2) * 3072 + 1536 + h * 384 + d * 16;
#pragma unroll
            for (int i = 0; i < 2; ++i)
#pragma unroll
                for (int s = 0; s < 2; ++s) {
                    u32x4 ov;
#pragma unroll
                    for (int gg = 0; gg < 2; ++gg) {
                        const int g = 2 * s + gg;
                        const f32x4 a = *(LAS f32x4*)LP(16384 + (wm * 64 + i * 32 + 8 * g + 4 * h) * 4), b = *(LAS f32x4*)LP(16896 + (wm * 64 + i * 32 + 8 * g + 4 * h) * 4);
                        const f32x16& c = acc[n][i];
                        const f32x4 v = (f32x4{c[4 * g], c[4 * g + 1], c[4 * g + 2], c[4 * g + 3]} + b * cs[n]) * a;
                        ov[2 * gg] = pk(v[0], v[1]); ov[2 * gg + 1] = pk(v[2], v[3]);
                    }
                    wst128(rs, off + i * 3072 + s * 768, ov);
                }
            if (d == 19) {
                const u32x4 one = {0x3F803F80u, 0x3F803F80u, 0x3F803F80u, 0x3F803F80u}, zr = {0u, 0u, 0u, 0u};
#pragma unroll
                for (int i = 0; i < 4; ++i) { wst128(rs, off + (i >> 1) * 3072 + (i & 1) * 768 + 16, one); wst128(rs, off + (i >> 1) * 3072 + (i & 1) * 768 + 32, zr); }
            }
        }
    }
#undef QRX
#undef QRW
#undef QMM
#undef QSTEP
#undef QLAST
}

DI float max16(const f32x16& s) {
    float a = __builtin_fmaxf(__builtin_fmaxf(s[0], s[1]), s[2]), b = __builtin_fmaxf(__builtin_fmaxf(s[3], s[4]), s[5]);
    a = __builtin_fmaxf(__builtin_fmaxf(a, s[6]), s[7]); b = __builtin_fmaxf(__builtin_fmaxf(b, s[8]), s[9]);
    a = __builtin_fmaxf(__builtin_fmaxf(a, s[10]), s[11]); b = __builtin_fmaxf(__builtin_fmaxf(b, s[12]), s[13]);
    a = __builtin_fmaxf(__builtin_fmaxf(a, s[14]), s[15]);
    return swap32_max(__builtin_fmaxf(a, b));
}

DI void attn_role(char* ws, rsrc_t rs, int hd, int qb, int w) {
    const int lane = lane_id(), r = lane & 31, h = lane >> 5;
    const char* kv = ws + WS_KV + (size_t)hd * 196608 + w * 3072;
    const unsigned voff = lane * 16;
#define KV_DMA(s, buf) do { set_m0((buf) + w * 3072); const char* p_ = kv + (s) * 24576; GLDS_SC1(voff, p_, 0); GLDS_SC1(voff, p_, 1024); GLDS_SC1(voff, p_, 2048); } while (0)
    const int qt = qb * 8 + w;
    const unsigned qo = WS_Q + (hd * 64 + qt) * 1536;
    const bf16x8 q1 = __builtin_bit_cast(bf16x8, wld128(rs, qo + lane * 16));
    bf16x4 q2 = __builtin_bit_cast(bf16x4, wld64(rs, qo + 1024 + r * 16 + (hd & 1) * 8));
    KV_DMA(0, 0);
    if (h) q2 = bf16x4{0, 0, 0, 0};
    const unsigned aK = lane * 16, aK2 = h ? 1536 + 320 : 1024 + r * 16 + (hd & 1) * 8, aV = 1536 + h * 384 + (r < 21 ? r : 21) * 16;
    const f32x16 zero = {};
    VM_BAR(0);
    KV_DMA(1, 24576);
    STAMP(2, 1);
    bf16x8 k1 = lds128(LP(aK)); bf16x4 k2 = lds64(LP(aK2));
    f32x16 sA = MFMA8(k2, q2, MFMA16(k1, q1, zero));
    const bf16x4 q2raw = q2;
    { const float m = max16(sA); if (h) q2[0] = (short)(pk(-m, 0.f) & 0xffffu); }
    sA = MFMA8(k2, q2, MFMA16(k1, q1, zero));
    k1 = lds128(LP(3072 + aK)); k2 = lds64(LP(3072 + aK2));
    bf16x8 v0 = lds128(LP(aV)), v1 = v0;
    f32x16 sB, o = {};
    u32x4 p0, p1 = {0u, 0u, 0u, 0u};
    float e12 = 0.f, e13 = 0.f, e14 = 0.f, e15 = 0.f;
#define EX(v) __builtin_amdgcn_exp2f(v)
#define ATT_STEP(SC, SN, K1A, K2A, V1A, V0A) do { \
        SN = MFMA16(k1, q1, zero); SBAR(); \
        k1 = lds128(K1A); \
        p1[2] = pk(e12, e13); p1[3] = pk(e14, e15); PIN(p1); \
        float e0 = EX(SC[0]), e1 = EX(SC[1]), e2 = EX(SC[2]), e3 = EX(SC[3]); PIN(e0); PIN(e1); PIN(e2); PIN(e3); SBAR(); \
        SN = MFMA8(k2, q2, SN); SBAR(); \
        k2 = lds64(K2A); \
        p0[0] = pk(e0, e1); p0[1] = pk(e2, e3); PIN(p0); \
        float e4 = EX(SC[4]), e5 = EX(SC[5]), e6 = EX(SC[6]), e7 = EX(SC[7]); PIN(e4); PIN(e5); PIN(e6); PIN(e7); SBAR(); \
        o = MFMA16(v1, __builtin_bit_cast(bf16x8, p1), o); SBAR(); \
        v1 = lds128(V1A); \
        p0[2] = pk(e4, e5); p0[3] = pk(e6, e7); PIN(p0); \
        float e8 = EX(SC[8]), e9 = EX(SC[9]), e10 = EX(SC[10]), e11 = EX(SC[11]); PIN(e8); PIN(e9); PIN(e10); PIN(e11); SBAR(); \
        o = MFMA16(v0, __builtin_bit_cast(bf16x8, p0), o); SBAR(); \
        v0 = lds128(V0A); \
        p1[0] = pk(e8, e9); p1[1] = pk(e10, e11); PIN(p1); \
        e12 = EX(SC[12]); e13 = EX(SC[13]); e14 = EX(SC[14]); e15 = EX(SC[15]); PIN(e12); PIN(e13); PIN(e14); PIN(e15); SBAR(); \
    } while (0)
#define ATT_T(SC, SN, t) ATT_STEP(SC, SN, cK + ((t) + 2) * 3072, cK2 + ((t) + 2) * 3072, cV + (t) * 3072 + 768, cV + ((t) + 1) * 3072)
    unsigned cur = 0;
    __builtin_amdgcn_s_setprio(0);
#pragma unroll 1
    for (int s = 0; s < 8; ++s) {
        const unsigned nxt = cur == 49152 ? 0 : cur + 24576;
        LAS char *cK = LP(aK + cur), *cK2 = LP(aK2 + cur), *cV = LP(aV + cur);
        ATT_T(sA, sB, 0); ATT_T(sB, sA, 1); ATT_T(sA, sB, 2); ATT_T(sB, sA, 3); ATT_T(sA, sB, 4); ATT_T(sB, sA, 5);
        VM_BAR(0);
        if (s < 6) KV_DMA(s + 2, nxt == 49152 ? 0 : nxt + 24576);
        LAS char *nK = LP(aK + nxt), *nK2 = LP(aK2 + nxt), *nV = LP(aV + nxt);
        ATT_STEP(sA, sB, nK, nK2, cV + 6 * 3072 + 768, cV + 7 * 3072);
        ATT_STEP(sB, sA, nK + 3072, nK2 + 3072, cV + 7 * 3072 + 768, nV);
        cur = nxt;
    }
    p1[2] = pk(e12, e13); p1[3] = pk(e14, e15);
    o = MFMA16(v1, __builtin_bit_cast(bf16x8, p1), o);
#undef ATT_STEP
#undef ATT_T
#undef KV_DMA
    STAMP(2, 3);
    float l;
    { auto rr = __builtin_amdgcn_permlane32_swap(__float_as_uint(o[8]), __float_as_uint(o[8]), false, false); l = __uint_as_float(rr[1]); }
    float am = 0.f;
#pragma unroll
    for (int i = 0; i < 12; ++i) am = __builtin_fmaxf(am, __builtin_fabsf(o[i]));
    const bool ok = (l > 5.4e-20f) && (l < 1.8e19f) && (am < 3.0e38f);
    if (__builtin_expect(!__all(ok), 0)) {
        float m = -INFINITY;
        o = zero;
#pragma unroll 1
        for (int t = 0; t < 64; ++t) {
            const unsigned rec = WS_KV + (hd * 64 + t) * 3072;
            const bf16x8 fk1 = __builtin_bit_cast(bf16x8, wld128(rs, rec + aK)); const bf16x4 fk2 = __builtin_bit_cast(bf16x4, wld64(rs, rec + aK2));
            const bf16x8 fv0 = __builtin_bit_cast(bf16x8, wld128(rs, rec + aV)), fv1 = __builtin_bit_cast(bf16x8, wld128(rs, rec + aV + 768));
            f32x16 sf = MFMA8(fk2, q2raw, MFMA16(fk1, q1, zero));
            const float mn = __builtin_fmaxf(m, max16(sf));
            const float al = EX(m - mn);
            m = mn;
            u32x4 f0, f1;
#pragma unroll
            for (int i = 0; i < 16; ++i) o[i] *= al;
#pragma unroll
            for (int e = 0; e < 4; ++e) { f0[e] = pk(EX(sf[2 * e] - mn), EX(sf[2 * e + 1] - mn)); f1[e] = pk(EX(sf[8 + 2 * e] - mn), EX(sf[8 + 2 * e + 1] - mn)); }
            o = MFMA16(fv0, __builtin_bit_cast(bf16x8, f0), o); o = MFMA16(fv1, __builtin_bit_cast(bf16x8, f1), o);
        }
        auto rr = __builtin_amdgcn_permlane32_swap(__float_as_uint(o[8]), __float_as_uint(o[8]), false, false); l = __uint_as_float(rr[1]);
    }
#undef EX
    const float inv = 1.f / l;
    const unsigned op = WS_O + qt * 40960;
    u32x4 oa;
#pragma unroll
    for (int e = 0; e < 4; ++e) oa[e] = pk(o[2 * e] * inv, o[2 * e + 1] * inv);
    wst128(rs, op + hd * 1024 + lane * 16, oa);
    if (h == 0) wst64(rs, op + 32768 + hd * 256 + r * 8, u32x2{pk(o[8] * inv, o[9] * inv), pk(o[10] * inv, o[11] * inv)});
}

DI void gu_role(const char* wg, const char* wu, char* ws, rsrc_t rs, gu32* flags, unsigned tag, int rb, int fb, int w) {
    const int lane = lane_id();
    const int h = lane >> 5, tt = w & 3, fh = w >> 2;
    const int q = (lane >> 2) & 3, c = lane & 3, gi = (lane >> 4) & 1;
    const char* xs = ws + WS_XN + (size_t)((rb * 4 + (w >> 1)) * 40 + (w & 1)) * 1024;
    const unsigned voffX = lane * 16;
    const int m0 = w >= 6, i0 = m0 ? w - 6 : w, st0 = 4 * i0 + (lane >> 4);
    const unsigned voffW0 = (4 * (st0 / 3) + q) * 3072 + (st0 % 3) * 64 + ((m0 ? c ^ 2 : c) << 4);
    const int st1 = 4 * (2 + (w & 3)) + (lane >> 4);
    const unsigned voffW1 = (4 * (st1 / 3) + q) * 3072 + (st1 % 3) * 64 + ((c ^ 2) << 4);
    const char* wb0 = (m0 ? wu : wg) + fb * 192;
    const char* wb1 = wu + fb * 192;
#define GU_P0(kt) do { set_m0(((kt) & 3) * 20480 + w * 1024); GLDS_SC1(voffX, xs + (kt) * 2048, 0); } while (0)
#define GU_P1(kt) do { set_m0(((kt) & 3) * 20480 + w * 1024 + 8192); GLDS(voffW0, wb0 + (kt) * 98304, 0); } while (0)
#define GU_P2(kt) do { if (fh == 0) { set_m0(((kt) & 3) * 20480 + w * 1024 + 16384); GLDS(voffW1, wb1 + (kt) * 98304, 0); } } while (0)
    GU_P1(0); GU_P2(0); GU_P1(1); GU_P2(1); GU_P1(2); GU_P2(2);
    if (w == 0) wait_tags(flags, F_LN + rb * 32 + (lane & 15), tag);
    BAR();
    STAMP(1, 0);
    GU_P0(0); GU_P0(1); GU_P0(2);
    unsigned ax = tt * 2048 + lane * 16;
    const int sw = ((c & 1) << 1) | (c >> 1);
    unsigned aw[3];
#pragma unroll
    for (int a = 0; a < 3; ++a) { const int nt = 3 * fh + a; aw[a] = 8192 + gi * 6144 + (2 * h * 3 + (nt >> 1)) * 256 + q * 64 + (((nt & 1) ^ gi) << 5) + sw * 8; }
    f32x16 acc[3] = {};
    bf16x8 xf[2], wf[2][3];
#define GU_RW(ks, a, OFF) wf[ks][a] = cat(ldstr(LP(aw[a]) + (OFF) + (ks) * 3072), ldstr(LP(aw[a]) + (OFF) + (ks) * 3072 + 768))
#define GU_MM(ks, a, OFF) do { acc[a] = MFMA16(wf[ks][a], xf[ks], acc[a]); PIN(acc[a]); SBAR(); GU_RW(ks, a, OFF); SBAR(); } while (0)
#define GU_STEP(kt, OFF, DMA) do { \
        LGKM0(); \
        if (DMA) VM_BAR2(fh, 3, 2); else VM_BAR(0); \
        GU_MM(0, 0, OFF); if (DMA) GU_P0((kt) + 3); GU_MM(0, 1, OFF); GU_MM(0, 2, OFF); xf[0] = lds128(LP(ax) + (OFF)); if (DMA) GU_P1((kt) + 3); \
        GU_MM(1, 0, OFF); GU_MM(1, 1, OFF); if (DMA) GU_P2((kt) + 3); GU_MM(1, 2, OFF); xf[1] = lds128(LP(ax) + (OFF) + 1024); SBAR(); } while (0)
    VM_BAR(1);
    STAMP(1, 1);
#pragma unroll
    for (int ks = 0; ks < 2; ++ks) { xf[ks] = lds128(LP(ax) + ks * 1024); GU_RW(ks, 0, 0); GU_RW(ks, 1, 0); GU_RW(ks, 2, 0); }
    __builtin_amdgcn_s_setprio(0);
#pragma unroll 1
    for (int kt = 0; kt < 16; kt += 4) {
        GU_STEP(kt, 20480, true); GU_STEP(kt + 1, 40960, true);
        ax += 61440; aw[0] += 61440; aw[1] += 61440; aw[2] += 61440;
        GU_STEP(kt + 2, 0, true);
        ax -= 61440; aw[0] -= 61440; aw[1] -= 61440; aw[2] -= 61440;
        GU_STEP(kt + 3, 0, true);
    }
    GU_STEP(16, 20480, true); GU_STEP(17, 40960, false);
    ax += 61440; aw[0] += 61440; aw[1] += 61440; aw[2] += 61440;
    GU_STEP(18, 0, false);
#pragma unroll
    for (int ks = 0; ks < 2; ++ks)
#pragma unroll
        for (int a = 0; a < 3; ++a) acc[a] = MFMA16(wf[ks][a], xf[ks], acc[a]);
#undef GU_P0
#undef GU_P1
#undef GU_P2
#undef GU_RW
#undef GU_MM
#undef GU_STEP
    asm volatile("s_nop 15" : "+v"(acc[0]), "+v"(acc[1]), "+v"(acc[2]));
    __builtin_amdgcn_s_setprio(PRIO_EPI);
    STAMP(1, 3);
    const unsigned hp = WS_H + ((rb * 4 + tt) * 96 + fb * 6 + 3 * fh) * 1024 + lane * 16;
#pragma unroll
    for (int a = 0; a < 3; ++a) {
        u32x4 o;
#pragma unroll
        for (int e = 0; e < 4; ++e)
            o[e] = pk(mulf(relu(acc[a][2 * e]), acc[a][8 + 2 * e]), mulf(relu(acc[a][2 * e + 1]), acc[a][8 + 2 * e + 1]));
        wst128(rs, hp + a * 1024, o);
    }
}

DI void down_role(const char* x, const char* wd, char* ws, rsrc_t rs, gu32* flags, unsigned tag, int rb, int cb, int kh, int w) {
    const int lane = lane_id();
    const int r = lane & 31, h = lane >> 5, tt = w & 1, kq = w >> 1;
    const int q = (lane >> 2) & 3, c = lane & 3, gi = (lane >> 4) & 1;
    const int ks0 = kh * 48 + kq * 12;
    unsigned vw[3];
#pragma unroll
    for (int i = 0; i < 3; ++i) { const int st = 4 * (tt * 3 + i) + (lane >> 4); vw[i] = (4 * (st / 5) + q) * 1280 + (st % 5) * 64 + c * 16 + 2048 - i * 1024; }
    const char* wb = wd + (size_t)ks0 * 20480 + cb * 320 - 2048;
    const char* hb = ws + WS_H + (size_t)((rb * 2 + tt) * 96 + ks0) * 1024;
    const unsigned voffH = lane * 16;
    bf16x8 hf0, hf1, hf2, wf[5];
#define DN_W(t) do { set_m0(((t) & 3) * 20480 + kq * 5120 + tt * 3072); GLDS(vw[0], wb + (t) * 20480, 0); GLDS(vw[1], wb + (t) * 20480, 1024); \
        if (tt == 0) GLDS(vw[2], wb + (t) * 20480, 2048); } while (0)
#define DN_H(t, HF) asm volatile("global_load_dwordx4 %0, %1, %2 sc1" : "=v"(HF) : "v"(voffH), "s"(hb + (t) * 1024) : "memory")
#define DN_WAIT(N0, N1, HF) asm volatile("s_cmp_eq_u32 %1, 0\n\ts_cbranch_scc1 1f\n\ts_waitcnt vmcnt(" #N1 ")\n1:\n\ts_waitcnt vmcnt(" #N0 ")\n\ts_barrier" \
        : "+v"(HF) : "s"(tt) : "memory", "scc")
#define DN_RW(nt, OFF) wf[nt] = cat(ldstr(LP(aw) + (OFF) + (nt) * 256), ldstr(LP(aw) + (OFF) + (nt) * 256 + 1280))
#define DN_MM(nt, OFF, HF) do { acc[nt] = MFMA16(wf[nt], HF, acc[nt]); PIN(acc[nt]); SBAR(); DN_RW(nt, OFF); SBAR(); } while (0)
    STAMP(3, 0);
    DN_W(0); DN_W(1); DN_W(2);
    if (w == 0) wait_tags(flags, F_H + ((rb >> 1) * 2 + kh) * 32 + (lane & 7), tag);
    VM_BAR(0);
    STAMP(3, 1);
    DN_H(0, hf0); DN_H(1, hf1);
    unsigned aw = kq * 5120 + 2 * h * 1280 + q * 64 + (4 * (c & 1) + 2 * gi + (c >> 1)) * 8;
    f32x16 acc[5] = {};
#pragma unroll
    for (int nt = 0; nt < 5; ++nt) DN_RW(nt, 0);
#define DN_STEP(t, OFF, HF, HN, N0, N1, DH, DW) do { \
        LGKM0(); DN_WAIT(N0, N1, HF); \
        DN_MM(0, OFF, HF); if (DH) DN_H((t) + 2, HN); DN_MM(1, OFF, HF); DN_MM(2, OFF, HF); if (DW) DN_W((t) + 3); DN_MM(3, OFF, HF); DN_MM(4, OFF, HF); } while (0)
    __builtin_amdgcn_s_setprio(0);
    DN_STEP(0, 20480, hf0, hf2, 1, 1, true, true);
    DN_STEP(1, 40960, hf1, hf0, 4, 3, true, true);
    aw += 61440;
    DN_STEP(2, 0, hf2, hf1, 4, 3, true, true);
    aw -= 61440;
    DN_STEP(3, 0, hf0, hf2, 4, 3, true, true);
    DN_STEP(4, 20480, hf1, hf0, 4, 3, true, true);
    DN_STEP(5, 40960, hf2, hf1, 4, 3, true, true);
    aw += 61440;
    DN_STEP(6, 0, hf0, hf2, 4, 3, true, true);
    aw -= 61440;
    DN_STEP(7, 0, hf1, hf0, 4, 3, true, true);
    DN_STEP(8, 20480, hf2, hf1, 4, 3, true, true);
    DN_STEP(9, 40960, hf0, hf2, 4, 3, true, false);
    aw += 61440;
    DN_STEP(10, 0, hf1, hf0, 1, 1, false, false);
    asm volatile("s_waitcnt vmcnt(0)" : "+v"(hf2) :: "memory");
#pragma unroll
    for (int nt = 0; nt < 5; ++nt) acc[nt] = MFMA16(wf[nt], hf2, acc[nt]);
#undef DN_W
#undef DN_H
#undef DN_WAIT
#undef DN_RW
#undef DN_MM
#undef DN_STEP
    __builtin_amdgcn_s_setprio(PRIO_EPI);
    STAMP(3, 3);
#define AQ(Q) f32x4{acc[(Q) >> 2][4 * ((Q) & 3)], acc[(Q) >> 2][4 * ((Q) & 3) + 1], acc[(Q) >> 2][4 * ((Q) & 3) + 2], acc[(Q) >> 2][4 * ((Q) & 3) + 3]}
    LAS char* mine = LP(w * 10240 + lane * 16);
    LAS char* oth2 = LP((w ^ 4) * 10240 + lane * 16);
    LAS char* oth1 = LP((w ^ 6) * 10240 + lane * 16);
    f32x4 k1[10], k2[5];
    BAR_L();
    if (kq & 2) {
#pragma unroll
        for (int i = 0; i < 10; ++i) *(LAS f32x4*)(mine + i * 1024) = AQ(i);
        BAR_L();
#pragma unroll
        for (int i = 0; i < 10; ++i) k1[i] = add4(AQ(10 + i), *(LAS f32x4*)(oth2 + i * 1024));
    } else {
#pragma unroll
        for (int i = 0; i < 10; ++i) *(LAS f32x4*)(mine + i * 1024) = AQ(10 + i);
        BAR_L();
#pragma unroll
        for (int i = 0; i < 10; ++i) k1[i] = add4(AQ(i), *(LAS f32x4*)(oth2 + i * 1024));
    }
#undef AQ
    SBAR();
    const char* xp = x + (size_t)(rb * 64 + tt * 32 + r) * 1280 + cb * 320 + h * 32;
    u32x2 xv[5];
#pragma unroll
    for (int i = 0; i < 5; ++i) { const int Q = 5 * kq + i; xv[i] = *(const u32x2*)(xp + (Q >> 2) * 64 + (Q & 3) * 8); }
    const unsigned xm = kh == 0 ? 0xffffffffu : 0u;
    if (kq & 1) {
#pragma unroll
        for (int i = 0; i < 5; ++i) *(LAS f32x4*)(oth2 + i * 1024) = k1[i];
        BAR_L();
#pragma unroll
        for (int i = 0; i < 5; ++i) k2[i] = add4(k1[5 + i], *(LAS f32x4*)(oth1 + i * 1024));
    } else {
#pragma unroll
        for (int i = 0; i < 5; ++i) *(LAS f32x4*)(oth2 + i * 1024) = k1[5 + i];
        BAR_L();
#pragma unroll
        for (int i = 0; i < 5; ++i) k2[i] = add4(k1[i], *(LAS f32x4*)(oth1 + i * 1024));
    }
    const unsigned yp = WS_Y + (((kh * 64 + rb * 2 + tt) * 4 + cb) * 20 + 5 * kq) * 1024 + lane * 16;
#pragma unroll
    for (int i = 0; i < 5; ++i) {
        const unsigned x0 = xv[i][0] & xm, x1 = xv[i][1] & xm;
        wst128(rs, yp + i * 1024, __builtin_bit_cast(u32x4, add4(k2[i], f32x4{bflo(x0), bfhi(x0), bflo(x1), bfhi(x1)})));
    }
}

DI void out_role(const char* wo, char* ws, rsrc_t rs, gu32* flags, unsigned tag, char* out, int rb, int cb, int w) {
    const int lane = lane_id();
    const int t16 = w & 3, kh = w >> 2, r16 = lane & 15, q4 = lane >> 4;
    unsigned va[5];
#pragma unroll
    for (int ii = 0; ii < 5; ++ii) {
        const int P = ii * 64 + lane, rho = P / 10, c = P - rho * 10, u = rho >> 4, qq = (rho >> 2) & 3, e = rho & 3;
        const int row = w < 6 ? 20 * (qq >> 1) + 4 * (qq & 1) + 8 * u + e : 20 * (4 * (qq >> 1) + 2 * (qq & 1) + u) + 16 + e;
        va[ii] = row * 1280 + c * 16 + 4096 - ii * 1024;
    }
    const char* wb = wo + cb * 160 - 4096;
#define WO_STEP(src, slot) do { set_m0((slot) * 5120); const char* p_ = (src); GLDS(va[0], p_, 0); GLDS(va[1], p_, 1024); GLDS(va[2], p_, 2048); GLDS(va[3], p_, 3072); \
        set_m0((slot) * 5120 + 4096); GLDS(va[4], p_ + 4096, 0); } while (0)
    STAMP(4, 0);
    if (w < 4) WO_STEP(wb + ((w >> 1) * 10 + (w & 1)) * 51200, w);
    VM_BAR(0);
    const unsigned aw = (4 * q4 + ((lane >> 2) & 3)) * 160 + (lane & 3) * 40;
    bf16x8 wr[2][5];
#pragma unroll
    for (int s = 0; s < 2; ++s)
#pragma unroll
        for (int nt = 0; nt < 5; ++nt) wr[s][nt] = cat(ldstr(LP(aw + kh * 10240) + s * 5120 + nt * 8), ldstr(LP(aw + kh * 10240) + s * 5120 + nt * 8 + 2560));
    BAR_L();
    {
        const int sg = kh * 10 + 2 + 2 * t16;
        const char* p0 = w < 6 ? wb + sg * 51200 : wb + (sg - 16) * 204800;
        WO_STEP(p0, 2 * w); WO_STEP(p0 + (w < 6 ? 51200 : 204800), 2 * w + 1);
    }
#undef WO_STEP
    if (w == 0) wait_tags(flags, F_Y + (rb * 4 + (cb >> 1)) * 32 + (lane & 1), tag);
    BAR();
    f32x4 acc[5];
#pragma unroll
    for (int nt = 0; nt < 5; ++nt) {
        const int c = (cb & 1) * 80 + 20 * q4 + 4 * nt;
        acc[nt] = __builtin_bit_cast(f32x4, wld128(rs, WS_Y + (((kh * 64 + rb * 2 + (t16 >> 1)) * 4 + (cb >> 1)) * 20 + (c >> 5) * 4 + ((c >> 2) & 3)) * 1024
                                                       + (((c >> 4) & 1) * 32 + (t16 & 1) * 16 + r16) * 16));
    }
    STAMP(4, 1);
    if (w == 0) wait_tags(flags, F_O + rb * 32 + (lane & 31), tag);
    VM_BAR(0);
    STAMP(4, 2);
    const unsigned op = WS_O + (rb * 2 + (t16 >> 1)) * 40960;
    const int rr = (t16 & 1) * 16 + r16;
    bf16x8 of[10];
#pragma unroll
    for (int s = 0; s < 6; ++s) of[s] = __builtin_bit_cast(bf16x8, wld128(rs, op + (kh * 20 + 2 * s + (q4 >> 1)) * 1024 + (q4 & 1) * 512 + rr * 16));
    if (kh == 0) {
#pragma unroll
        for (int s = 6; s < 10; ++s) of[s] = __builtin_bit_cast(bf16x8, wld128(rs, op + (2 * s + (q4 >> 1)) * 1024 + (q4 & 1) * 512 + rr * 16));
    } else {
#pragma unroll
        for (int s = 6; s < 10; ++s) {
            const unsigned a = op + 32768 + (8 * (s - 6) + 2 * q4) * 256 + rr * 8;
            of[s] = cat(__builtin_bit_cast(bf16x4, wld64(rs, a)), __builtin_bit_cast(bf16x4, wld64(rs, a + 256)));
        }
    }
#pragma unroll
    for (int s = 0; s < 2; ++s)
#pragma unroll
        for (int nt = 0; nt < 5; ++nt) acc[nt] = MFMA16X16(wr[s][nt], of[s], acc[nt]);
#pragma unroll
    for (int s = 2; s < 10; ++s)
#pragma unroll
        for (int nt = 0; nt < 5; ++nt) {
            const bf16x8 wf = cat(ldstr(LP(aw + kh * 40960) + (s - 2) * 5120 + nt * 8), ldstr(LP(aw + kh * 40960) + (s - 2) * 5120 + nt * 8 + 2560));
            acc[nt] = MFMA16X16(wf, of[s], acc[nt]);
        }
    STAMP(4, 3);
    BAR_L();
    if (kh == 1) {
#pragma unroll
        for (int nt = 0; nt < 5; ++nt) *(LAS f32x4*)LP((t16 * 5 + nt) * 1024 + lane * 16) = acc[nt];
    }
    BAR_L();
    if (kh == 0) {
        char* og = out + (size_t)(rb * 64 + t16 * 16 + r16) * 1280 + cb * 160 + q4 * 40;
#pragma unroll
        for (int nt = 0; nt < 5; ++nt) {
            const f32x4 t = acc[nt] + *(LAS f32x4*)LP((t16 * 5 + nt) * 1024 + lane * 16);
            *(u32x2*)(og + nt * 8) = u32x2{pk(t[0], t[1]), pk(t[2], t[3])};
        }
    }
}

extern "C" __global__ __launch_bounds__(512, 4) void k_block(const char* __restrict__ x, const char* __restrict__ wqkv, const char* __restrict__ wo,
                                                             const char* __restrict__ wg, const char* __restrict__ wu, const char* __restrict__ wd,
                                                             char* __restrict__ ws, char* __restrict__ out) {
    extern __shared__ __attribute__((aligned(16))) char smem[];
    const int b = blockIdx.x & 255, j = b & 7, i = b >> 3;
    const int w = __builtin_amdgcn_readfirstlane(threadIdx.x >> 6);
    const rsrc_t rs = __builtin_amdgcn_make_buffer_rsrc(ws, 0, WS_END, 0x00020000);
    gu32* flags = (gu32*)(ws + WS_FLAG);
    const unsigned gen = __hip_atomic_load(flags + F_GEN, __ATOMIC_RELAXED, __HIP_MEMORY_SCOPE_AGENT);
    if (blockIdx.x < 256) {
        STAMP(0, 0);
        __builtin_amdgcn_s_setprio(PRIO_A0);
        const int rb = 4 * (j >> 1) + (i & 3), cb = (j & 1) * 8 + (i >> 2);
        unsigned tag = 0;
        if (w >= 4) { if (cb < 15) qkv_loader(x, wqkv, rs, gen, rb, cb, w - 4); }
        else {
            tag = TAG_OF(gen);
            if (cb < 10) qkv_tile<false>(ws, rs, rb, cb, w);
            else if (cb < 15) qkv_tile<true>(ws, rs, rb, cb, w);
        }
        STAMP(0, 3);
        DRAIN_BAR();
        const int hd = j * 4 + (i >> 3), qb = i & 7;
        if (w == 0) {
            const int lane = lane_id();
            const int ty = cb / 5, cl = cb - ty * 5, ph = lane & 31, hi = lane >> 5;
            const int g = (ph >> 1) * 5 + (ph & 1) * 2 + 2 * hi;
            const int blk = ty == 2 ? (ph * 20 + 19 * hi) >> 7 : g >> 4;
            publish(flags, F_QKV + ph * 128 + ((ty == 0 ? 4 : ty == 1 ? 0 : 2) + hi) * 16 + rb, ty < 3 && blk == cl, tag);
            publish(flags, F_ACK + blockIdx.x, lane == 0, tag);
            STAMP(0, 4);
            wait_tags2(flags, F_QKV + hd * 128 + lane, F_QKV + hd * 128 + 64 + (lane & 1) * 16 + qb * 2 + ((lane >> 1) & 1), tag);
        }
        BAR();
        STAMP(2, 0);
        attn_role(ws, rs, hd, qb, w);
        DRAIN_BAR();
        if (w == 0) { const int lane = lane_id(); publish(flags, F_O + (qb * 4 + lane) * 32 + hd, lane < 4, tag); }
        STAMP(2, 4);
    } else {
        STAMP(5, 0);
        __builtin_amdgcn_s_setprio(PRIO_B0);
        const int rb = 4 * (j >> 1) + (i & 3), fb = (j & 1) * 8 + (i >> 2), n = rb * 16 + fb;
        const unsigned tag = ln_role(x, wqkv, rs, gen, n, w);
        DRAIN_BAR();
        if (w == 0) {
            const int lane = lane_id();
            publish(flags, lane == 0 ? F_LN + rb * 32 + fb : F_ACK + blockIdx.x, lane < 2, tag);
            STAMP(5, 3);
        }
        gu_role(wg, wu, ws, rs, flags, tag, rb, fb, w);
        DRAIN_BAR();
        if (w == 0) publish(flags, F_H + (rb * 2 + (fb >> 3)) * 32 + (fb & 7), lane_id() == 0, tag);
        STAMP(1, 4);
        const int rb64 = (j >> 1) * 8 + (i & 7);
        down_role(x, wd, ws, rs, flags, tag, rb64, i >> 3, j & 1, w);
        DRAIN_BAR();
        if (w == 0) publish(flags, F_Y + (rb64 * 4 + (i >> 3)) * 32 + (j & 1), lane_id() == 0, tag);
        if (b == 0 && w == 0) {
            const int lane = lane_id();
#pragma unroll 1
            for (int k = 0; k < 8; ++k) wait_tags(flags, F_ACK + k * 64 + lane, tag);
            publish(flags, F_GEN, lane == 0, tag);
        }
        STAMP(3, 4);
        out_role(wo, ws, rs, flags, tag, out, j * 4 + (i & 3), i >> 2, w);
        STAMP(4, 4);
    }
}

extern "C" void kernel_launch(void* const* d_in, const int* in_sizes, int n_in, void* d_out, int out_size, void* d_ws, size_t ws_size,
                              hipStream_t stream) {
    (void)hipFuncSetAttribute((const void*)k_block, hipFuncAttributeMaxDynamicSharedMemorySize, 81920);
    k_block<<<512, 512, 81920, stream>>>((const char*)d_in[0], (const char*)d_in[1], (const char*)d_in[2], (const char*)d_in[4], (const char*)d_in[5],
                                         (const char*)d_in[6], (char*)d_ws, (char*)d_out);
}
```

```cpp
#include <hip/hip_runtime.h>
#include <hip/hip_bf16.h>
#include <stdint.h>

typedef __attribute__((ext_vector_type(8))) short bf16x8;
typedef __attribute__((ext_vector_type(4))) short bf16x4;
typedef __attribute__((ext_vector_type(16))) float f32x16;
typedef __attribute__((ext_vector_type(4))) float f32x4;
typedef __attribute__((ext_vector_type(2))) float f32x2;
typedef __attribute__((ext_vector_type(4))) unsigned u32x4;
typedef __attribute__((ext_vector_type(2))) unsigned u32x2;
typedef __attribute__((ext_vector_type(2))) __bf16 bf2;
typedef __attribute__((address_space(1))) unsigned gu32;
typedef __amdgpu_buffer_rsrc_t rsrc_t;
#define LAS __attribute__((address_space(3)))
#define DI __device__ __forceinline__

constexpr unsigned WS_Q = 0;
constexpr unsigned WS_KV = WS_Q + 32 * 64 * 1536;
constexpr unsigned WS_H = WS_KV + 32 * 64 * 3072;
constexpr unsigned WS_O = WS_H + 64 * 96 * 1024;
constexpr unsigned WS_ST = WS_O + 64 * 40 * 1024;
constexpr unsigned WS_CQ = WS_ST + 16 * 10 * 128 * 16;
constexpr unsigned WS_CG = WS_CQ + 16 * 10 * 128 * 8;
constexpr unsigned WS_FLAG = WS_CG + 16 * 10 * 192 * 8;
constexpr unsigned WS_END = WS_FLAG + 65536;
constexpr unsigned OOB = 0x7fffff00u;
constexpr int F_GEN = 0;
constexpr int F_ACK = 64;
constexpr int F_H = 1024;
constexpr int F_O = 3072;
constexpr int F_QKV = 8192;
constexpr size_t WS_DBG = 64u << 20;
#define STAMP(kid, i) do {} while (0)

#define MFMA16(a, b, c) __builtin_amdgcn_mfma_f32_32x32x16_bf16((a), (b), (c), 0, 0, 0)
#define MFMA8(a, b, c) __builtin_amdgcn_mfma_f32_32x32x8bf16_1k((a), (b), (c), 0, 0, 0)
#define MFMA16X16(a, b, c) __builtin_amdgcn_mfma_f32_16x16x32_bf16((a), (b), (c), 0, 0, 0)
#define LP(a) ((LAS char*)(uintptr_t)(a))
#define SBAR() __builtin_amdgcn_sched_barrier(0)
#define PIN(x) asm volatile("" : "+v"(x))
#define LGKM0() __builtin_amdgcn_s_waitcnt(0xC07F)
#define BAR() asm volatile("s_barrier" ::: "memory")
#define BAR_L() do { asm volatile("" ::: "memory"); LGKM0(); BAR(); } while (0)
#define VM_BAR(N) asm volatile("s_waitcnt vmcnt(" #N ")\n\ts_barrier" ::: "memory")
#define DRAIN_BAR() asm volatile("s_waitcnt vmcnt(0) lgkmcnt(0)\n\ts_barrier" ::: "memory")
#define VM_BAR2(sel, NA, NB) asm volatile("s_cmp_eq_u32 %0, 0\n\ts_cbranch_scc1 1f\n\ts_waitcnt vmcnt(" #NB ")\n1:\n\ts_waitcnt vmcnt(" #NA ")\n\ts_barrier" :: "s"((int)(sel)) : "memory", "scc")

DI void set_m0(unsigned lds) { asm volatile("s_mov_b32 m0, %0\n\ts_nop 0" :: "s"(lds) : "memory"); }
#define GLDS(voff, sbase, OFF) asm volatile("global_load_lds_dwordx4 %0, %1 offset:" #OFF :: "v"(voff), "s"(sbase) : "memory")
#define GLDS_SC1(voff, sbase, OFF) asm volatile("global_load_lds_dwordx4 %0, %1 offset:" #OFF " sc1" :: "v"(voff), "s"(sbase) : "memory")

DI int lane_id() { int l; asm volatile("v_mbcnt_lo_u32_b32 %0, -1, 0\n\tv_mbcnt_hi_u32_b32 %0, -1, %0" : "=v"(l)); return l; }
DI bf16x8 lds128(LAS char* p) { return *(LAS bf16x8*)p; }
DI bf16x4 lds64(LAS char* p) { return *(LAS bf16x4*)p; }
DI bf16x4 ldstr(LAS char* p) { return __builtin_amdgcn_ds_read_tr16_b64_v4i16((LAS bf16x4*)p); }
DI bf16x8 cat(bf16x4 a, bf16x4 b) { return __builtin_shufflevector(a, b, 0, 1, 2, 3, 4, 5, 6, 7); }
DI unsigned pk(float a, float b) { f32x2 f = {a, b}; return __builtin_bit_cast(unsigned, __builtin_convertvector(f, bf2)); }
DI float bflo(unsigned u) { return __uint_as_float(u << 16); }
DI float bfhi(unsigned u) { return __uint_as_float(u & 0xffff0000u); }
DI float dot2(unsigned a, unsigned b, float c) { return __builtin_amdgcn_fdot2_f32_bf16(__builtin_bit_cast(bf2, a), __builtin_bit_cast(bf2, b), c, false); }
DI float swap32_add(float v) { auto r = __builtin_amdgcn_permlane32_swap(__float_as_uint(v), __float_as_uint(v), false, false); return __uint_as_float(r[0]) + __uint_as_float(r[1]); }
DI float swap32_max(float v) { auto r = __builtin_amdgcn_permlane32_swap(__float_as_uint(v), __float_as_uint(v), false, false); return __builtin_fmaxf(__uint_as_float(r[0]), __uint_as_float(r[1])); }
DI float addf(float a, float b) { float r; asm("v_add_f32 %0, %1, %2" : "=v"(r) : "v"(a), "v"(b)); return r; }
DI float mulf(float a, float b) { float r; asm("v_mul_f32 %0, %1, %2" : "=v"(r) : "v"(a), "v"(b)); return r; }
DI float relu(float a) { float r; asm("v_max_f32 %0, 0, %1" : "=v"(r) : "v"(a)); return r; }
DI f32x4 add4(f32x4 a, f32x4 b) { return f32x4{addf(a[0], b[0]), addf(a[1], b[1]), addf(a[2], b[2]), addf(a[3], b[3])}; }
DI void wst128(rsrc_t rs, unsigned off, u32x4 v) { __builtin_amdgcn_raw_buffer_store_b128(v, rs, off, 0, 16); }
DI void wst64(rsrc_t rs, unsigned off, u32x2 v) { __builtin_amdgcn_raw_buffer_store_b64(v, rs, off, 0, 16); }
DI void wst32(rsrc_t rs, unsigned off, unsigned v) { __builtin_amdgcn_raw_buffer_store_b32(v, rs, off, 0, 16); }
DI u32x4 wld128(rsrc_t rs, unsigned off) { return __builtin_amdgcn_raw_buffer_load_b128(rs, off, 0, 16); }
DI u32x2 wld64(rsrc_t rs, unsigned off) { return __builtin_amdgcn_raw_buffer_load_b64(rs, off, 0, 16); }
DI unsigned wld32(rsrc_t rs, unsigned off) { return __builtin_amdgcn_raw_buffer_load_b32(rs, off, 0, 16); }
#define POLL_SLEEP 4
DI void wait_tags(gu32* flags, int word, unsigned tag) {
    for (int spin = 0; spin < (1 << 18); ++spin) {
        const unsigned v = __hip_atomic_load(flags + word, __ATOMIC_RELAXED, __HIP_MEMORY_SCOPE_AGENT);
        if (__all(v == tag)) break;
        __builtin_amdgcn_s_sleep(POLL_SLEEP);
    }
    asm volatile("" ::: "memory");
}
DI void wait_tags2(gu32* flags, int word0, int word1, unsigned tag) {
    for (int spin = 0; spin < (1 << 18); ++spin) {
        const unsigned v0 = __hip_atomic_load(flags + word0, __ATOMIC_RELAXED, __HIP_MEMORY_SCOPE_AGENT);
        const unsigned v1 = __hip_atomic_load(flags + word1, __ATOMIC_RELAXED, __HIP_MEMORY_SCOPE_AGENT);
        if (__all(v0 == tag && v1 == tag)) break;
        __builtin_amdgcn_s_sleep(POLL_SLEEP);
    }
    asm volatile("" ::: "memory");
}
DI void publish(gu32* flags, int word, bool on, unsigned tag) { if (on) __hip_atomic_store(flags + word, tag, __ATOMIC_RELAXED, __HIP_MEMORY_SCOPE_AGENT); }
#define TAG_OF(gen) ((unsigned)__builtin_amdgcn_readfirstlane(gen) + 1u)

DI float max16(const f32x16& s) {
    float a = __builtin_fmaxf(__builtin_fmaxf(s[0], s[1]), s[2]), b = __builtin_fmaxf(__builtin_fmaxf(s[3], s[4]), s[5]);
    a = __builtin_fmaxf(__builtin_fmaxf(a, s[6]), s[7]); b = __builtin_fmaxf(__builtin_fmaxf(b, s[8]), s[9]);
    a = __builtin_fmaxf(__builtin_fmaxf(a, s[10]), s[11]); b = __builtin_fmaxf(__builtin_fmaxf(b, s[12]), s[13]);
    a = __builtin_fmaxf(__builtin_fmaxf(a, s[14]), s[15]);
    return swap32_max(__builtin_fmaxf(a, b));
}

template <int I> struct IC { static constexpr int v = I; };
template <int I, int N, class F> DI void sfor(F&& f) { if constexpr (I < N) { f(IC<I>{}); sfor<I + 1, N>(f); } }
#define SFOR(var, N) sfor<0, N>([&](auto var##_) __attribute__((always_inline)) { constexpr int var = decltype(var##_)::v;
#define SEND });

constexpr unsigned SCR = 131072;
constexpr unsigned KVB = 65536;
DI constexpr unsigned gu_slot(int g) { return g % 5 == 0 ? 139264u : g % 5 == 1 ? 65536u : g % 5 == 2 ? 90112u : g % 5 == 3 ? 0u : 24576u; }
DI constexpr unsigned po_slot(int r) {
    return r >= 9 ? po_slot(r - 7) : r < 3 ? r * 20480u : r == 3 ? 139264u : r < 6 ? (r - 4) * 20480u : r == 6 ? KVB + 49152u : KVB + (r - 7) * 24576u;
}

DI int kstage(int kt, int rk) { const int k = kt + rk; return k >= 10 ? k - 10 : k; }
DI unsigned gu_voff(int lane, int p, int up) { const int P = 64 * p + lane, row = P / 12, c = P - row * 12; return row * 3072 + ((up ? c ^ 2 : c) << 4); }

#define X_ADDR() unsigned ax[4]; _Pragma("unroll") for (int p = 0; p < 4; ++p) ax[p] = (xrow >> 4) * 4096 + (xrow & 15) * 128 + (((2 * p + h) ^ ((xrow >> 1) & 7)) << 4)
DI void qkv_loader(const char* x, const char* wqkv, const char* wg, const char* wu, char* ws, rsrc_t rs, int rk, int rb, int cb, int tt, bf16x8 (&xf)[40],
                   float& ra, float& rm, unsigned gen, unsigned& tag) {
    const int lane = lane_id(), r = lane & 31, h = lane >> 5, xrow = tt * 32 + r;
    const int cbl = cb < 15 ? cb : 14;
    const unsigned vA0 = (lane >> 3) * 1280 + (((lane & 7) ^ (lane >> 4)) << 4), vA1 = (vA0 ^ 64) + 10240 - 1024;
    const unsigned vW0 = ((lane >> 3) & 3) * 3840 + h * 128 + (((lane & 7) ^ ((lane >> 2) & 4)) << 4), vW1 = vW0 + 15360 - 1024;
    const char* xa = x + (size_t)(rb * 128 + tt * 32) * 1280;
    const char* wb = wqkv + (size_t)tt * 16 * 3840 + cbl * 256 - 2048;
#define Q_STAGE(kt) do { const int ka_ = kstage(kt, rk); const char *px_ = xa + ka_ * 128, *pw_ = wb + ka_ * 245760; \
        set_m0(((kt) & 3) * 32768 + tt * 8192); GLDS(vA0, px_, 0); GLDS(vW0, pw_, 2048); GLDS(vA1, px_, 1024); GLDS(vW1, pw_, 3072); \
        set_m0(((kt) & 3) * 32768 + tt * 8192 + 4096); GLDS(vA0, px_ + 20480, 0); GLDS(vW0, pw_ + 30720, 2048); GLDS(vA1, px_ + 20480, 1024); GLDS(vW1, pw_ + 30720, 3072); } while (0)
    Q_STAGE(0); Q_STAGE(1);
    SBAR();
    unsigned gv[3];
#pragma unroll
    for (int p = 0; p < 3; ++p) gv[p] = gu_voff(lane, p, 0) + 2048 - p * 1024;
    const size_t go_ = (size_t)tt * 49152 + cb * 192 - 2048;
#define G_STAGE(g) do { const size_t o_ = go_ + (size_t)kstage(g, rk) * 196608; \
        set_m0(gu_slot(g) + tt * 6144); GLDS(gv[0], wg + o_, 0); GLDS(gv[1], wg + o_, 1024); GLDS(gv[2], wg + o_, 2048); \
        set_m0(gu_slot(g) + tt * 6144 + 3072); GLDS(gv[0] ^ 32, wu + o_, 0); GLDS(gv[1] ^ 32, wu + o_, 1024); GLDS(gv[2] ^ 32, wu + o_, 2048); } while (0)
    X_ADDR();
    VM_BAR(8);
    asm volatile("v_readfirstlane_b32 %0, %1" : "=s"(tag) : "v"(gen));
    tag += 1;
    Q_STAGE(2);
    {
        const int c4 = lane & 3, cq = cb >= 10 ? c4 : (((c4 & 1) << 1) | (c4 >> 1));
        const unsigned aw = h * 4096 + 2048 + (tt >> 1) * 512 + ((lane >> 2) & 3) * 128 + (((tt ^ (lane >> 3)) & 1) << 6) + ((lane >> 4) & 1) * 32 + cq * 8;
        float sx = 0.f, sq = 0.f, c0 = 0.f;
#pragma unroll
        for (int p = 0; p < 4; ++p) {
            xf[p] = lds128(LP(ax[p]));
            const u32x4 wv = __builtin_bit_cast(u32x4, cat(ldstr(LP(aw) + p * 8192), ldstr(LP(aw) + p * 8192 + 1024)));
#pragma unroll
            for (int e = 0; e < 4; ++e) {
                const unsigned u = __builtin_bit_cast(u32x4, xf[p])[e]; const float lo = bflo(u), hi = bfhi(u);
                sx += lo + hi; sq = __builtin_fmaf(lo, lo, __builtin_fmaf(hi, hi, sq)); c0 += bflo(wv[e]) + bfhi(wv[e]);
            }
        }
        sx = swap32_add(sx); sq = swap32_add(sq); c0 = swap32_add(c0);
        wst128(rs, h == 0 ? WS_ST + ((rb * 10 + rk) * 128 + xrow) * 16 : OOB, u32x4{__float_as_uint(sx), tag, __float_as_uint(sq), tag});
        wst64(rs, (h == 0 && cb < 15) ? WS_CQ + ((cb * 10 + rk) * 128 + xrow) * 8 : OOB, u32x2{__float_as_uint(c0), tag});
        const int hd = ((cb - 10) * 128 + 19) / 20 + (lane >> 3);
        const unsigned v = (lane & 1) ? 0u : 0x3F803F80u;
        wst128(rs, (cb >= 10 && cb < 15 && hd * 20 < (cb - 9) * 128) ? WS_KV + (hd * 64 + rb * 4 + tt) * 3072 + 1536 + ((lane >> 1) & 3) * 384 + 320 + (lane & 1) * 16 : OOB, u32x4{v, v, v, v});
        SBAR();
    }
    u32x4 gs[5]; u32x2 gc[5];
    const unsigned so = WS_ST + ((rb * 10 + h * 5) * 128 + xrow) * 16, co = WS_CQ + ((cbl * 10 + h * 5) * 128 + xrow) * 8;
    SFOR(kt, 10)
        LGKM0();
        if (kt == 0) VM_BAR(11); else if (kt == 5) VM_BAR(14); else if (kt == 6) VM_BAR(18); else if (kt < 8) VM_BAR(8); else if (kt == 8) VM_BAR(6); else BAR();
        if constexpr (kt < 9) {
            if (kt & 1) {
#pragma unroll
                for (int p = 0; p < 4; ++p) ax[p] ^= 65536;
            }
#pragma unroll
            for (int p = 0; p < 4; ++p) xf[4 * kt + 4 + p] = lds128(LP(ax[p]) + ((kt & 1) ? 0 : 32768));
        }
        if constexpr (kt < 7) Q_STAGE(kt + 3); else G_STAGE(kt - 6);
        if constexpr (kt == 4) G_STAGE(0);
        if constexpr (kt == 5) {
#pragma unroll
            for (int k = 0; k < 5; ++k) {
                asm volatile("global_load_dwordx4 %0, %1, %2 sc1" : "=v"(gs[k]) : "v"(so), "s"(ws + k * 2048) : "memory");
                asm volatile("global_load_dwordx2 %0, %1, %2 sc1" : "=v"(gc[k]) : "v"(co), "s"(ws + k * 1024) : "memory");
            }
        }
        if constexpr (kt == 7) {
            float s0 = 0.f, s1 = 0.f, s2 = 0.f;
            bool ok = true;
#pragma unroll
            for (int k = 0; k < 5; ++k) {
                PIN(gs[k]); PIN(gc[k]);
                s0 += __uint_as_float(gs[k][0]); s1 += __uint_as_float(gs[k][2]); s2 += __uint_as_float(gc[k][0]);
                ok = ok && gs[k][1] == tag && gs[k][3] == tag && gc[k][1] == tag;
            }
            for (int spin = 0; spin < (1 << 18) && __builtin_expect(!__all(ok), 0); ++spin) {
                __builtin_amdgcn_s_sleep(POLL_SLEEP);
                s0 = 0.f; s1 = 0.f; s2 = 0.f; ok = true;
#pragma unroll 1
                for (int k = 0; k < 5; ++k) {
                    const u32x4 g = wld128(rs, so + k * 2048); const u32x2 c = wld64(rs, co + k * 1024);
                    s0 += __uint_as_float(g[0]); s1 += __uint_as_float(g[2]); s2 += __uint_as_float(c[0]); ok = ok && g[1] == tag && g[3] == tag && c[1] == tag;
                }
            }
            const float mu = swap32_add(s0) * (1.f / 640.f);
            ra = __builtin_amdgcn_rsqf(__builtin_fmaxf(__builtin_fmaf(-mu, mu, swap32_add(s1) * (1.f / 640.f)), 0.f) + 1e-5f);
            rm = -mu;
            *(LAS float*)LP(SCR + h * 512 + xrow * 4) = h ? rm : ra;
            *(LAS float*)LP(SCR + 1024 + xrow * 4) = swap32_add(s2);
        }
    SEND
    VM_BAR(12);
    G_STAGE(4);
#undef Q_STAGE
#undef G_STAGE
}

template <bool VT>
DI void qkv_compute(char* ws, rsrc_t rs, int rb, int cb, int w, bf16x8 (&xf)[40], float& ra, float& rm, unsigned gen, unsigned& tag) {
    const int lane = lane_id(), tt = w, r = lane & 31, h = lane >> 5, xrow = tt * 32 + r, tile = rb * 4 + tt;
    const int c4 = lane & 3, cq = VT ? c4 : (((c4 & 1) << 1) | (c4 >> 1));
    unsigned aw = h * 4096 + 2048 + ((lane >> 2) & 3) * 128 + (((lane >> 3) & 1) << 6) + ((lane >> 4) & 1) * 32 + cq * 8, aw1 = aw ^ 64;
    X_ADDR();
    f32x16 acc[4] = {};
    bf16x8 wf[4];
#define Q_OFF(s) ((((s) >> 2) & 1) * 32768)
#define Q_RW(s, n) wf[n] = cat(ldstr(LP(((n) & 1) ? aw1 : aw) + Q_OFF(s) + ((s) & 3) * 8192 + ((n) >> 1) * 512), ldstr(LP(((n) & 1) ? aw1 : aw) + Q_OFF(s) + ((s) & 3) * 8192 + ((n) >> 1) * 512 + 1024))
    VM_BAR(0);
    asm volatile("v_readfirstlane_b32 %0, %1" : "=s"(tag) : "v"(gen));
    tag += 1;
    STAMP(0, 1);
    xf[0] = lds128(LP(ax[0]));
    Q_RW(0, 0); Q_RW(0, 1); Q_RW(0, 2); Q_RW(0, 3);
    SFOR(s, 40)
        if constexpr ((s & 3) == 3) {
            BAR();
            if constexpr (s == 11) STAMP(5, 1);
            if constexpr (s == 23) STAMP(5, 2);
            if constexpr (s == 31) STAMP(5, 3);
            if constexpr (s == 35) STAMP(5, 4);
            if constexpr ((s & 7) == 7) {
#pragma unroll
                for (int p = 0; p < 4; ++p) ax[p] ^= 65536;
                aw ^= 65536; aw1 ^= 65536;
            }
        }
        if constexpr (s < 39) xf[s + 1] = lds128(LP(ax[(s + 1) & 3]) + Q_OFF(s + 1));
        SBAR();
#pragma unroll
        for (int n = 0; n < 4; ++n) {
            acc[n] = VT ? MFMA16(xf[s], wf[n], acc[n]) : MFMA16(wf[n], xf[s], acc[n]); PIN(acc[n]); SBAR();
            if constexpr (s < 39) Q_RW(s + 1, n);
            SBAR();
        }
    SEND
#undef Q_OFF
#undef Q_RW
    BAR();
    STAMP(0, 2);
    const int ln = lane_id(), rr = ln & 31, hh = ln >> 5;
    ra = *(LAS float*)LP(SCR + (tt * 32 + rr) * 4); rm = *(LAS float*)LP(SCR + 512 + (tt * 32 + rr) * 4);
    if (!VT) {
        const float a = cb < 5 ? ra * (0.22360679774997896f * 1.4426950408889634f) : ra;
        const unsigned rec = cb < 5 ? 1536 : 3072;
#pragma unroll
        for (int k = 0; k < 8; ++k) {
            unsigned o2[2];
#pragma unroll
            for (int u = 0; u < 2; ++u) {
                const int Gc = (cb % 5) * 16 + k * 2 + u, P = Gc / 5, gp = Gc - P * 5;
                o2[u] = (cb < 5 ? WS_Q : WS_KV) + tile * rec + (2 * P + (gp > 2)) * 64 * rec + (gp > 2 ? gp - 3 : gp) * 512 + (gp == 2);
            }
            const unsigned off = (hh ? o2[1] : o2[0]) + rr * 16;
            const f32x4 e0 = *(LAS f32x4*)LP(SCR + 1024 + (k * 16 + 4 * hh) * 4), e1 = *(LAS f32x4*)LP(SCR + 1024 + (k * 16 + 8 + 4 * hh) * 4);
            const f32x16& c = acc[k >> 1]; const int o = 8 * (k & 1);
            u32x4 ov;
#pragma unroll
            for (int e = 0; e < 2; ++e) {
                ov[e] = pk(mulf(__builtin_fmaf(e0[2 * e], rm, c[o + 2 * e]), a), mulf(__builtin_fmaf(e0[2 * e + 1], rm, c[o + 2 * e + 1]), a));
                ov[2 + e] = pk(mulf(__builtin_fmaf(e1[2 * e], rm, c[o + 4 + 2 * e]), a), mulf(__builtin_fmaf(e1[2 * e + 1], rm, c[o + 4 + 2 * e + 1]), a));
            }
            wst128(rs, off & ~1u, ov);
            if ((o2[0] | o2[1]) & 1) wst128(rs, (off & 1) ? off - 1 + 64 * rec : OOB, ov);
        }
    } else {
#pragma unroll
        for (int n = 0; n < 4; ++n) {
            const int col = (cb - 10) * 128 + n * 32 + rr, hd = col / 20, d = col - hd * 20;
            const unsigned off = cb < 15 ? WS_KV + (hd * 64 + tile) * 3072 + 1536 + hh * 384 + d * 16 : OOB;
            const float cn = *(LAS float*)LP(SCR + 1024 + (n * 32 + rr) * 4);
#pragma unroll
            for (int sb = 0; sb < 2; ++sb) {
                u32x4 ov;
#pragma unroll
                for (int gg = 0; gg < 2; ++gg) {
                    const int g = 2 * sb + gg;
                    const f32x4 a = *(LAS f32x4*)LP(SCR + (tt * 32 + 8 * g + 4 * hh) * 4), b = *(LAS f32x4*)LP(SCR + 512 + (tt * 32 + 8 * g + 4 * hh) * 4);
                    const f32x16& c = acc[n];
                    ov[2 * gg] = pk(mulf(__builtin_fmaf(b[0], cn, c[4 * g]), a[0]), mulf(__builtin_fmaf(b[1], cn, c[4 * g + 1]), a[1]));
                    ov[2 * gg + 1] = pk(mulf(__builtin_fmaf(b[2], cn, c[4 * g + 2]), a[2]), mulf(__builtin_fmaf(b[3], cn, c[4 * g + 3]), a[3]));
                }
                wst128(rs, off + sb * 768, ov);
            }
        }
    }
}
#undef X_ADDR

extern "C" __global__ __launch_bounds__(512, 2) void k_block(const char* __restrict__ x, const char* __restrict__ wqkv, const char* __restrict__ wo,
                                                             const char* __restrict__ wg, const char* __restrict__ wu, const char* __restrict__ wd,
                                                             char* __restrict__ ws, char* __restrict__ out) {
    extern __shared__ __attribute__((aligned(16))) char smem[];
    const int b = blockIdx.x, j = b & 7, i = b >> 3;
    const int w = __builtin_amdgcn_readfirstlane(threadIdx.x >> 6);
    const rsrc_t rs = __builtin_amdgcn_make_buffer_rsrc(ws, 0, WS_END, 0x00020000);
    gu32* flags = (gu32*)(ws + WS_FLAG);
    unsigned gen, tag;
    asm volatile("global_load_dword %0, %1, %2 sc1" : "=v"(gen) : "v"(0), "s"(flags + F_GEN) : "memory");
    const int rb = 4 * (j >> 1) + (i & 3), cb = (j & 1) * 8 + (i >> 2);
    const int hd = j * 4 + (i >> 3), qb = i & 7;
    const int rb64 = j * 4 + (i & 3), cb80 = i >> 2;
    STAMP(0, 0);
    bf16x8 xf[40];
    float ra, rm;
    const int rk = (((rb & 3) * 5 >> 1) + (cb & 7) + ((cb >> 2) & 1) + (j & 1) + (j >> 1)) % 10;
    if (w >= 4) qkv_loader(x, wqkv, wg, wu, ws, rs, rk, rb, cb, w - 4, xf, ra, rm, gen, tag);
    else if (cb < 10) qkv_compute<false>(ws, rs, rb, cb, w, xf, ra, rm, gen, tag);
    else qkv_compute<true>(ws, rs, rb, cb, w, xf, ra, rm, gen, tag);
    STAMP(0, 3);

    const char* kv = ws + WS_KV + (size_t)hd * 196608 + w * 3072;
#define KV_DMA(s, buf) do { set_m0((buf) + w * 3072); const char* p_ = kv + (s) * 24576; GLDS_SC1(voff, p_, 0); GLDS_SC1(voff, p_, 1024); GLDS_SC1(voff, p_, 2048); } while (0)
    bf16x8 q1; bf16x4 q2;
    {
        const int tt = w & 3, nh = w >> 2;
        unsigned awA, awB;
        {
            const int lane = lane_id(), q = (lane >> 2) & 3, c = lane & 3, gi = (lane >> 4) & 1, sw = ((c & 1) << 1) | (c >> 1);
            const unsigned ab = gi * 3072 + (lane >> 5) * 1536 + q * 192 + sw * 8;
            awA = ab + nh * 64 + ((nh ^ gi) << 5); awB = ab + nh * 128 + (((nh ^ 1) ^ gi) << 5);
        }
        unsigned gv[3];
        const char* gsrc = ((w & 1) ? wu : wg) + (size_t)(w >> 1) * 49152 + cb * 192;
        f32x16 acc[3] = {};
        float cl[3] = {0.f, 0.f, 0.f};
        bf16x8 wf[2][3];
        f32x4 e0[6], e1[6];
        u32x4 gs[5];
#define G_BASE(s) (gu_slot((s) >> 2) & ~65535u)
#define G_OFF(s) ((gu_slot((s) >> 2) & 65535u) + ((s) & 3) * 6144)
#define G_ADDR(s) do { if (((s) & 3) == 0 && (s) > 0 && G_BASE(s) != G_BASE((s) - 1)) { awA += G_BASE(s) - G_BASE((s) - 1); awB += G_BASE(s) - G_BASE((s) - 1); } } while (0)
#define G_RW(s, a) wf[(s) & 1][a] = (a) == 1 ? cat(ldstr(LP(awB) + G_OFF(s)), ldstr(LP(awB) + G_OFF(s) + 768)) : cat(ldstr(LP(awA) + G_OFF(s) + (a) * 32), ldstr(LP(awA) + G_OFF(s) + (a) * 32 + 768))
        awA += G_BASE(0); awB += G_BASE(0);
        STAMP(1, 0);
        G_RW(0, 0); G_RW(0, 1); G_RW(0, 2); G_RW(1, 0); G_RW(1, 1); G_RW(1, 2);
#define G_REQ(s) do { constexpr int k_ = ((s) - 2) >> 2, p_ = ((s) - 2) & 3; \
            if constexpr ((s) >= 6 && k_ <= 5 && p_ < 3) { set_m0(gu_slot(k_ + 4) + w * 3072 + p_ * 1024); GLDS(gv[p_ % 3], gsrc + kstage(k_ + 4, rk) * 196608, 0); } \
            if constexpr ((s) >= 34) { set_m0(KVB + ((s) - 34) / 3 * 24576 + ((s) - 34) % 3 * 1024 + w * 3072); GLDS_SC1(voff, kv + ((s) - 34) / 3 * 24576 + ((s) - 34) % 3 * 1024, 0); } } while (0)
        unsigned voff;
        SFOR(s, 40)
            constexpr int kt = s >> 2;
            if constexpr ((s & 3) == 2 && kt >= 1 && kt <= 8) {
                if constexpr (kt == 4) STAMP(3, 0);
                if constexpr (kt == 8) {
                    STAMP(3, 2);
                    const int lane = lane_id(), r = lane & 31;
                    if (w == 0) wait_tags2(flags, F_QKV + hd * 128 + lane, F_QKV + hd * 128 + 64 + (lane & 1) * 16 + qb * 2 + ((lane >> 1) & 1), tag);
                    else if (w < 4) {
                        const unsigned go = WS_CG + ((cb * 10 + (lane >> 5) * 5) * 192 + (w - 1) * 64 + 2 * r) * 8;
                        float s0 = 0.f, s1 = 0.f;
                        bool ok = true;
#pragma unroll
                        for (int k = 0; k < 5; ++k) { s0 += __uint_as_float(gs[k][0]); s1 += __uint_as_float(gs[k][2]); ok = ok && gs[k][1] == tag && gs[k][3] == tag; }
                        for (int spin = 0; spin < (1 << 18) && __builtin_expect(!__all(ok), 0); ++spin) {
                            __builtin_amdgcn_s_sleep(POLL_SLEEP);
                            s0 = 0.f; s1 = 0.f; ok = true;
#pragma unroll 1
                            for (int k = 0; k < 5; ++k) { const u32x4 g = wld128(rs, go + k * 1536); s0 += __uint_as_float(g[0]); s1 += __uint_as_float(g[2]); ok = ok && g[1] == tag && g[3] == tag; }
                        }
                        *(LAS f32x2*)LP(SCR + 2048 + ((w - 1) * 64 + 2 * r) * 4) = f32x2{swap32_add(s0), swap32_add(s1)};
                        LGKM0();
                    }
                }
                if (kt == 1) VM_BAR2(nh, 63, 12); else if (kt == 2) VM_BAR2(nh, 63, 9); else if (kt == 3) VM_BAR2(nh, 63, 6); else if (kt < 7) VM_BAR(6); else if (kt == 7) VM_BAR(3); else { VM_BAR(0); __builtin_amdgcn_s_waitcnt(0x0F70); }
                if constexpr (kt == 4) STAMP(3, 1);
                if constexpr (kt == 8) STAMP(3, 3);
                if constexpr (kt == 4) if (w == 0) {
                    const int lane = lane_id(), r = lane & 31, h = lane >> 5;
                    const int ty = cb / 5, cc = cb - ty * 5, g = (r >> 1) * 5 + (r & 1) * 2 + 2 * h;
                    const int blk = ty == 2 ? (r * 20 + 19 * h) >> 7 : g >> 4;
                    publish(flags, F_QKV + r * 128 + ((ty == 0 ? 4 : ty == 1 ? 0 : 2) + h) * 16 + rb, ty < 3 && blk == cc, tag);
                    publish(flags, F_ACK + b, lane == 0, tag);
                }
                if constexpr (kt == 7) if (w >= 1 && w < 4) {
                    const int lane = lane_id();
                    const unsigned go = WS_CG + ((cb * 10 + (lane >> 5) * 5) * 192 + (w - 1) * 64 + 2 * (lane & 31)) * 8;
#pragma unroll
                    for (int k = 0; k < 5; ++k) gs[k] = wld128(rs, go + k * 1536);
                }
                if constexpr (kt == 8) {
                    const int lane = lane_id();
                    const char* qp = ws + WS_Q + (hd * 64 + qb * 8 + w) * 1536;
                    voff = lane * 16;
                    asm volatile("global_load_dwordx4 %0, %1, %2 sc1" : "=v"(q1) : "v"(voff), "s"(qp) : "memory");
                    asm volatile("global_load_dwordx2 %0, %1, %2 offset:1024 sc1" : "=v"(q2) : "v"((lane & 31) * 16 + (hd & 1) * 8), "s"(qp) : "memory");
                }
                SBAR();
            }
            if constexpr (s == 35) STAMP(3, 4);
            if constexpr (s == 5) {
                const int lane = lane_id();
#pragma unroll
                for (int p = 0; p < 3; ++p) gv[p] = gu_voff(lane, p, w & 1);
            }
            G_ADDR(s + 2);
            if constexpr (s < 38) __builtin_amdgcn_s_waitcnt(0xC67F);
            if constexpr (s < 4) if (tt == 3) {
#pragma unroll
                for (int e = 0; e < 4; ++e)
#pragma unroll
                    for (int a = 0; a < 3; ++a) cl[a] = dot2(__builtin_bit_cast(u32x4, wf[s & 1][a])[e], 0x3F803F80u, cl[a]);
                if constexpr ((s & 3) == 3) {
                    const int lane = lane_id();
                    const unsigned o = WS_CG + ((cb * 10 + rk + kt) * 192 + nh * 96 + lane) * 8;
                    const float s0 = swap32_add(cl[0]), s1 = swap32_add(cl[1]), s2 = swap32_add(cl[2]);
                    wst64(rs, o, u32x2{__float_as_uint(lane < 32 ? s0 : s1), tag});
                    wst64(rs, lane < 32 ? o + 512 : OOB, u32x2{__float_as_uint(s2), tag});
                }
            }
            SBAR();
#pragma unroll
            for (int a = 0; a < 3; ++a) {
                acc[a] = MFMA16(wf[s & 1][a], xf[s], acc[a]); PIN(acc[a]); SBAR();
                if (s < 38) G_RW(s + 2, a);
                if constexpr (s == 38) {
                    const int h = lane_id() >> 5;
#pragma unroll
                    for (int t = 2 * a; t < 2 * a + 2; ++t) {
                        e0[t] = *(LAS f32x4*)LP(SCR + 2048 + (nh * 96 + a * 32 + 8 * (t & 1) + 4 * h) * 4); e1[t] = *(LAS f32x4*)LP(SCR + 2048 + (nh * 96 + a * 32 + 16 + 8 * (t & 1) + 4 * h) * 4);
                    }
                }
                if (a != 1 && nh == (a >> 1)) G_REQ(s);
                SBAR();
            }
        SEND
#undef G_REQ
#undef G_BASE
#undef G_OFF
#undef G_ADDR
#undef G_RW
        STAMP(1, 3);
        const int lane = lane_id();
        const float a2 = ra * ra;
        const unsigned hp = WS_H + ((rb * 4 + tt) * 96 + cb * 6 + 3 * nh) * 1024 + lane * 16;
#pragma unroll
        for (int a = 0; a < 3; ++a) {
            u32x4 o;
#pragma unroll
            for (int g = 0; g < 2; ++g) {
                const f32x4 cg = e0[2 * a + g], cu = e1[2 * a + g];
                const f32x16& c = acc[a];
                const f32x4 gv = f32x4{c[4 * g], c[4 * g + 1], c[4 * g + 2], c[4 * g + 3]} + cg * rm;
                const f32x4 uv = (f32x4{c[8 + 4 * g], c[8 + 4 * g + 1], c[8 + 4 * g + 2], c[8 + 4 * g + 3]} + cu * rm) * a2;
                o[2 * g] = pk(__builtin_fmaxf(gv[0], 0.f) * uv[0], __builtin_fmaxf(gv[1], 0.f) * uv[1]);
                o[2 * g + 1] = pk(__builtin_fmaxf(gv[2], 0.f) * uv[2], __builtin_fmaxf(gv[3], 0.f) * uv[3]);
            }
            if (a == 0) asm volatile("s_waitcnt vmcnt(3)" : "+v"(q1), "+v"(q2) :: "memory");
            wst128(rs, hp + a * 1024, o);
        }
    }
    STAMP(1, 4);

    bf16x8 hf[12][2], pf[2][5];
    unsigned paw;
    unsigned pv[3];
    const char* pw;
    const int ptt = w & 1, pkq = w >> 1;
#define PO_DMA(rnd, src) do { set_m0(po_slot(rnd) + pkq * 5120 + ptt * 3072); const char* p_ = (src); GLDS(pv[0], p_, 0); GLDS(pv[1], p_, 1024); if (ptt == 0) GLDS(pv[2], p_, 2048); } while (0)
#define PO_VOFF(ROW) do { const int lane_ = lane_id(); _Pragma("unroll") for (int ii = 0; ii < 3; ++ii) { \
        const int P = (ptt * 3 + ii) * 64 + lane_, rho = (P / 10) & 31, c = P % 10, u = rho >> 4, qq = (rho >> 2) & 3, e = rho & 3; \
        pv[ii] = (ROW) * 1280 + c * 16 + 2048 - ii * 1024; } } while (0)
    PO_VOFF(8 * qq + 4 * u + e);
    pw = wd + (size_t)pkq * 491520 + cb80 * 160 - 2048;
    const int prot = 3 * (rb64 & 3);
#define PO_K(rnd) ((rnd) + prot >= 12 ? (rnd) + prot - 12 : (rnd) + prot)
    f32x16 o = {};
    float l;
    {
        const int lane = lane_id(), r = lane & 31, h = lane >> 5;
        const unsigned voff = lane * 16;
        if (h) q2 = bf16x4{0, 0, 0, 0};
        const unsigned aK = lane * 16, aK2 = h ? 1536 + 320 : 1024 + r * 16 + (hd & 1) * 8, aV = 1536 + h * 384 + (r < 21 ? r : 21) * 16;
        const unsigned ho = WS_H + ((rb64 * 2 + ptt) * 96 + pkq * 24 + (lane >> 5)) * 1024 + ((lane >> 4) & 1) * 512 + (lane & 15) * 16;
        const f32x16 zero = {};
        BAR_L();
        STAMP(2, 0);
        bf16x8 k1 = lds128(LP(KVB + aK)); bf16x4 k2 = lds64(LP(KVB + aK2));
        f32x16 sA = MFMA8(k2, q2, MFMA16(k1, q1, zero));
        const bf16x4 q2raw = q2;
        { const float m = max16(sA); if (h) q2[0] = (short)(pk(-m, 0.f) & 0xffffu); }
        sA = MFMA8(k2, q2, MFMA16(k1, q1, zero));
        k1 = lds128(LP(KVB + 3072 + aK)); k2 = lds64(LP(KVB + 3072 + aK2));
        bf16x8 v0 = lds128(LP(KVB + aV)), v1 = v0;
        f32x16 sB;
        u32x4 p0, p1 = {0u, 0u, 0u, 0u};
        float e12 = 0.f, e13 = 0.f, e14 = 0.f, e15 = 0.f;
#define EX(v) __builtin_amdgcn_exp2f(v)
#define ATT_STEP(SC, SN, K1A, K2A, V1A, V0A, HI) do { constexpr int WI = (HI) < 10 ? (HI) : -1; \
        SN = MFMA16(k1, q1, zero); SBAR(); \
        k1 = lds128(K1A); \
        p1[2] = pk(e12, e13); p1[3] = pk(e14, e15); PIN(p1); \
        float e0 = EX(SC[0]), e1 = EX(SC[1]), e2 = EX(SC[2]), e3 = EX(SC[3]); PIN(e0); PIN(e1); PIN(e2); PIN(e3); SBAR(); \
        SN = MFMA8(k2, q2, SN); SBAR(); \
        k2 = lds64(K2A); \
        if ((HI) >= 0) hf[((HI) >> 1) % 12][(HI) & 1] = __builtin_bit_cast(bf16x8, __builtin_amdgcn_raw_buffer_load_b128(rs, ho + ((HI) & 1) * 256, PO_K((HI) >> 1) * 2048, 16)); \
        p0[0] = pk(e0, e1); p0[1] = pk(e2, e3); PIN(p0); \
        float e4 = EX(SC[4]), e5 = EX(SC[5]), e6 = EX(SC[6]), e7 = EX(SC[7]); PIN(e4); PIN(e5); PIN(e6); PIN(e7); SBAR(); \
        o = MFMA16(v1, __builtin_bit_cast(bf16x8, p1), o); SBAR(); \
        v1 = lds128(V1A); \
        if (WI >= 0) pf[WI >= 5][WI < 0 ? 0 : WI % 5] = cat(ldstr(LP(paw) + (WI >= 5) * 20480 + WI % 5 * 8), ldstr(LP(paw) + (WI >= 5) * 20480 + WI % 5 * 8 + 2560)); \
        p0[2] = pk(e4, e5); p0[3] = pk(e6, e7); PIN(p0); \
        float e8 = EX(SC[8]), e9 = EX(SC[9]), e10 = EX(SC[10]), e11 = EX(SC[11]); PIN(e8); PIN(e9); PIN(e10); PIN(e11); SBAR(); \
        o = MFMA16(v0, __builtin_bit_cast(bf16x8, p0), o); SBAR(); \
        v0 = lds128(V0A); \
        p1[0] = pk(e8, e9); p1[1] = pk(e10, e11); PIN(p1); \
        e12 = EX(SC[12]); e13 = EX(SC[13]); e14 = EX(SC[14]); e15 = EX(SC[15]); PIN(e12); PIN(e13); PIN(e14); PIN(e15); SBAR(); \
    } while (0)
#define ATT_T(SC, SN, t, HI) ATT_STEP(SC, SN, cK + ((t) + 2) * 3072, cK2 + ((t) + 2) * 3072, cV + (t) * 3072 + 768, cV + ((t) + 1) * 3072, HI)
#define ATT_STAGE(H0, WAITC, MID) do { \
        const unsigned nxt = cur == KVB + 49152 ? KVB : cur + 24576; \
        LAS char *cK = LP(aK + cur), *cK2 = LP(aK2 + cur), *cV = LP(aV + cur); \
        ATT_T(sA, sB, 0, H0); ATT_T(sB, sA, 1, (H0) < 0 ? -1 : (H0) + 1); ATT_T(sA, sB, 2, (H0) < 0 ? -1 : (H0) + 2); ATT_T(sB, sA, 3, (H0) < 0 ? -1 : (H0) + 3); \
        ATT_T(sA, sB, 4, (H0) < 0 ? -1 : (H0) + 4); ATT_T(sB, sA, 5, (H0) < 0 ? -1 : (H0) + 5); \
        WAITC; \
        MID; \
        LAS char *nK = LP(aK + nxt), *nK2 = LP(aK2 + nxt), *nV = LP(aV + nxt); \
        ATT_STEP(sA, sB, nK, nK2, cV + 6 * 3072 + 768, cV + 7 * 3072, (H0) < 0 ? -1 : (H0) + 6); \
        ATT_STEP(sB, sA, nK + 3072, nK2 + 3072, cV + 7 * 3072 + 768, nV, (H0) < 0 ? -1 : (H0) + 7); \
        cur = nxt; } while (0)
        unsigned cur = KVB;
#pragma unroll 1
        for (int s = 0; s < 4; ++s) {
            ATT_STAGE(-1,
                      { if (s == 3 && w == 0) wait_tags(flags, F_H + rb64 * 32 + (lane & 15), tag); VM_BAR(0); },
                      { KV_DMA(s + 2, nxt == KVB + 49152 ? KVB : nxt + 24576);
                        if (s == 0) { if (w == 0) publish(flags, F_H + (rb * 2 + lane) * 32 + cb, lane < 2, tag);
                               set_m0(w * 3072); const char* p_ = wo + (size_t)(i * 25 + w * 3) * 1024;
                               GLDS(voff, p_, 0); GLDS(voff, p_, 1024); GLDS(voff, p_, 2048); if (w == 7) GLDS(voff, p_, 3072); }
                        else { set_m0((s - 1) * 20480u + pkq * 5120 + ptt * 3072); const char* p_ = pw + PO_K(s - 1) * 40960;
                               GLDS(pv[0], p_, 0); GLDS(pv[1], p_, 1024); if (ptt == 0) GLDS(pv[2], p_, 2048); }
                        if (s == 2 && b == 0 && w == 0) {
                            for (int k = 0; k < 4; ++k) wait_tags(flags, F_ACK + k * 64 + lane, tag);
                            publish(flags, F_GEN, lane == 0, tag); } });
        }
        paw = pkq * 5120 + (4 * (lane >> 4) + ((lane >> 2) & 3)) * 160 + (lane & 3) * 40;
        ATT_STAGE(0, VM_BAR(6), { KV_DMA(6, KVB); PO_DMA(3, pw + PO_K(3) * 40960); });
        ATT_STAGE(8, VM_BAR(8), { KV_DMA(7, KVB + 24576); PO_DMA(4, pw + PO_K(4) * 40960); PO_DMA(5, pw + PO_K(5) * 40960); });
        ATT_STAGE(16, VM_BAR(8), PO_DMA(6, pw + PO_K(6) * 40960));
        ATT_STAGE(-1, { asm volatile("" : "+v"(hf[11][1])); VM_BAR(0); }, PO_DMA(7, pw + PO_K(7) * 40960));
        p1[2] = pk(e12, e13); p1[3] = pk(e14, e15);
        o = MFMA16(v1, __builtin_bit_cast(bf16x8, p1), o);
#undef ATT_STEP
#undef ATT_T
#undef ATT_STAGE
        STAMP(2, 3);
        { auto rr = __builtin_amdgcn_permlane32_swap(__float_as_uint(o[8]), __float_as_uint(o[8]), false, false); l = __uint_as_float(rr[1]); }
        float am = 0.f;
#pragma unroll
        for (int e = 0; e < 12; ++e) am = __builtin_fmaxf(am, __builtin_fabsf(o[e]));
        const bool ok = (l > 5.4e-20f) && (l < 1.8e19f) && (am < 3.0e38f);
        if (__builtin_expect(!__all(ok), 0)) {
            float m = -INFINITY;
            o = zero;
#pragma unroll 1
            for (int t = 0; t < 64; ++t) {
                const unsigned rec = WS_KV + (hd * 64 + t) * 3072;
                const bf16x8 fk1 = __builtin_bit_cast(bf16x8, wld128(rs, rec + aK)); const bf16x4 fk2 = __builtin_bit_cast(bf16x4, wld64(rs, rec + aK2));
                const bf16x8 fv0 = __builtin_bit_cast(bf16x8, wld128(rs, rec + aV)), fv1 = __builtin_bit_cast(bf16x8, wld128(rs, rec + aV + 768));
                f32x16 sf = MFMA8(fk2, q2raw, MFMA16(fk1, q1, zero));
                const float mn = __builtin_fmaxf(m, max16(sf));
                const float al = EX(m - mn);
                m = mn;
                u32x4 f0, f1;
#pragma unroll
                for (int e = 0; e < 16; ++e) o[e] *= al;
#pragma unroll
                for (int e = 0; e < 4; ++e) { f0[e] = pk(EX(sf[2 * e] - mn), EX(sf[2 * e + 1] - mn)); f1[e] = pk(EX(sf[8 + 2 * e] - mn), EX(sf[8 + 2 * e + 1] - mn)); }
                o = MFMA16(fv0, __builtin_bit_cast(bf16x8, f0), o); o = MFMA16(fv1, __builtin_bit_cast(bf16x8, f1), o);
            }
            auto rr = __builtin_amdgcn_permlane32_swap(__float_as_uint(o[8]), __float_as_uint(o[8]), false, false); l = __uint_as_float(rr[1]);
        }
#undef EX
        const float inv = 1.f / l;
        const unsigned op = WS_O + (qb * 8 + w) * 40960;
        u32x4 oa;
#pragma unroll
        for (int e = 0; e < 4; ++e) oa[e] = pk(o[2 * e] * inv, o[2 * e + 1] * inv);
        wst128(rs, op + hd * 1024 + lane * 16, oa);
        wst64(rs, h ? OOB : op + 32768 + hd * 256 + r * 8, u32x2{pk(o[8] * inv, o[9] * inv), pk(o[10] * inv, o[11] * inv)});
    }
#undef KV_DMA

    {
        const int lane = lane_id(), r16 = lane & 15, q4 = lane >> 4;
        const unsigned awH = paw + 65536, awT = paw + 139264;
        f32x4 acc[2][5] = {};
        bf16x8 of[5][2];
        u32x2 xv[3];
#define PO_A(rnd) (po_slot(rnd) < 65536 ? LP(paw) + po_slot(rnd) : po_slot(rnd) < 139264 ? LP(awH) + (po_slot(rnd) - 65536) : LP(awT))
        const char* po = wo + (size_t)pkq * 204800 + cb80 * 160 - 2048;
        const size_t xo = (size_t)(rb64 * 64 + ptt * 32 + r16) * 1280 + cb80 * 160 + q4 * 40;
        SFOR(rnd, 17)
            if constexpr (rnd <= 12) LGKM0();
            if constexpr (rnd == 1) { VM_BAR2(ptt, 3, 2); if (w == 0) publish(flags, F_O + (qb * 4 + lane) * 32 + hd, lane < 4, tag); }
            else if (rnd < 6) BAR();
            else if (rnd < 10) VM_BAR2(ptt, 15, 10);
            else if constexpr (rnd == 10) { if (w == 0) wait_tags(flags, F_O + rb64 * 32 + (lane & 31), tag); VM_BAR2(ptt, 12, 8); }
            else if (rnd == 11) VM_BAR2(ptt, 24, 21);
            else if (rnd == 12) VM_BAR(0);
            if constexpr (rnd == 0) STAMP(4, 0);
            if constexpr (rnd == 6) STAMP(4, 1);
            if constexpr (rnd == 10) {
                STAMP(4, 2);
                const unsigned op = WS_O + (rb64 * 2 + ptt) * 40960;
#pragma unroll
                for (int s = 0; s < 4; ++s)
#pragma unroll
                    for (int t = 0; t < 2; ++t) of[s][t] = __builtin_bit_cast(bf16x8, wld128(rs, op + (8 * pkq + 2 * s + (q4 >> 1)) * 1024 + (q4 & 1) * 512 + (t * 16 + r16) * 16));
#pragma unroll
                for (int t = 0; t < 2; ++t) {
                    const unsigned a = op + 32768 + (8 * pkq + 2 * q4) * 256 + (t * 16 + r16) * 8;
                    of[4][t] = cat(__builtin_bit_cast(bf16x4, wld64(rs, a)), __builtin_bit_cast(bf16x4, wld64(rs, a + 256)));
                }
                SBAR();
#pragma unroll
                for (int k = 0; k < 3; ++k) { const int Q = (pkq + 4 * k) % 10, t = Q >= 5, nt = Q - 5 * t; xv[k] = *(const u32x2*)(x + xo + t * 20480 + nt * 8); }
            }
            if constexpr (rnd == 4) PO_VOFF(20 * (qq >> 1) + 4 * (qq & 1) + 8 * u + e);
            if constexpr (rnd == 8) PO_VOFF(20 * (4 * (qq >> 1) + 2 * (qq & 1) + u) + 16 + e);
            if (rnd < 4) PO_DMA(rnd + 8, pw + PO_K(rnd + 8) * 40960);
            else if (rnd < 8) PO_DMA(rnd + 8, po + (rnd - 4) * 51200);
            else if (rnd == 8) PO_DMA(16, po);
            SBAR();
#pragma unroll
            for (int nt = 0; nt < 5; ++nt) {
#pragma unroll
                for (int t = 0; t < 2; ++t) { acc[t][nt] = MFMA16X16(pf[rnd & 1][nt], rnd < 12 ? hf[rnd % 12][t] : of[(rnd + 3) % 5][t], acc[t][nt]); PIN(acc[t][nt]); }
                SBAR();
                if (rnd < 15) pf[rnd & 1][nt] = cat(ldstr(PO_A(rnd + 2) + nt * 8), ldstr(PO_A(rnd + 2) + nt * 8 + 2560));
                SBAR();
            }
            if constexpr (rnd == 15) BAR_L();
        SEND
#undef PO_A
        STAMP(4, 3);
#pragma unroll
        for (int Q = 0; Q < 10; ++Q) *(LAS f32x4*)(LP(w * 10240 + lane * 16) + Q * 1024) = acc[Q / 5][Q % 5];
        BAR_L();
#pragma unroll
        for (int k = 0; k < 3; ++k) {
            const int Q = pkq + 4 * k, t = Q >= 5, nt = Q - 5 * t;
            if (Q < 10) {
                f32x4 v = f32x4{bflo(xv[k][0]), bfhi(xv[k][0]), bflo(xv[k][1]), bfhi(xv[k][1])};
#pragma unroll
                for (int p = 0; p < 4; ++p) v += *(LAS f32x4*)(LP(ptt * 10240 + Q * 1024 + lane * 16) + p * 20480);
                *(u32x2*)(out + xo + t * 20480 + nt * 8) = u32x2{pk(v[0], v[1]), pk(v[2], v[3])};
            }
        }
    }
#undef PO_DMA
#undef PO_K
#undef PO_VOFF
    STAMP(4, 4);
}

extern "C" void kernel_launch(void* const* d_in, const int* in_sizes, int n_in, void* d_out, int out_size, void* d_ws, size_t ws_size,
                              hipStream_t stream) {
    (void)hipFuncSetAttribute((const void*)k_block, hipFuncAttributeMaxDynamicSharedMemorySize, 163840);
    k_block<<<256, 512, 163840, stream>>>((const char*)d_in[0], (const char*)d_in[1], (const char*)d_in[2], (const char*)d_in[4], (const char*)d_in[5],
                                          (const char*)d_in[6], (char*)d_ws, (char*)d_out);
}
```

```cpp
#include <hip/hip_runtime.h>
#include <hip/hip_bf16.h>
#include <stdint.h>

typedef __attribute__((ext_vector_type(8))) short bf16x8;
typedef __attribute__((ext_vector_type(4))) short bf16x4;
typedef __attribute__((ext_vector_type(16))) float f32x16;
typedef __attribute__((ext_vector_type(4))) float f32x4;
typedef __attribute__((ext_vector_type(2))) float f32x2;
typedef __attribute__((ext_vector_type(4))) unsigned u32x4;
typedef __attribute__((ext_vector_type(2))) unsigned u32x2;
typedef __attribute__((ext_vector_type(2))) __bf16 bf2;
typedef __attribute__((address_space(1))) unsigned gu32;
typedef __amdgpu_buffer_rsrc_t rsrc_t;
#define LAS __attribute__((address_space(3)))
#define DI __device__ __forceinline__

constexpr unsigned WS_Q = 0;
constexpr unsigned WS_KV = WS_Q + 32 * 64 * 1536;
constexpr unsigned WS_H = WS_KV + 32 * 64 * 3072;
constexpr unsigned WS_O = WS_H + 64 * 96 * 1024;
constexpr unsigned WS_ST = WS_O + 64 * 40 * 1024;
constexpr unsigned WS_CQ = WS_ST + 16 * 10 * 128 * 16;
constexpr unsigned WS_CG = WS_CQ + 16 * 10 * 128 * 8;
constexpr unsigned WS_FLAG = WS_CG + 16 * 10 * 192 * 8;
constexpr unsigned WS_END = WS_FLAG + 65536;
constexpr unsigned OOB = 0x7fffff00u;
constexpr int F_GEN = 0;
constexpr int F_ACK = 64;
constexpr int F_H = 1024;
constexpr int F_O = 3072;
constexpr int F_QKV = 8192;
constexpr size_t WS_DBG = 64u << 20;
#define STAMP(kid, i) do {} while (0)

#define MFMA16(a, b, c) __builtin_amdgcn_mfma_f32_32x32x16_bf16((a), (b), (c), 0, 0, 0)
#define MFMA8(a, b, c) __builtin_amdgcn_mfma_f32_32x32x8bf16_1k((a), (b), (c), 0, 0, 0)
#define MFMA16X16(a, b, c) __builtin_amdgcn_mfma_f32_16x16x32_bf16((a), (b), (c), 0, 0, 0)
#define LP(a) ((LAS char*)(uintptr_t)(a))
#define SBAR() __builtin_amdgcn_sched_barrier(0)
#define PIN(x) asm volatile("" : "+v"(x))
#define LGKM0() __builtin_amdgcn_s_waitcnt(0xC07F)
#define BAR() asm volatile("s_barrier" ::: "memory")
#define BAR_L() do { asm volatile("" ::: "memory"); LGKM0(); BAR(); } while (0)
#define VM_BAR(N) asm volatile("s_waitcnt vmcnt(" #N ")\n\ts_barrier" ::: "memory")
#define DRAIN_BAR() asm volatile("s_waitcnt vmcnt(0) lgkmcnt(0)\n\ts_barrier" ::: "memory")
#define VM_BAR2(sel, NA, NB) asm volatile("s_cmp_eq_u32 %0, 0\n\ts_cbranch_scc1 1f\n\ts_waitcnt vmcnt(" #NB ")\n1:\n\ts_waitcnt vmcnt(" #NA ")\n\ts_barrier" :: "s"((int)(sel)) : "memory", "scc")

DI void set_m0(unsigned lds) { asm volatile("s_mov_b32 m0, %0\n\ts_nop 0" :: "s"(lds) : "memory"); }
#define GLDS(voff, sbase, OFF) asm volatile("global_load_lds_dwordx4 %0, %1 offset:" #OFF :: "v"(voff), "s"(sbase) : "memory")
#define GLDS_SC1(voff, sbase, OFF) asm volatile("global_load_lds_dwordx4 %0, %1 offset:" #OFF " sc1" :: "v"(voff), "s"(sbase) : "memory")

DI int lane_id() { int l; asm volatile("v_mbcnt_lo_u32_b32 %0, -1, 0\n\tv_mbcnt_hi_u32_b32 %0, -1, %0" : "=v"(l)); return l; }
DI bf16x8 lds128(LAS char* p) { return *(LAS bf16x8*)p; }
DI bf16x4 lds64(LAS char* p) { return *(LAS bf16x4*)p; }
DI bf16x4 ldstr(LAS char* p) { return __builtin_amdgcn_ds_read_tr16_b64_v4i16((LAS bf16x4*)p); }
DI bf16x8 cat(bf16x4 a, bf16x4 b) { return __builtin_shufflevector(a, b, 0, 1, 2, 3, 4, 5, 6, 7); }
DI unsigned pk(float a, float b) { f32x2 f = {a, b}; return __builtin_bit_cast(unsigned, __builtin_convertvector(f, bf2)); }
DI float bflo(unsigned u) { return __uint_as_float(u << 16); }
DI float bfhi(unsigned u) { return __uint_as_float(u & 0xffff0000u); }
DI float dot2(unsigned a, unsigned b, float c) { return __builtin_amdgcn_fdot2_f32_bf16(__builtin_bit_cast(bf2, a), __builtin_bit_cast(bf2, b), c, false); }
DI float swap32_add(float v) { auto r = __builtin_amdgcn_permlane32_swap(__float_as_uint(v), __float_as_uint(v), false, false); return __uint_as_float(r[0]) + __uint_as_float(r[1]); }
DI float swap32_max(float v) { auto r = __builtin_amdgcn_permlane32_swap(__float_as_uint(v), __float_as_uint(v), false, false); return __builtin_fmaxf(__uint_as_float(r[0]), __uint_as_float(r[1])); }
DI float addf(float a, float b) { float r; asm("v_add_f32 %0, %1, %2" : "=v"(r) : "v"(a), "v"(b)); return r; }
DI float mulf(float a, float b) { float r; asm("v_mul_f32 %0, %1, %2" : "=v"(r) : "v"(a), "v"(b)); return r; }
DI float relu(float a) { float r; asm("v_max_f32 %0, 0, %1" : "=v"(r) : "v"(a)); return r; }
DI f32x4 add4(f32x4 a, f32x4 b) { return f32x4{addf(a[0], b[0]), addf(a[1], b[1]), addf(a[2], b[2]), addf(a[3], b[3])}; }
DI void wst128(rsrc_t rs, unsigned off, u32x4 v) { __builtin_amdgcn_raw_buffer_store_b128(v, rs, off, 0, 16); }
DI void wst64(rsrc_t rs, unsigned off, u32x2 v) { __builtin_amdgcn_raw_buffer_store_b64(v, rs, off, 0, 16); }
DI void wst32(rsrc_t rs, unsigned off, unsigned v) { __builtin_amdgcn_raw_buffer_store_b32(v, rs, off, 0, 16); }
DI u32x4 wld128(rsrc_t rs, unsigned off) { return __builtin_amdgcn_raw_buffer_load_b128(rs, off, 0, 16); }
DI u32x2 wld64(rsrc_t rs, unsigned off) { return __builtin_amdgcn_raw_buffer_load_b64(rs, off, 0, 16); }
DI unsigned wld32(rsrc_t rs, unsigned off) { return __builtin_amdgcn_raw_buffer_load_b32(rs, off, 0, 16); }
#define POLL_SLEEP 4
DI void wait_tags(gu32* flags, int word, unsigned tag) {
    for (int spin = 0; spin < (1 << 18); ++spin) {
        const unsigned v = __hip_atomic_load(flags + word, __ATOMIC_RELAXED, __HIP_MEMORY_SCOPE_AGENT);
        if (__all(v == tag)) break;
        __builtin_amdgcn_s_sleep(POLL_SLEEP);
    }
    asm volatile("" ::: "memory");
}
DI void wait_tags2(gu32* flags, int word0, int word1, unsigned tag) {
    for (int spin = 0; spin < (1 << 18); ++spin) {
        const unsigned v0 = __hip_atomic_load(flags + word0, __ATOMIC_RELAXED, __HIP_MEMORY_SCOPE_AGENT);
        const unsigned v1 = __hip_atomic_load(flags + word1, __ATOMIC_RELAXED, __HIP_MEMORY_SCOPE_AGENT);
        if (__all(v0 == tag && v1 == tag)) break;
        __builtin_amdgcn_s_sleep(POLL_SLEEP);
    }
    asm volatile("" ::: "memory");
}
DI void publish(gu32* flags, int word, bool on, unsigned tag) { if (on) __hip_atomic_store(flags + word, tag, __ATOMIC_RELAXED, __HIP_MEMORY_SCOPE_AGENT); }
#define TAG_OF(gen) ((unsigned)__builtin_amdgcn_readfirstlane(gen) + 1u)

DI float max16(const f32x16& s) {
    float a = __builtin_fmaxf(__builtin_fmaxf(s[0], s[1]), s[2]), b = __builtin_fmaxf(__builtin_fmaxf(s[3], s[4]), s[5]);
    a = __builtin_fmaxf(__builtin_fmaxf(a, s[6]), s[7]); b = __builtin_fmaxf(__builtin_fmaxf(b, s[8]), s[9]);
    a = __builtin_fmaxf(__builtin_fmaxf(a, s[10]), s[11]); b = __builtin_fmaxf(__builtin_fmaxf(b, s[12]), s[13]);
    a = __builtin_fmaxf(__builtin_fmaxf(a, s[14]), s[15]);
    return swap32_max(__builtin_fmaxf(a, b));
}

template <int I> struct IC { static constexpr int v = I; };
template <int I, int N, class F> DI void sfor(F&& f) { if constexpr (I < N) { f(IC<I>{}); sfor<I + 1, N>(f); } }
#define SFOR(var, N) sfor<0, N>([&](auto var##_) __attribute__((always_inline)) { constexpr int var = decltype(var##_)::v;
#define SEND });
#define VM0() __builtin_amdgcn_s_waitcnt(0x0F70)

constexpr unsigned SCR = 122880, KVB = 90112;
constexpr unsigned po_slot_f(int r) {
    return r < 4 ? r * 20480u : r == 4 ? KVB + 24576u : r == 5 ? KVB + 49152u : r < 12 ? po_slot_f(r - 2) : r == 12 ? KVB : po_slot_f(r - 13);
}
template <int R> struct PoSlot { static constexpr unsigned v = po_slot_f(R); };
#define po_slot(r) (PoSlot<(r)>::v)
DI int kstage(int kt, int rk) { const int k = kt + rk; return k >= 10 ? k - 10 : k; }
#define GSPIN (1 << 18)
#define ROTM 1
#define GATHER2(S0, S1, G, OFF, STRIDE) do { S0 = 0.f; S1 = 0.f; bool ok_ = true; \
        _Pragma("unroll") for (int k_ = 0; k_ < 5; ++k_) { PIN(G[k_]); S0 += __uint_as_float(G[k_][0]); S1 += __uint_as_float(G[k_][2]); ok_ = ok_ && G[k_][1] == tag && G[k_][3] == tag; } \
        for (int spin_ = 0; spin_ < GSPIN && __builtin_expect(!__all(ok_), 0); ++spin_) { \
            __builtin_amdgcn_s_sleep(POLL_SLEEP); S0 = 0.f; S1 = 0.f; ok_ = true; \
            _Pragma("unroll 1") for (int k_ = 0; k_ < 5; ++k_) { const u32x4 g_ = wld128(rs, (OFF) + k_ * (STRIDE)); \
                S0 += __uint_as_float(g_[0]); S1 += __uint_as_float(g_[2]); ok_ = ok_ && g_[1] == tag && g_[3] == tag; } } \
        S0 = swap32_add(S0); S1 = swap32_add(S1); } while (0)
#define GATHER_REQ(G, OFF, STRIDE) do { _Pragma("unroll") for (int k_ = 0; k_ < 5; ++k_) \
        asm volatile("global_load_dwordx4 %0, %1, %2 sc1" : "=v"(G[k_]) : "v"(OFF), "s"(ws + k_ * (STRIDE)) : "memory"); } while (0)
DI float fsum8(bf16x8 f) { const u32x4 u = __builtin_bit_cast(u32x4, f); return ((bflo(u[0]) + bfhi(u[0])) + (bflo(u[1]) + bfhi(u[1]))) + ((bflo(u[2]) + bfhi(u[2])) + (bflo(u[3]) + bfhi(u[3]))); }
DI unsigned x_addr(int row, int p, int h) { return row * 128 + (((2 * p + h) ^ ((row >> 1) & 7)) << 4); }
DI unsigned wq_addr(int lane, int nt, bool vt) {
    const int c = lane & 3;
    return 16384 + (lane >> 5) * 2048 + (nt >> 1) * 512 + ((lane >> 2) & 3) * 128 + (((nt ^ (lane >> 3)) & 1) << 6) + ((lane >> 4) & 1) * 32 + (vt ? c : ((c & 1) << 1) | (c >> 1)) * 8;
}
DI unsigned gu_addr(int lane, int nt) {
    const int c = lane & 3, gi = (lane >> 4) & 1;
    return 16384 + gi * 3072 + (lane >> 5) * 1536 + ((lane >> 2) & 3) * 192 + (nt >> 1) * 64 + (((nt ^ gi) & 1) << 5) + (((c & 1) << 1) | (c >> 1)) * 8;
}
#define VMW(N) asm volatile("s_waitcnt vmcnt(" #N ")" ::: "memory")
struct Ids { int rb, cb, hd, qb, rb64, cb80, rk, i; };

DI void helper_role(const char* x, const char* wqkv, const char* wo, const char* wg, const char* wu, const char* wd, char* ws, rsrc_t rs, gu32* flags,
                    const Ids id, int lw) {
    const int w = lw;
#define HPRIO 3
    __builtin_amdgcn_s_setprio(HPRIO);
    unsigned gen, tag;
    asm volatile("global_load_dword %0, %1, %2 sc1" : "=v"(gen) : "v"(0), "s"(flags + F_GEN) : "memory");
    const int lane = lane_id(), r = lane & 31, h = lane >> 5;
    const int rb = id.rb, cb = id.cb, rk = id.rk, cbl = cb < 15 ? cb : 14;
    const unsigned voff = lane * 16;
    if (lw < 2) {
        const unsigned vA0 = (lane >> 3) * 1280 + (((lane & 7) ^ (lane >> 4)) << 4), vA1 = (vA0 ^ 64) + 10240 - 1024;
        const unsigned vW0 = ((lane >> 3) & 3) * 3840 + h * 128 + (((lane & 7) ^ ((lane >> 2) & 4)) << 4), vW1 = vW0 + 15360 - 1024;
        const char* xa = x + (size_t)(rb * 128 + lw * 64) * 1280;
        const char* wb = wqkv + (size_t)lw * 32 * 3840 + cbl * 256;
#define H_4(V0, V1, P, STEP) do { GLDS(V0, P, 0); GLDS(V1, P, 1024); GLDS(V0, (P) + (STEP) - 2048, 2048); GLDS(V1, (P) + (STEP) - 2048, 3072); } while (0)
#define H_X(slot, ka) do { const char* p_ = xa + (ka) * 128; set_m0((slot) + lw * 8192); H_4(vA0, vA1, p_, 20480); set_m0((slot) + lw * 8192 + 4096); H_4(vA0, vA1, p_ + 40960, 20480); } while (0)
#define H_Q(slot, ka) do { H_X(slot, ka); const char* q_ = wb + (ka) * 245760; set_m0((slot) + 16384 + lw * 8192); H_4(vW0, vW1, q_, 30720); \
        set_m0((slot) + 16384 + lw * 8192 + 4096); H_4(vW0, vW1, q_ + 61440, 30720); } while (0)
        H_Q(0, rk); H_Q(40960, kstage(1, rk));
        SBAR();
        unsigned gv[3];
#pragma unroll
        for (int p = 0; p < 3; ++p) { const int P = 64 * p + lane, row = P / 12; gv[p] = row * 3072 + (((P - row * 12) ^ (2 * lw)) << 4) + 2048 - p * 1024; }
        const char* gb = (lw ? wu : wg) + cb * 192 - 2048;
#define H_G(slot, ka) do { H_X(slot, ka); const char* q_ = gb + (size_t)(ka) * 196608; _Pragma("unroll") for (int st_ = 0; st_ < 4; ++st_) { \
        set_m0((slot) + 16384 + st_ * 6144 + lw * 3072); GLDS(gv[0], q_ + st_ * 49152, 0); GLDS(gv[1], q_ + st_ * 49152, 1024); GLDS(gv[2], q_ + st_ * 49152, 2048); } } while (0)
        VM_BAR(16);
        H_Q(81920, kstage(2, rk));
        unsigned slot = 0; int ka = kstage(3, rk);
#pragma unroll 1
        for (int g = 0; g < 7; ++g) { VM_BAR(16); H_Q(slot, ka); slot = slot == 81920 ? 0 : slot + 40960; ka = ka == 9 ? 0 : ka + 1; }
        VM_BAR(16);
#pragma unroll 1
        for (int g = 7; g < 17; ++g) { H_G(slot, ka); slot = slot == 81920 ? 0 : slot + 40960; ka = ka == 9 ? 0 : ka + 1; VM_BAR(20); }
        VM_BAR(0);
        asm volatile("v_readfirstlane_b32 %0, %1" : "=s"(tag) : "v"(gen));
        tag += 1;
#undef H_4
#undef H_X
#undef H_Q
#undef H_G
    } else {
        const int sv = lw - 2;
        VM_BAR(0);
        asm volatile("v_readfirstlane_b32 %0, %1" : "=s"(tag) : "v"(gen));
        tag += 1;
        bf16x8 f0[2][4], f1[2][4], f2[4];
#pragma unroll
        for (int t = 0; t < 2; ++t)
#pragma unroll
            for (int p = 0; p < 4; ++p) {
                f0[t][p] = lds128(LP(x_addr(sv * 64 + t * 32 + r, p, h)));
                const unsigned a = wq_addr(lane, sv * 2 + t, cb >= 10); f1[t][p] = cat(ldstr(LP(a) + p * 4096), ldstr(LP(a) + p * 4096 + 1024));
            }
        u32x4 gs[2][5], gc[5];
        const unsigned so = WS_ST + ((rb * 10 + h * 5) * 128 + sv * 64 + r) * 16, co = WS_CQ + ((cbl * 10 + h * 5) * 128 + sv * 64 + 2 * r) * 8;
        const unsigned cg = WS_CG + ((cb * 10 + h * 5) * 192 + sv * 64 + 2 * r) * 8;
        SFOR(g, 19)
            BAR_L();
            if constexpr (g < 2) {
                float sx = 0.f, sq = 0.f, c0 = 0.f;
#pragma unroll
                for (int p = 0; p < 4; ++p) {
                    sx += fsum8(f0[g][p]); c0 += fsum8(f1[g][p]);
#pragma unroll
                    for (int e = 0; e < 4; ++e) { const unsigned v = __builtin_bit_cast(u32x4, f0[g][p])[e]; sq = __builtin_fmaf(bflo(v), bflo(v), __builtin_fmaf(bfhi(v), bfhi(v), sq)); }
                }
                sx = swap32_add(sx); sq = swap32_add(sq); c0 = swap32_add(c0);
                const int row = sv * 64 + g * 32 + r;
                wst128(rs, h == 0 ? WS_ST + ((rb * 10 + rk) * 128 + row) * 16 : OOB, u32x4{__float_as_uint(sx), tag, __float_as_uint(sq), tag});
                wst64(rs, (h == 0 && cb < 15) ? WS_CQ + ((cb * 10 + rk) * 128 + row) * 8 : OOB, u32x2{__float_as_uint(c0), tag});
            }
            if constexpr (g == 2) {
                const int vh = ((cb - 10) * 128 + 19) / 20 + (lane >> 3);
                const unsigned v = (lane & 1) ? 0u : 0x3F803F80u;
                const unsigned o = (cb >= 10 && cb < 15 && vh * 20 < (cb - 9) * 128) ? WS_KV + (vh * 64 + rb * 4 + sv * 2) * 3072 + 1536 + ((lane >> 1) & 3) * 384 + 320 + (lane & 1) * 16 : OOB;
                wst128(rs, o, u32x4{v, v, v, v}); wst128(rs, o + 3072, u32x4{v, v, v, v});
            }
            if constexpr (g == 5) { GATHER_REQ(gs[0], so, 2048); GATHER_REQ(gs[1], so + 512, 2048); GATHER_REQ(gc, co, 1024); }
            if constexpr (g == 8) {
                VMW(0);
                float s0, s1;
#pragma unroll
                for (int t = 0; t < 2; ++t) {
                    GATHER2(s0, s1, gs[t], so + t * 512, 2048);
                    const float mu = s0 * (1.f / 640.f);
                    *(LAS float*)LP(SCR + h * 512 + (sv * 64 + t * 32 + r) * 4) = h ? -mu : __builtin_amdgcn_rsqf(__builtin_fmaxf(__builtin_fmaf(-mu, mu, s1 * (1.f / 640.f)), 0.f) + 1e-5f);
                }
                GATHER2(s0, s1, gc, co, 1024);
                *(LAS f32x2*)LP(SCR + 1024 + (sv * 64 + 2 * r) * 4) = f32x2{s0, s1};
            }
            if constexpr (g == 9) {
#pragma unroll
                for (int p = 0; p < 4; ++p) {
                    const unsigned a = gu_addr(lane, 3 * sv) + 40960, c = gu_addr(lane, 3 * sv + 1) + 40960;
                    f0[0][p] = cat(ldstr(LP(a) + p * 6144), ldstr(LP(a) + p * 6144 + 768)); f0[1][p] = cat(ldstr(LP(c) + p * 6144), ldstr(LP(c) + p * 6144 + 768));
                    f2[p] = cat(ldstr(LP(a) + p * 6144 + 64), ldstr(LP(a) + p * 6144 + 832));
                }
            }
            if constexpr (g == 10) {
                const float c0 = swap32_add((fsum8(f0[0][0]) + fsum8(f0[0][1])) + (fsum8(f0[0][2]) + fsum8(f0[0][3])));
                const float c1 = swap32_add((fsum8(f0[1][0]) + fsum8(f0[1][1])) + (fsum8(f0[1][2]) + fsum8(f0[1][3])));
                const float c2 = swap32_add((fsum8(f2[0]) + fsum8(f2[1])) + (fsum8(f2[2]) + fsum8(f2[3])));
                const unsigned o = WS_CG + ((cb * 10 + rk) * 192 + sv * 96 + lane) * 8;
                wst64(rs, o, u32x2{__float_as_uint(h ? c1 : c0), tag});
                wst64(rs, h == 0 ? o + 512 : OOB, u32x2{__float_as_uint(c2), tag});
            }
            if constexpr (g == 13) if (sv == 0) {
                const int ty = cb / 5, cc = cb - ty * 5, gg = (r >> 1) * 5 + (r & 1) * 2 + 2 * h;
                const int blk = ty == 2 ? (r * 20 + 19 * h) >> 7 : gg >> 4;
                publish(flags, F_QKV + r * 128 + ((ty == 0 ? 4 : ty == 1 ? 0 : 2) + h) * 16 + rb, ty < 3 && blk == cc, tag);
                publish(flags, F_ACK + blockIdx.x, lane == 0, tag);
            }
            if constexpr (g == 14) { GATHER_REQ(gc, cg, 1536); GATHER_REQ(gs[0], cg - sv * 512 + 1024, 1536); }
            if constexpr (g == 16) {
                VMW(0);
                float s0, s1;
                GATHER2(s0, s1, gc, cg, 1536);
                *(LAS f32x2*)LP(SCR + 1536 + (sv * 64 + 2 * r) * 4) = f32x2{s0, s1};
                GATHER2(s0, s1, gs[0], cg - sv * 512 + 1024, 1536);
                *(LAS f32x2*)LP(SCR + 1536 + (128 + 2 * r) * 4) = f32x2{s0, s1};
            }
            if constexpr (g == 17) if (sv == 1) wait_tags2(flags, F_QKV + id.hd * 128 + lane, F_QKV + id.hd * 128 + 64 + (lane & 1) * 16 + id.qb * 2 + ((lane >> 1) & 1), tag);
        SEND
    }
    const char* kv = ws + WS_KV + (size_t)id.hd * 196608 + lw * 6144;
#define KV_DMA(s) do { const char* p_ = kv + (s) * 24576; set_m0(KVB + ((s) + 2) % 3 * 24576 + lw * 6144); GLDS_SC1(voff, p_, 0); GLDS_SC1(voff, p_, 1024); GLDS_SC1(voff, p_, 2048); \
        set_m0(KVB + ((s) + 2) % 3 * 24576 + lw * 6144 + 3072); GLDS_SC1(voff, p_ + 3072, 0); GLDS_SC1(voff, p_ + 3072, 1024); GLDS_SC1(voff, p_ + 3072, 2048); } while (0)
    KV_DMA(0); KV_DMA(1);
    unsigned pv[5];
#define PO_VOFF(ROW) do { _Pragma("unroll") for (int ii = 0; ii < 5; ++ii) { \
        const int P = ii * 64 + lane, rho = P / 10, c = P - rho * 10, u = rho >> 4, qq = (rho >> 2) & 3, e = rho & 3; pv[ii] = (ROW) * 1280 + c * 16 + 3072 - (ii & 3) * 1024; } } while (0)
#define PO_DMA(rnd, src) do { const char* p_ = (src); set_m0(po_slot(rnd) + lw * 5120); GLDS(pv[0], p_, 0); GLDS(pv[1], p_, 1024); GLDS(pv[2], p_, 2048); GLDS(pv[3], p_, 3072); \
        set_m0(po_slot(rnd) + lw * 5120 + 4096); GLDS(pv[4], p_, 0); } while (0)
#define PO_REG(k, src) do { const char* p_ = (src); _Pragma("unroll") for (int ii = 0; ii < 5; ++ii) \
        asm volatile("global_load_dwordx4 %0, %1, %2" : "=v"(pr[k][ii]) : "v"(pv[ii]), "s"(p_ + (ii & 3) * 1024) : "memory"); } while (0)
    PO_VOFF(8 * qq + 4 * u + e);
    const char* pw = wd + (size_t)lw * 491520 + id.cb80 * 160 - 3072;
    const char* po = wo + (size_t)lw * 204800 + id.cb80 * 160 - 3072;
    const int prot = 3 * (id.rb64 & 3);
#define PO_W(rnd) (pw + ((rnd) + prot >= 12 ? (rnd) + prot - 12 : (rnd) + prot) * 40960)
    u32x4 pr[6][5];
    VM_BAR(6);
    SFOR(s, 8)
        if constexpr (s == 1) if (lw == 0) publish(flags, F_H + (rb * 2 + lane) * 32 + cb, lane < 2, tag);
        if constexpr (s < 2) { PO_DMA(2 * s, PO_W(2 * s)); PO_DMA(2 * s + 1, PO_W(2 * s + 1)); }
        if constexpr (s >= 2 && s < 5) { PO_REG(2 * s - 4, PO_W(2 * s + 2)); PO_REG(2 * s - 3, PO_W(2 * s + 3)); }
        if constexpr (s == 3) {
            if (lw == 1) wait_tags(flags, F_H + id.rb64 * 32 + (lane & 15), tag);
            if (lw == 2 && blockIdx.x == 0) {
#pragma unroll 1
                for (int k = 0; k < 4; ++k) wait_tags(flags, F_ACK + k * 64 + lane, tag);
                publish(flags, F_GEN, lane == 0, tag);
            }
        }
        if constexpr (s == 5) {
            unsigned dummy;
            asm volatile("global_load_dword %0, %1, %2\n\ts_waitcnt vmcnt(0)" : "=&v"(dummy) : "v"((lane < 50 ? lane : 49) * 128), "s"(wo + (size_t)(id.i * 4 + lw) * 6400) : "memory");
        }
        if constexpr (s == 7) PO_DMA(4, PO_W(4));
        VM_BAR(0);
        if constexpr (s < 6) KV_DMA(s + 2);
    SEND
    PO_DMA(5, PO_W(5));
    PO_VOFF(20 * (qq >> 1) + 4 * (qq & 1) + 8 * u + e);
    SFOR(q, 13)
        if constexpr (q == 0) BAR(); else if constexpr (q == 3) VM_BAR(8); else if constexpr (q == 11) { LGKM0(); VM_BAR(20); } else if constexpr (q == 12) { VM_BAR(0); } else BAR_L();
        if constexpr (q == 2) if (lw == 0) publish(flags, F_O + (id.qb * 4 + lane) * 32 + id.hd, lane < 4, tag);
        if constexpr (q >= 4 && q < 10) {
#pragma unroll
            for (int ii = 0; ii < 5; ++ii) { PIN(pr[q - 4][ii]); *(LAS u32x4*)(LP(po_slot(q + 2) + lw * 5120 + voff) + ii * 1024) = pr[q - 4][ii]; }
        }
        if constexpr (q == 8) PO_VOFF(20 * (4 * (qq >> 1) + 2 * (qq & 1) + u) + 16 + e);
        if constexpr (q < 10) {
            const char* p_ = po + (q < 8 ? (q >> 1) * 51200 : 0);
            set_m0(po_slot(12 + (q >> 1)) + lw * 5120);
            if constexpr (q & 1) { GLDS(pv[3], p_, 3072); set_m0(po_slot(12 + (q >> 1)) + lw * 5120 + 4096); GLDS(pv[4], p_, 0); }
            else { GLDS(pv[0], p_, 0); GLDS(pv[1], p_, 1024); GLDS(pv[2], p_, 2048); }
        }
        if constexpr (q == 9) if (lw == 1) wait_tags(flags, F_O + id.rb64 * 32 + r, tag);
    SEND
#undef KV_DMA
#undef PO_VOFF
#undef PO_DMA
#undef PO_REG
#undef PO_W
}

template <bool VT>
DI void qkv_phase(char* ws, rsrc_t rs, int rb, int cb, int w) {
    const int lane = lane_id(), wm = w >> 2, wn = w & 3, r = lane & 31, h = lane >> 5, xrow = wm * 64 + r;
    unsigned ax[4];
#pragma unroll
    for (int p = 0; p < 4; ++p) ax[p] = x_addr(xrow, p, h);
    const unsigned aw = wq_addr(lane, wn, VT);
    const unsigned rec = cb < 5 ? 1536 : 3072;
    unsigned off[2];
    if (!VT) {
#pragma unroll
        for (int o = 0; o < 2; ++o) {
            const int Gc = (cb % 5) * 16 + wn * 4 + o * 2 + h, P = Gc / 5, gp = Gc - P * 5;
            off[o] = (cb < 5 ? WS_Q : WS_KV) + (rb * 4 + wm * 2) * rec + r * 16 + (2 * P + (gp > 2)) * 64 * rec + (gp > 2 ? gp - 3 : gp) * 512 + (gp == 2);
            PIN(off[o]);
        }
    } else {
        const int col = (cb - 10) * 128 + wn * 32 + r, vh = col / 20, d = col - vh * 20;
        off[0] = cb < 15 ? WS_KV + (vh * 64 + rb * 4 + wm * 2) * 3072 + 1536 + h * 384 + d * 16 : OOB;
        PIN(off[0]);
    }
    f32x16 acc[2] = {};
    bf16x8 xA[4][2], wA[4], xB[4][2], wB[4];
#define Q_MM(X, W, p, i) do { acc[i] = VT ? MFMA16(X[p][i], W[p], acc[i]) : MFMA16(W[p], X[p][i], acc[i]); PIN(acc[i]); SBAR(); } while (0)
#define Q_RW(p, A) cat(ldstr(LP(A) + (p) * 4096), ldstr(LP(A) + (p) * 4096 + 1024))
#define Q_STAGE(XC, WC, XN, WN) do { BAR_L(); const unsigned a_ = aw + sl; \
        _Pragma("unroll") for (int p = 0; p < 4; ++p) { const unsigned b_ = ax[p] + sl; \
            XN[p][0] = lds128(LP(b_)); XN[p][1] = lds128(LP(b_) + 4096); if (p == 3) WN[3] = Q_RW(3, a_); SBAR(); Q_MM(XC, WC, p, 0); \
            if (p < 3) WN[p] = Q_RW(p, a_); SBAR(); Q_MM(XC, WC, p, 1); } \
        sl = sl == 81920 ? 0 : sl + 40960; } while (0)
    BAR();
    STAMP(0, 1);
#pragma unroll
    for (int p = 0; p < 4; ++p) { xA[p][0] = lds128(LP(ax[p])); xA[p][1] = lds128(LP(ax[p]) + 4096); wA[p] = Q_RW(p, aw); }
    unsigned sl = 40960;
#pragma unroll 1
    for (int g = 0; g < 8; g += 2) {
        Q_STAGE(xA, wA, xB, wB); Q_STAGE(xB, wB, xA, wA);
        if (g == 2) STAMP(5, 1);
        if (g == 4) STAMP(5, 2);
    }
    Q_STAGE(xA, wA, xB, wB);
    BAR_L();
    f32x4 e0[VT ? 8 : 2], e1[VT ? 8 : 2];
    float sa[2], sb[2];
    Q_MM(xB, wB, 0, 0); Q_MM(xB, wB, 0, 1);
    if (!VT) {
#pragma unroll
        for (int i = 0; i < 2; ++i) { sa[i] = *(LAS float*)LP(SCR + (xrow + 32 * i) * 4); sb[i] = *(LAS float*)LP(SCR + 512 + (xrow + 32 * i) * 4); }
#pragma unroll
        for (int o = 0; o < 2; ++o) { e0[o] = *(LAS f32x4*)LP(SCR + 1024 + (wn * 32 + o * 16 + 4 * h) * 4); e1[o] = *(LAS f32x4*)LP(SCR + 1024 + (wn * 32 + o * 16 + 8 + 4 * h) * 4); }
    } else {
        sa[0] = *(LAS float*)LP(SCR + 1024 + (wn * 32 + r) * 4);
#pragma unroll
        for (int k = 0; k < (VT ? 8 : 2); ++k) { e0[k] = *(LAS f32x4*)LP(SCR + (wm * 64 + 8 * k + 4 * h) * 4); e1[k] = *(LAS f32x4*)LP(SCR + 512 + (wm * 64 + 8 * k + 4 * h) * 4); }
    }
    SBAR();
    Q_MM(xB, wB, 1, 0); Q_MM(xB, wB, 2, 0); Q_MM(xB, wB, 3, 0); Q_MM(xB, wB, 1, 1); Q_MM(xB, wB, 2, 1); Q_MM(xB, wB, 3, 1);
#undef Q_MM
#undef Q_RW
#undef Q_STAGE
    STAMP(0, 2);
#pragma unroll
    for (int i = 0; i < 2; ++i) {
        const f32x16& c = acc[i];
#pragma unroll
        for (int o = 0; o < 2; ++o) {
            const f32x4 c0 = {c[8 * o], c[8 * o + 1], c[8 * o + 2], c[8 * o + 3]}, c1 = {c[8 * o + 4], c[8 * o + 5], c[8 * o + 6], c[8 * o + 7]};
            if (!VT) {
                const float a = cb < 5 ? sa[i] * (0.22360679774997896f * 1.4426950408889634f) : sa[i];
                const f32x4 v0 = (c0 + e0[o] * sb[i]) * a, v1 = (c1 + e1[o] * sb[i]) * a;
                const u32x4 ov = {pk(v0[0], v0[1]), pk(v0[2], v0[3]), pk(v1[0], v1[1]), pk(v1[2], v1[3])};
                wst128(rs, (off[o] & ~1u) + i * rec, ov);
                if (off[o] & 1) wst128(rs, off[o] - 1 + (64 + i) * rec, ov);
            } else {
                constexpr int M = VT ? 7 : 1;
                const f32x4 v0 = (c0 + e1[(4 * i + 2 * o) & M] * sa[0]) * e0[(4 * i + 2 * o) & M], v1 = (c1 + e1[(4 * i + 2 * o + 1) & M] * sa[0]) * e0[(4 * i + 2 * o + 1) & M];
                wst128(rs, off[0] + i * 3072 + o * 768, u32x4{pk(v0[0], v0[1]), pk(v0[2], v0[3]), pk(v1[0], v1[1]), pk(v1[2], v1[3])});
            }
        }
    }
}

extern "C" __global__ __launch_bounds__(768, 3) void k_block(const char* __restrict__ x, const char* __restrict__ wqkv, const char* __restrict__ wo,
                                                             const char* __restrict__ wg, const char* __restrict__ wu, const char* __restrict__ wd,
                                                             char* __restrict__ ws, char* __restrict__ out) {
    extern __shared__ __attribute__((aligned(16))) char smem[];
    const int b = blockIdx.x, j = b & 7, i = b >> 3;
    const int w = __builtin_amdgcn_readfirstlane(threadIdx.x >> 6);
    const rsrc_t rs = __builtin_amdgcn_make_buffer_rsrc(ws, 0, WS_END, 0x00020000);
    const int rb = 4 * (j >> 1) + (i & 3), cb = (j & 1) * 8 + (i >> 2);
    const int hd = j * 4 + (i >> 3), qb = i & 7;
    const int rb64 = j * 4 + (i & 3), cb80 = i >> 2;
    if (w >= 8) {
        const Ids id = {rb, cb, hd, qb, rb64, cb80, (((rb & 3) * 5 >> 1) + (cb & 7) + ((cb >> 2) & 1) + (j & 1) + (j >> 1)) % 10 * ROTM, i};
        helper_role(x, wqkv, wo, wg, wu, wd, ws, rs, (gu32*)(ws + WS_FLAG), id, w - 8);
        return;
    }
    STAMP(0, 0);
    if (cb < 10) qkv_phase<false>(ws, rs, rb, cb, w); else qkv_phase<true>(ws, rs, rb, cb, w);
    STAMP(0, 3);

    bf16x8 q1; bf16x4 q2;
    {
        const int lane = lane_id(), tt = w & 3, fh = w >> 2, r = lane & 31, h = lane >> 5, xrow = tt * 32 + r;
        unsigned ax[4];
#pragma unroll
        for (int p = 0; p < 4; ++p) ax[p] = x_addr(xrow, p, h);
        const unsigned awA = gu_addr(lane, 3 * fh), awB = gu_addr(lane, 3 * fh + 1);
        f32x16 acc[3] = {};
        bf16x8 xf[3], wf[4][3], xA, wA[2], xB, wB[2];
#define G_RW(p, a, A, B) ((a) == 1 ? cat(ldstr(LP(B) + (p) * 6144), ldstr(LP(B) + (p) * 6144 + 768)) : cat(ldstr(LP(A) + (p) * 6144 + (a) * 32), ldstr(LP(A) + (p) * 6144 + (a) * 32 + 768)))
#define G_MM(X, W, a) do { acc[a] = MFMA16(W, X, acc[a]); PIN(acc[a]); SBAR(); } while (0)
#define G_STAGE(XC, WC, XN, WN) do { const unsigned a_ = awA + sl, c_ = awB + sl; \
        WN[0] = G_RW(3, 1, a_, c_); SBAR(); \
        _Pragma("unroll") for (int p = 0; p < 3; ++p) { \
            G_MM(xf[p], wf[p][0], 0); wf[p][0] = G_RW(p, 0, a_, c_); if (p == 0) XN = lds128(LP(ax[3] + sl)); if (p == 1) WN[1] = G_RW(3, 2, a_, c_); SBAR(); \
            G_MM(xf[p], wf[p][1], 1); wf[p][1] = G_RW(p, 1, a_, c_); SBAR(); \
            G_MM(xf[p], wf[p][2], 2); wf[p][2] = G_RW(p, 2, a_, c_); xf[p] = lds128(LP(ax[p] + sl)); SBAR(); } \
        G_MM(XC, wf[3][0], 0); wf[3][0] = G_RW(3, 0, a_, c_); SBAR(); G_MM(XC, WC[0], 1); G_MM(XC, WC[1], 2); \
        sl = sl == 81920 ? 0 : sl + 40960; } while (0)
        STAMP(1, 0);
#pragma unroll
        for (int p = 0; p < 3; ++p) {
            xf[p] = lds128(LP(ax[p] + 40960));
#pragma unroll
            for (int a = 0; a < 3; ++a) wf[p][a] = G_RW(p, a, awA + 40960, awB + 40960);
        }
        xA = lds128(LP(ax[3] + 40960)); wf[3][0] = G_RW(3, 0, awA + 40960, awB + 40960); wA[0] = G_RW(3, 1, awA + 40960, awB + 40960); wA[1] = G_RW(3, 2, awA + 40960, awB + 40960);
        unsigned sl = 81920;
#pragma unroll 1
        for (int g = 10; g < 18; g += 2) {
            BAR_L();
            G_STAGE(xA, wA, xB, wB);
            if (g == 12) VM0();
            BAR_L();
            G_STAGE(xB, wB, xA, wA);
            if (g == 12) STAMP(1, 1);
        }
        BAR_L();
        {
            const char* qp = ws + WS_Q + (hd * 64 + qb * 8 + w) * 1536;
            asm volatile("global_load_dwordx4 %0, %1, %2 sc1" : "=v"(q1) : "v"(lane * 16), "s"(qp) : "memory");
            asm volatile("global_load_dwordx2 %0, %1, %2 offset:1024 sc1" : "=v"(q2) : "v"(r * 16 + (hd & 1) * 8), "s"(qp) : "memory");
        }
        G_STAGE(xA, wA, xB, wB);
        const float ra = *(LAS float*)LP(SCR + xrow * 4), rm = *(LAS float*)LP(SCR + 512 + xrow * 4);
        f32x4 e0[6], e1[6];
        G_MM(xf[0], wf[0][0], 0); G_MM(xf[0], wf[0][1], 1); G_MM(xf[0], wf[0][2], 2); G_MM(xf[1], wf[1][0], 0); G_MM(xf[1], wf[1][1], 1); G_MM(xf[1], wf[1][2], 2);
#pragma unroll
        for (int t = 0; t < 6; ++t) { e0[t] = *(LAS f32x4*)LP(SCR + 1536 + (fh * 96 + (t >> 1) * 32 + 8 * (t & 1) + 4 * h) * 4); e1[t] = *(LAS f32x4*)LP(SCR + 1536 + (fh * 96 + (t >> 1) * 32 + 16 + 8 * (t & 1) + 4 * h) * 4); }
        SBAR();
        G_MM(xf[2], wf[2][0], 0); G_MM(xB, wf[3][0], 0); G_MM(xf[2], wf[2][1], 1); G_MM(xB, wB[0], 1); G_MM(xf[2], wf[2][2], 2); G_MM(xB, wB[1], 2);
#undef G_RW
#undef G_MM
#undef G_STAGE
        STAMP(1, 3);
        const float a2 = ra * ra;
        const unsigned hp = WS_H + ((rb * 4 + tt) * 96 + cb * 6 + 3 * fh) * 1024 + lane * 16;
#pragma unroll
        for (int a = 0; a < 3; ++a) {
            u32x4 o;
#pragma unroll
            for (int g = 0; g < 2; ++g) {
                const f32x16& c = acc[a];
                const f32x4 gv = f32x4{c[4 * g], c[4 * g + 1], c[4 * g + 2], c[4 * g + 3]} + e0[2 * a + g] * rm;
                const f32x4 uv = (f32x4{c[8 + 4 * g], c[8 + 4 * g + 1], c[8 + 4 * g + 2], c[8 + 4 * g + 3]} + e1[2 * a + g] * rm) * a2;
                o[2 * g] = pk(__builtin_fmaxf(gv[0], 0.f) * uv[0], __builtin_fmaxf(gv[1], 0.f) * uv[1]);
                o[2 * g + 1] = pk(__builtin_fmaxf(gv[2], 0.f) * uv[2], __builtin_fmaxf(gv[3], 0.f) * uv[3]);
            }
            wst128(rs, hp + a * 1024, o);
        }
    }
    STAMP(1, 4);

    bf16x8 hf[12][2];
    const int ptt = w & 1, pkq = w >> 1, prot = 3 * (rb64 & 3);
    unsigned ho;
#define H_REQ(HI) hf[((HI) >> 1) % 12][(HI) & 1] = __builtin_bit_cast(bf16x8, __builtin_amdgcn_raw_buffer_load_b128(rs, ho + ((HI) & 1) * 256, \
        (((HI) >> 1) + prot >= 12 ? ((HI) >> 1) + prot - 12 : ((HI) >> 1) + prot) * 2048, 16))
    {
        const int lane = lane_id(), r = lane & 31, h = lane >> 5;
        f32x16 o = {};
        float l;
        asm volatile("s_waitcnt vmcnt(3)" : "+v"(q1), "+v"(q2) :: "memory");
        if (h) q2 = bf16x4{0, 0, 0, 0};
        const unsigned aK = lane * 16, aK2 = h ? 1536 + 320 : 1024 + r * 16 + (hd & 1) * 8, aV = 1536 + h * 384 + (r < 21 ? r : 21) * 16;
        const f32x16 zero = {};
        BAR_L();
        STAMP(2, 0);
        constexpr unsigned B0 = KVB + 49152;
        bf16x8 k1 = lds128(LP(B0 + aK)); bf16x4 k2 = lds64(LP(B0 + aK2));
        f32x16 sA = MFMA8(k2, q2, MFMA16(k1, q1, zero));
        { const float m = max16(sA); if (h) q2[0] = (short)(pk(-m, 0.f) & 0xffffu); }
        sA = MFMA8(k2, q2, MFMA16(k1, q1, zero));
        k1 = lds128(LP(B0 + 3072 + aK)); k2 = lds64(LP(B0 + 3072 + aK2));
        bf16x8 v0 = lds128(LP(B0 + aV)), v1 = v0;
        f32x16 sB;
        u32x4 p0, p1 = {0u, 0u, 0u, 0u};
        float e12 = 0.f, e13 = 0.f, e14 = 0.f, e15 = 0.f;
#define EX(v) __builtin_amdgcn_exp2f(v)
#define ATT_STEP(SC, SN, K1A, K2A, V1A, V0A, HI) do { \
        SN = MFMA16(k1, q1, zero); SBAR(); \
        k1 = lds128(K1A); \
        p1[2] = pk(e12, e13); p1[3] = pk(e14, e15); PIN(p1); \
        float e0 = EX(SC[0]), e1 = EX(SC[1]), e2 = EX(SC[2]), e3 = EX(SC[3]); PIN(e0); PIN(e1); PIN(e2); PIN(e3); SBAR(); \
        SN = MFMA8(k2, q2, SN); SBAR(); \
        k2 = lds64(K2A); \
        if ((HI) >= 0) H_REQ(HI); \
        p0[0] = pk(e0, e1); p0[1] = pk(e2, e3); PIN(p0); \
        float e4 = EX(SC[4]), e5 = EX(SC[5]), e6 = EX(SC[6]), e7 = EX(SC[7]); PIN(e4); PIN(e5); PIN(e6); PIN(e7); SBAR(); \
        o = MFMA16(v1, __builtin_bit_cast(bf16x8, p1), o); SBAR(); \
        v1 = lds128(V1A); \
        p0[2] = pk(e4, e5); p0[3] = pk(e6, e7); PIN(p0); \
        float e8 = EX(SC[8]), e9 = EX(SC[9]), e10 = EX(SC[10]), e11 = EX(SC[11]); PIN(e8); PIN(e9); PIN(e10); PIN(e11); SBAR(); \
        o = MFMA16(v0, __builtin_bit_cast(bf16x8, p0), o); SBAR(); \
        v0 = lds128(V0A); \
        p1[0] = pk(e8, e9); p1[1] = pk(e10, e11); PIN(p1); \
        e12 = EX(SC[12]); e13 = EX(SC[13]); e14 = EX(SC[14]); e15 = EX(SC[15]); PIN(e12); PIN(e13); PIN(e14); PIN(e15); SBAR(); \
    } while (0)
#define ATT_T(SC, SN, t, HI) ATT_STEP(SC, SN, cK + ((t) + 2) * 3072, cK2 + ((t) + 2) * 3072, cV + (t) * 3072 + 768, cV + ((t) + 1) * 3072, HI)
#define ATT_STAGE(H0, WAITC) do { \
        const unsigned nxt = cur == KVB + 49152 ? KVB : cur + 24576; \
        LAS char *cK = LP(aK + cur), *cK2 = LP(aK2 + cur), *cV = LP(aV + cur); \
        ATT_T(sA, sB, 0, H0); ATT_T(sB, sA, 1, (H0) < 0 ? -1 : (H0) + 1); ATT_T(sA, sB, 2, (H0) < 0 ? -1 : (H0) + 2); ATT_T(sB, sA, 3, (H0) < 0 ? -1 : (H0) + 3); \
        ATT_T(sA, sB, 4, (H0) < 0 ? -1 : (H0) + 4); ATT_T(sB, sA, 5, (H0) < 0 ? -1 : (H0) + 5); \
        WAITC; \
        cK = LP(aK + nxt); cK2 = LP(aK2 + nxt); \
        ATT_STEP(sA, sB, cK, cK2, cV + 6 * 3072 + 768, cV + 7 * 3072, (H0) < 0 ? -1 : (H0) + 6); \
        ATT_STEP(sB, sA, cK + 3072, cK2 + 3072, cV + 7 * 3072 + 768, LP(aV + nxt), (H0) < 0 ? -1 : (H0) + 7); \
        cur = nxt; } while (0)
        unsigned cur = B0;
#pragma unroll 1
        for (int s = 0; s < 6; ++s) ATT_STAGE(-1, VM_BAR(0));
        ho = WS_H + ((rb64 * 2 + ptt) * 96 + pkq * 24 + h) * 1024 + ((lane >> 4) & 1) * 512 + (lane & 15) * 16;
        ATT_STAGE(0, BAR());
        ATT_STAGE(8, BAR());
        p1[2] = pk(e12, e13); p1[3] = pk(e14, e15);
        o = MFMA16(v1, __builtin_bit_cast(bf16x8, p1), o);
#undef ATT_STEP
#undef ATT_T
#undef ATT_STAGE
        STAMP(2, 3);
        { auto rr = __builtin_amdgcn_permlane32_swap(__float_as_uint(o[8]), __float_as_uint(o[8]), false, false); l = __uint_as_float(rr[1]); }
        float am = 0.f;
#pragma unroll
        for (int e = 0; e < 12; ++e) am = __builtin_fmaxf(am, __builtin_fabsf(o[e]));
        const bool ok = (l > 5.4e-20f) && (l < 1.8e19f) && (am < 3.0e38f);
        if (__builtin_expect(!__all(ok), 0)) {
            float m = -INFINITY;
            o = zero;
            if (h) q2[0] = 0;
#pragma unroll 1
            for (int t = 0; t < 64; ++t) {
                const unsigned rec = WS_KV + (hd * 64 + t) * 3072;
                const bf16x8 fk1 = __builtin_bit_cast(bf16x8, wld128(rs, rec + aK)); const bf16x4 fk2 = __builtin_bit_cast(bf16x4, wld64(rs, rec + aK2));
                const bf16x8 fv0 = __builtin_bit_cast(bf16x8, wld128(rs, rec + aV)), fv1 = __builtin_bit_cast(bf16x8, wld128(rs, rec + aV + 768));
                f32x16 sf = MFMA8(fk2, q2, MFMA16(fk1, q1, zero));
                const float mn = __builtin_fmaxf(m, max16(sf));
                const float al = EX(m - mn);
                m = mn;
                u32x4 f0, f1;
#pragma unroll
                for (int e = 0; e < 16; ++e) o[e] *= al;
#pragma unroll
                for (int e = 0; e < 4; ++e) { f0[e] = pk(EX(sf[2 * e] - mn), EX(sf[2 * e + 1] - mn)); f1[e] = pk(EX(sf[8 + 2 * e] - mn), EX(sf[8 + 2 * e + 1] - mn)); }
                o = MFMA16(fv0, __builtin_bit_cast(bf16x8, f0), o); o = MFMA16(fv1, __builtin_bit_cast(bf16x8, f1), o);
            }
            auto rr = __builtin_amdgcn_permlane32_swap(__float_as_uint(o[8]), __float_as_uint(o[8]), false, false); l = __uint_as_float(rr[1]);
        }
#undef EX
        const float inv = __builtin_amdgcn_rcpf(l);
        const unsigned op = WS_O + (qb * 8 + w) * 40960;
        u32x4 oa;
#pragma unroll
        for (int e = 0; e < 4; ++e) oa[e] = pk(o[2 * e] * inv, o[2 * e + 1] * inv);
        wst128(rs, op + hd * 1024 + lane * 16, oa);
        wst64(rs, h ? OOB : op + 32768 + hd * 256 + r * 8, u32x2{pk(o[8] * inv, o[9] * inv), pk(o[10] * inv, o[11] * inv)});
    }

    {
        const int lane = lane_id(), r16 = lane & 15, q4 = lane >> 4;
        const unsigned paw = pkq * 5120 + (4 * q4 + ((lane >> 2) & 3)) * 160 + (lane & 3) * 40, awH = paw + 61440, awT = paw + 114688;
        f32x4 acc[2][5] = {};
        bf16x8 pf[2][5];
        u32x2 xv[3];
#define PO_A(rnd) (po_slot(rnd) < 61440 ? LP(paw) + po_slot(rnd) : po_slot(rnd) < 114688 ? LP(awH) + (po_slot(rnd) - 61440) : LP(awT) + (po_slot(rnd) - 114688))
#define PO_RW(rnd, nt) pf[(rnd) & 1][nt] = cat(ldstr(PO_A(rnd) + (nt) * 8), ldstr(PO_A(rnd) + (nt) * 8 + 2560))
        const size_t xo = (size_t)(rb64 * 64 + ptt * 32 + r16) * 1280 + cb80 * 160 + q4 * 40;
#pragma unroll
        for (int nt = 0; nt < 5; ++nt) PO_RW(0, nt);
        SFOR(q, 17)
            if constexpr (q == 2) asm volatile("s_waitcnt vmcnt(2)" ::: "memory");
            if constexpr (q <= 12) BAR_L();
            if constexpr (q == 0) STAMP(4, 0);
            if constexpr (q == 6) STAMP(4, 1);
            if constexpr (q >= 1 && q < 5) { H_REQ(14 + 2 * q); H_REQ(15 + 2 * q); }
            if constexpr (q == 6) {
#pragma unroll
                for (int k = 0; k < 3; ++k) { const int Q = (pkq + 4 * k) % 10, t = Q >= 5, nt = Q - 5 * t; xv[k] = *(const u32x2*)(x + xo + t * 20480 + nt * 8); }
            }
            if constexpr (q == 10) {
                STAMP(4, 2);
                const unsigned op = WS_O + (rb64 * 2 + ptt) * 40960;
#pragma unroll
                for (int s = 0; s < 4; ++s)
#pragma unroll
                    for (int t = 0; t < 2; ++t) hf[s][t] = __builtin_bit_cast(bf16x8, wld128(rs, op + (8 * pkq + 2 * s + (q4 >> 1)) * 1024 + (q4 & 1) * 512 + (t * 16 + r16) * 16));
#pragma unroll
                for (int t = 0; t < 2; ++t) {
                    const unsigned a = op + 32768 + (8 * pkq + 2 * q4) * 256 + (t * 16 + r16) * 8;
                    hf[4][t] = cat(__builtin_bit_cast(bf16x4, wld64(rs, a)), __builtin_bit_cast(bf16x4, wld64(rs, a + 256)));
                }
            }
            SBAR();
#pragma unroll
            for (int nt = 0; nt < 5; ++nt) {
#pragma unroll
                for (int t = 0; t < 2; ++t) {
                    acc[t][nt] = MFMA16X16(pf[q & 1][nt], hf[q % 12][t], acc[t][nt]); PIN(acc[t][nt]); SBAR();
                    if constexpr (q < 16) if (2 * nt + t < 5) PO_RW(q + 1, (2 * nt + t) % 5);
                    SBAR();
                }
            }
            if constexpr (q == 15) BAR_L();
        SEND
#undef PO_A
#undef PO_RW
        STAMP(4, 3);
#pragma unroll
        for (int Q = 0; Q < 10; ++Q) *(LAS f32x4*)(LP(w * 10240 + lane * 16) + Q * 1024) = acc[Q / 5][Q % 5];
        BAR_L();
#pragma unroll
        for (int k = 0; k < 3; ++k) {
            const int Q = pkq + 4 * k, t = Q >= 5, nt = Q - 5 * t;
            if (Q < 10) {
                f32x4 v = f32x4{bflo(xv[k][0]), bfhi(xv[k][0]), bflo(xv[k][1]), bfhi(xv[k][1])};
#pragma unroll
                for (int p = 0; p < 4; ++p) v += *(LAS f32x4*)(LP(ptt * 10240 + Q * 1024 + lane * 16) + p * 20480);
                *(u32x2*)(out + xo + t * 20480 + nt * 8) = u32x2{pk(v[0], v[1]), pk(v[2], v[3])};
            }
        }
    }
#undef H_REQ
    STAMP(4, 4);
}

extern "C" void kernel_launch(void* const* d_in, const int* in_sizes, int n_in, void* d_out, int out_size, void* d_ws, size_t ws_size,
                              hipStream_t stream) {
    (void)hipFuncSetAttribute((const void*)k_block, hipFuncAttributeMaxDynamicSharedMemorySize, 163840);
    k_block<<<256, 768, 163840, stream>>>((const char*)d_in[0], (const char*)d_in[1], (const char*)d_in[2], (const char*)d_in[4], (const char*)d_in[5],
                                          (const char*)d_in[6], (char*)d_ws, (char*)d_out);
}
```

```cpp
#define MEAS 0
#include <hip/hip_runtime.h>
#include <hip/hip_bf16.h>
#include <stdint.h>

typedef __attribute__((ext_vector_type(8))) short bf16x8;
typedef __attribute__((ext_vector_type(4))) short bf16x4;
typedef __attribute__((ext_vector_type(16))) float f32x16;
typedef __attribute__((ext_vector_type(4))) float f32x4;
typedef __attribute__((ext_vector_type(2))) float f32x2;
typedef __attribute__((ext_vector_type(4))) unsigned u32x4;
typedef __attribute__((ext_vector_type(2))) unsigned u32x2;
typedef __attribute__((ext_vector_type(2))) __bf16 bf2;
typedef __attribute__((address_space(1))) unsigned gu32;
typedef __amdgpu_buffer_rsrc_t rsrc_t;
#define LAS __attribute__((address_space(3)))
#define DI __device__ __forceinline__

constexpr unsigned WS_Q = 0;
constexpr unsigned WS_KV = WS_Q + 32 * 64 * 1536;
constexpr unsigned WS_H = WS_KV + 32 * 64 * 3072;
constexpr unsigned WS_O = WS_H + 64 * 96 * 1024;
constexpr unsigned WS_ST = WS_O + 64 * 40 * 1024;
constexpr unsigned WS_CQ = WS_ST + 16 * 10 * 128 * 16;
constexpr unsigned WS_CG = WS_CQ + 16 * 10 * 128 * 8;
constexpr unsigned WS_FLAG = WS_CG + 16 * 10 * 192 * 8;
constexpr unsigned WS_END = WS_FLAG + 65536;
constexpr unsigned OOB = 0x7fffff00u;
constexpr int F_GEN = 0;
constexpr int F_ACK = 64;
constexpr int F_H = 1024;
constexpr int F_O = 3072;
constexpr int F_QKV = 8192;
constexpr size_t WS_DBG = 64u << 20;
#define STAMP(kid, i) do {} while (0)

#define MFMA16(a, b, c) __builtin_amdgcn_mfma_f32_32x32x16_bf16((a), (b), (c), 0, 0, 0)
#define MFMA8(a, b, c) __builtin_amdgcn_mfma_f32_32x32x8bf16_1k((a), (b), (c), 0, 0, 0)
#define MFMA16X16(a, b, c) __builtin_amdgcn_mfma_f32_16x16x32_bf16((a), (b), (c), 0, 0, 0)
#define LP(a) ((LAS char*)(uintptr_t)(a))
#define SBAR() __builtin_amdgcn_sched_barrier(0)
#define PIN(x) asm volatile("" : "+v"(x))
#define LGKM0() __builtin_amdgcn_s_waitcnt(0xC07F)
#define BAR() asm volatile("s_barrier" ::: "memory")
#define BAR_L() do { asm volatile("" ::: "memory"); LGKM0(); BAR(); } while (0)
#define VM_BAR(N) asm volatile("s_waitcnt vmcnt(" #N ")\n\ts_barrier" ::: "memory")
#define DRAIN_BAR() asm volatile("s_waitcnt vmcnt(0) lgkmcnt(0)\n\ts_barrier" ::: "memory")
#define VM_BAR2(sel, NA, NB) asm volatile("s_cmp_eq_u32 %0, 0\n\ts_cbranch_scc1 1f\n\ts_waitcnt vmcnt(" #NB ")\n1:\n\ts_waitcnt vmcnt(" #NA ")\n\ts_barrier" :: "s"((int)(sel)) : "memory", "scc")

DI void set_m0(unsigned lds) { asm volatile("s_mov_b32 m0, %0\n\ts_nop 0" :: "s"(lds) : "memory"); }
#define GLDS(voff, sbase, OFF) asm volatile("global_load_lds_dwordx4 %0, %1 offset:" #OFF :: "v"(voff), "s"(sbase) : "memory")
#define GLDS_SC1(voff, sbase, OFF) asm volatile("global_load_lds_dwordx4 %0, %1 offset:" #OFF " sc1" :: "v"(voff), "s"(sbase) : "memory")

DI int lane_id() { int l; asm volatile("v_mbcnt_lo_u32_b32 %0, -1, 0\n\tv_mbcnt_hi_u32_b32 %0, -1, %0" : "=v"(l)); return l; }
DI bf16x8 lds128(LAS char* p) { return *(LAS bf16x8*)p; }
DI bf16x4 lds64(LAS char* p) { return *(LAS bf16x4*)p; }
DI bf16x4 ldstr(LAS char* p) { return __builtin_amdgcn_ds_read_tr16_b64_v4i16((LAS bf16x4*)p); }
DI bf16x8 cat(bf16x4 a, bf16x4 b) { return __builtin_shufflevector(a, b, 0, 1, 2, 3, 4, 5, 6, 7); }
DI unsigned pk(float a, float b) { f32x2 f = {a, b}; return __builtin_bit_cast(unsigned, __builtin_convertvector(f, bf2)); }
DI float bflo(unsigned u) { return __uint_as_float(u << 16); }
DI float bfhi(unsigned u) { return __uint_as_float(u & 0xffff0000u); }
DI float dot2(unsigned a, unsigned b, float c) { return __builtin_amdgcn_fdot2_f32_bf16(__builtin_bit_cast(bf2, a), __builtin_bit_cast(bf2, b), c, false); }
DI float swap32_add(float v) { auto r = __builtin_amdgcn_permlane32_swap(__float_as_uint(v), __float_as_uint(v), false, false); return __uint_as_float(r[0]) + __uint_as_float(r[1]); }
DI float swap32_max(float v) { auto r = __builtin_amdgcn_permlane32_swap(__float_as_uint(v), __float_as_uint(v), false, false); return __builtin_fmaxf(__uint_as_float(r[0]), __uint_as_float(r[1])); }
DI float addf(float a, float b) { float r; asm("v_add_f32 %0, %1, %2" : "=v"(r) : "v"(a), "v"(b)); return r; }
DI float mulf(float a, float b) { float r; asm("v_mul_f32 %0, %1, %2" : "=v"(r) : "v"(a), "v"(b)); return r; }
DI float relu(float a) { float r; asm("v_max_f32 %0, 0, %1" : "=v"(r) : "v"(a)); return r; }
DI f32x4 add4(f32x4 a, f32x4 b) { return f32x4{addf(a[0], b[0]), addf(a[1], b[1]), addf(a[2], b[2]), addf(a[3], b[3])}; }
DI void wst128(rsrc_t rs, unsigned off, u32x4 v) { __builtin_amdgcn_raw_buffer_store_b128(v, rs, off, 0, 16); }
DI void wst64(rsrc_t rs, unsigned off, u32x2 v) { __builtin_amdgcn_raw_buffer_store_b64(v, rs, off, 0, 16); }
DI void wst32(rsrc_t rs, unsigned off, unsigned v) { __builtin_amdgcn_raw_buffer_store_b32(v, rs, off, 0, 16); }
DI u32x4 wld128(rsrc_t rs, unsigned off) { return __builtin_amdgcn_raw_buffer_load_b128(rs, off, 0, 16); }
DI u32x2 wld64(rsrc_t rs, unsigned off) { return __builtin_amdgcn_raw_buffer_load_b64(rs, off, 0, 16); }
DI unsigned wld32(rsrc_t rs, unsigned off) { return __builtin_amdgcn_raw_buffer_load_b32(rs, off, 0, 16); }
#define POLL_SLEEP 4
DI void wait_tags(gu32* flags, int word, unsigned tag) {
    for (int spin = 0; spin < (1 << 18); ++spin) {
        const unsigned v = __hip_atomic_load(flags + word, __ATOMIC_RELAXED, __HIP_MEMORY_SCOPE_AGENT);
        if (__all(v == tag)) break;
        __builtin_amdgcn_s_sleep(POLL_SLEEP);
    }
    asm volatile("" ::: "memory");
}
DI void wait_tags2(gu32* flags, int word0, int word1, unsigned tag) {
    for (int spin = 0; spin < (1 << 18); ++spin) {
        const unsigned v0 = __hip_atomic_load(flags + word0, __ATOMIC_RELAXED, __HIP_MEMORY_SCOPE_AGENT);
        const unsigned v1 = __hip_atomic_load(flags + word1, __ATOMIC_RELAXED, __HIP_MEMORY_SCOPE_AGENT);
        if (__all(v0 == tag && v1 == tag)) break;
        __builtin_amdgcn_s_sleep(POLL_SLEEP);
    }
    asm volatile("" ::: "memory");
}
DI void publish(gu32* flags, int word, bool on, unsigned tag) { if (on) __hip_atomic_store(flags + word, tag, __ATOMIC_RELAXED, __HIP_MEMORY_SCOPE_AGENT); }
#define TAG_OF(gen) ((unsigned)__builtin_amdgcn_readfirstlane(gen) + 1u)

DI float max16(const f32x16& s) {
    float a = __builtin_fmaxf(__builtin_fmaxf(s[0], s[1]), s[2]), b = __builtin_fmaxf(__builtin_fmaxf(s[3], s[4]), s[5]);
    a = __builtin_fmaxf(__builtin_fmaxf(a, s[6]), s[7]); b = __builtin_fmaxf(__builtin_fmaxf(b, s[8]), s[9]);
    a = __builtin_fmaxf(__builtin_fmaxf(a, s[10]), s[11]); b = __builtin_fmaxf(__builtin_fmaxf(b, s[12]), s[13]);
    a = __builtin_fmaxf(__builtin_fmaxf(a, s[14]), s[15]);
    return swap32_max(__builtin_fmaxf(a, b));
}

template <int I> struct IC { static constexpr int v = I; };
template <int I, int N, class F> DI void sfor(F&& f) { if constexpr (I < N) { f(IC<I>{}); sfor<I + 1, N>(f); } }
#define SFOR(var, N) sfor<0, N>([&](auto var##_) __attribute__((always_inline)) { constexpr int var = decltype(var##_)::v;
#define SEND });
#define VM0() __builtin_amdgcn_s_waitcnt(0x0F70)

constexpr unsigned SCR = 122880, KVB = 90112;
constexpr unsigned po_slot_f(int r) {
    return r < 4 ? r * 20480u : r == 4 ? KVB + 24576u : r == 5 ? KVB + 49152u : r < 12 ? po_slot_f(r - 2) : r == 12 ? KVB : po_slot_f(r - 13);
}
template <int R> struct PoSlot { static constexpr unsigned v = po_slot_f(R); };
#define po_slot(r) (PoSlot<(r)>::v)
DI int kstage(int kt, int rk) { const int k = kt + rk; return k >= 10 ? k - 10 : k; }
#define GSPIN (1 << 18)
#define ROTM 1
#define GATHER2(S0, S1, G, OFF, STRIDE) do { S0 = 0.f; S1 = 0.f; bool ok_ = true; \
        _Pragma("unroll") for (int k_ = 0; k_ < 5; ++k_) { PIN(G[k_]); S0 += __uint_as_float(G[k_][0]); S1 += __uint_as_float(G[k_][2]); ok_ = ok_ && G[k_][1] == tag && G[k_][3] == tag; } \
        for (int spin_ = 0; spin_ < GSPIN && __builtin_expect(!__all(ok_), 0); ++spin_) { \
            __builtin_amdgcn_s_sleep(POLL_SLEEP); S0 = 0.f; S1 = 0.f; ok_ = true; \
            _Pragma("unroll 1") for (int k_ = 0; k_ < 5; ++k_) { const u32x4 g_ = wld128(rs, (OFF) + k_ * (STRIDE)); \
                S0 += __uint_as_float(g_[0]); S1 += __uint_as_float(g_[2]); ok_ = ok_ && g_[1] == tag && g_[3] == tag; } } \
        S0 = swap32_add(S0); S1 = swap32_add(S1); } while (0)
#define GATHER_REQ(G, OFF, STRIDE) do { _Pragma("unroll") for (int k_ = 0; k_ < 5; ++k_) \
        asm volatile("global_load_dwordx4 %0, %1, %2 sc1" : "=v"(G[k_]) : "v"(OFF), "s"(ws + k_ * (STRIDE)) : "memory"); } while (0)
DI float fsum8(bf16x8 f) { const u32x4 u = __builtin_bit_cast(u32x4, f); return ((bflo(u[0]) + bfhi(u[0])) + (bflo(u[1]) + bfhi(u[1]))) + ((bflo(u[2]) + bfhi(u[2])) + (bflo(u[3]) + bfhi(u[3]))); }
DI unsigned x_addr(int row, int p, int h) { return row * 128 + (((2 * p + h) ^ ((row >> 1) & 7)) << 4); }
DI unsigned wq_addr(int lane, int nt, bool vt) {
    const int c = lane & 3;
    return 16384 + (lane >> 5) * 2048 + (nt >> 1) * 512 + ((lane >> 2) & 3) * 128 + (((nt ^ (lane >> 3)) & 1) << 6) + ((lane >> 4) & 1) * 32 + (vt ? c : ((c & 1) << 1) | (c >> 1)) * 8;
}
DI unsigned gu_addr(int lane, int nt) {
    const int c = lane & 3, gi = (lane >> 4) & 1;
    return 16384 + gi * 3072 + (lane >> 5) * 1536 + ((lane >> 2) & 3) * 192 + (nt >> 1) * 64 + (((nt ^ gi) & 1) << 5) + (((c & 1) << 1) | (c >> 1)) * 8;
}
#define VMW(N) asm volatile("s_waitcnt vmcnt(" #N ")" ::: "memory")
struct Ids { int rb, cb, hd, qb, rb64, cb80, rk, i; };

DI void helper_role(const char* x, const char* wqkv, const char* wo, const char* wg, const char* wu, const char* wd, char* ws, rsrc_t rs, gu32* flags,
                    const Ids id, int lw) {
    const int w = lw;
#define HPRIO 3
    __builtin_amdgcn_s_setprio(HPRIO);
    unsigned gen, tag;
    asm volatile("global_load_dword %0, %1, %2 sc1" : "=v"(gen) : "v"(0), "s"(flags + F_GEN) : "memory");
    const int lane = lane_id(), r = lane & 31, h = lane >> 5;
    const int rb = id.rb, cb = id.cb, rk = id.rk, cbl = cb < 15 ? cb : 14;
    const unsigned voff = lane * 16;
    if (lw < 2) {
        const unsigned vA0 = (lane >> 3) * 1280 + (((lane & 7) ^ (lane >> 4)) << 4), vA1 = (vA0 ^ 64) + 10240 - 1024;
        const unsigned vW0 = ((lane >> 3) & 3) * 3840 + h * 128 + (((lane & 7) ^ ((lane >> 2) & 4)) << 4), vW1 = vW0 + 15360 - 1024;
        const char* xa = x + (size_t)(rb * 128 + lw * 64) * 1280;
        const char* wb = wqkv + (size_t)lw * 32 * 3840 + cbl * 256;
#define H_4(V0, V1, P, STEP) do { GLDS(V0, P, 0); GLDS(V1, P, 1024); GLDS(V0, (P) + (STEP) - 2048, 2048); GLDS(V1, (P) + (STEP) - 2048, 3072); } while (0)
#define H_X(slot, ka) do { const char* p_ = xa + (ka) * 128; set_m0((slot) + lw * 8192); H_4(vA0, vA1, p_, 20480); set_m0((slot) + lw * 8192 + 4096); H_4(vA0, vA1, p_ + 40960, 20480); } while (0)
#define H_Q(slot, ka) do { H_X(slot, ka); const char* q_ = wb + (ka) * 245760; set_m0((slot) + 16384 + lw * 8192); H_4(vW0, vW1, q_, 30720); \
        set_m0((slot) + 16384 + lw * 8192 + 4096); H_4(vW0, vW1, q_ + 61440, 30720); } while (0)
        H_Q(0, rk); H_Q(40960, kstage(1, rk));
        SBAR();
        unsigned gv[3];
#pragma unroll
        for (int p = 0; p < 3; ++p) { const int P = 64 * p + lane, row = P / 12; gv[p] = row * 3072 + (((P - row * 12) ^ (2 * lw)) << 4) + 2048 - p * 1024; }
        const char* gb = (lw ? wu : wg) + cb * 192 - 2048;
#define H_G(slot, ka) do { H_X(slot, ka); const char* q_ = gb + (size_t)(ka) * 196608; _Pragma("unroll") for (int st_ = 0; st_ < 4; ++st_) { \
        set_m0((slot) + 16384 + st_ * 6144 + lw * 3072); GLDS(gv[0], q_ + st_ * 49152, 0); GLDS(gv[1], q_ + st_ * 49152, 1024); GLDS(gv[2], q_ + st_ * 49152, 2048); } } while (0)
        VM_BAR(16);
        H_Q(81920, kstage(2, rk));
        unsigned slot = 0; int ka = kstage(3, rk);
#pragma unroll 1
        for (int g = 0; g < 7; ++g) { VM_BAR(16); H_Q(slot, ka); slot = slot == 81920 ? 0 : slot + 40960; ka = ka == 9 ? 0 : ka + 1; }
        VM_BAR(16);
#pragma unroll 1
        for (int g = 7; g < 17; ++g) { H_G(slot, ka); slot = slot == 81920 ? 0 : slot + 40960; ka = ka == 9 ? 0 : ka + 1; VM_BAR(20); }
        VM_BAR(0);
        asm volatile("v_readfirstlane_b32 %0, %1" : "=s"(tag) : "v"(gen));
        tag += 1;
#undef H_4
#undef H_X
#undef H_Q
#undef H_G
    } else {
        const int sv = lw - 2;
        VM_BAR(0);
        asm volatile("v_readfirstlane_b32 %0, %1" : "=s"(tag) : "v"(gen));
        tag += 1;
        bf16x8 f0[2][4], f1[2][4], f2[4];
#pragma unroll
        for (int t = 0; t < 2; ++t)
#pragma unroll
            for (int p = 0; p < 4; ++p) {
                f0[t][p] = lds128(LP(x_addr(sv * 64 + t * 32 + r, p, h)));
                const unsigned a = wq_addr(lane, sv * 2 + t, cb >= 10); f1[t][p] = cat(ldstr(LP(a) + p * 4096), ldstr(LP(a) + p * 4096 + 1024));
            }
        u32x4 gs[2][5], gc[5];
        const unsigned so = WS_ST + ((rb * 10 + h * 5) * 128 + sv * 64 + r) * 16, co = WS_CQ + ((cbl * 10 + h * 5) * 128 + sv * 64 + 2 * r) * 8;
        const unsigned cg = WS_CG + ((cb * 10 + h * 5) * 192 + sv * 64 + 2 * r) * 8;
        SFOR(g, 19)
            BAR_L();
            if constexpr (g < 2) {
                float sx = 0.f, sq = 0.f, c0 = 0.f;
#pragma unroll
                for (int p = 0; p < 4; ++p) {
                    sx += fsum8(f0[g][p]); c0 += fsum8(f1[g][p]);
#pragma unroll
                    for (int e = 0; e < 4; ++e) { const unsigned v = __builtin_bit_cast(u32x4, f0[g][p])[e]; sq = __builtin_fmaf(bflo(v), bflo(v), __builtin_fmaf(bfhi(v), bfhi(v), sq)); }
                }
                sx = swap32_add(sx); sq = swap32_add(sq); c0 = swap32_add(c0);
                const int row = sv * 64 + g * 32 + r;
                wst128(rs, h == 0 ? WS_ST + ((rb * 10 + rk) * 128 + row) * 16 : OOB, u32x4{__float_as_uint(sx), tag, __float_as_uint(sq), tag});
                wst64(rs, (h == 0 && cb < 15) ? WS_CQ + ((cb * 10 + rk) * 128 + row) * 8 : OOB, u32x2{__float_as_uint(c0), tag});
            }
            if constexpr (g == 2) {
                const int vh = ((cb - 10) * 128 + 19) / 20 + (lane >> 3);
                const unsigned v = (lane & 1) ? 0u : 0x3F803F80u;
                const unsigned o = (cb >= 10 && cb < 15 && vh * 20 < (cb - 9) * 128) ? WS_KV + (vh * 64 + rb * 4 + sv * 2) * 3072 + 1536 + ((lane >> 1) & 3) * 384 + 320 + (lane & 1) * 16 : OOB;
                wst128(rs, o, u32x4{v, v, v, v}); wst128(rs, o + 3072, u32x4{v, v, v, v});
            }
            if constexpr (g == 5) { GATHER_REQ(gs[0], so, 2048); GATHER_REQ(gs[1], so + 512, 2048); GATHER_REQ(gc, co, 1024); }
            if constexpr (g == 8) {
                VMW(0);
                float s0, s1;
#pragma unroll
                for (int t = 0; t < 2; ++t) {
                    GATHER2(s0, s1, gs[t], so + t * 512, 2048);
                    const float mu = s0 * (1.f / 640.f);
                    *(LAS float*)LP(SCR + h * 512 + (sv * 64 + t * 32 + r) * 4) = h ? -mu : __builtin_amdgcn_rsqf(__builtin_fmaxf(__builtin_fmaf(-mu, mu, s1 * (1.f / 640.f)), 0.f) + 1e-5f);
                }
                GATHER2(s0, s1, gc, co, 1024);
                *(LAS f32x2*)LP(SCR + 1024 + (sv * 64 + 2 * r) * 4) = f32x2{s0, s1};
            }
            if constexpr (g == 9) {
#pragma unroll
                for (int p = 0; p < 4; ++p) {
                    const unsigned a = gu_addr(lane, 3 * sv) + 40960, c = gu_addr(lane, 3 * sv + 1) + 40960;
                    f0[0][p] = cat(ldstr(LP(a) + p * 6144), ldstr(LP(a) + p * 6144 + 768)); f0[1][p] = cat(ldstr(LP(c) + p * 6144), ldstr(LP(c) + p * 6144 + 768));
                    f2[p] = cat(ldstr(LP(a) + p * 6144 + 64), ldstr(LP(a) + p * 6144 + 832));
                }
            }
            if constexpr (g == 10) {
                const float c0 = swap32_add((fsum8(f0[0][0]) + fsum8(f0[0][1])) + (fsum8(f0[0][2]) + fsum8(f0[0][3])));
                const float c1 = swap32_add((fsum8(f0[1][0]) + fsum8(f0[1][1])) + (fsum8(f0[1][2]) + fsum8(f0[1][3])));
                const float c2 = swap32_add((fsum8(f2[0]) + fsum8(f2[1])) + (fsum8(f2[2]) + fsum8(f2[3])));
                const unsigned o = WS_CG + ((cb * 10 + rk) * 192 + sv * 96 + lane) * 8;
                wst64(rs, o, u32x2{__float_as_uint(h ? c1 : c0), tag});
                wst64(rs, h == 0 ? o + 512 : OOB, u32x2{__float_as_uint(c2), tag});
            }
            if constexpr (g == 13) if (sv == 0) {
                const int ty = cb / 5, cc = cb - ty * 5, gg = (r >> 1) * 5 + (r & 1) * 2 + 2 * h;
                const int blk = ty == 2 ? (r * 20 + 19 * h) >> 7 : gg >> 4;
                publish(flags, F_QKV + r * 128 + ((ty == 0 ? 4 : ty == 1 ? 0 : 2) + h) * 16 + rb, ty < 3 && blk == cc, tag);
                publish(flags, F_ACK + blockIdx.x, lane == 0, tag);
            }
            if constexpr (g == 14) { GATHER_REQ(gc, cg, 1536); GATHER_REQ(gs[0], cg - sv * 512 + 1024, 1536); }
            if constexpr (g == 16) {
                VMW(0);
                float s0, s1;
                GATHER2(s0, s1, gc, cg, 1536);
                *(LAS f32x2*)LP(SCR + 1536 + (sv * 64 + 2 * r) * 4) = f32x2{s0, s1};
                GATHER2(s0, s1, gs[0], cg - sv * 512 + 1024, 1536);
                *(LAS f32x2*)LP(SCR + 1536 + (128 + 2 * r) * 4) = f32x2{s0, s1};
            }
            if constexpr (g == 17) if (sv == 1) wait_tags2(flags, F_QKV + id.hd * 128 + lane, F_QKV + id.hd * 128 + 64 + (lane & 1) * 16 + id.qb * 2 + ((lane >> 1) & 1), tag);
        SEND
    }
    const char* kv = ws + WS_KV + (size_t)id.hd * 196608 + lw * 6144;
#define KV_DMA(s) do { const char* p_ = kv + (s) * 24576; set_m0(KVB + ((s) + 2) % 3 * 24576 + lw * 6144); GLDS_SC1(voff, p_, 0); GLDS_SC1(voff, p_, 1024); GLDS_SC1(voff, p_, 2048); \
        set_m0(KVB + ((s) + 2) % 3 * 24576 + lw * 6144 + 3072); GLDS_SC1(voff, p_ + 3072, 0); GLDS_SC1(voff, p_ + 3072, 1024); GLDS_SC1(voff, p_ + 3072, 2048); } while (0)
    KV_DMA(0); KV_DMA(1);
    unsigned pv[5];
#define PO_VOFF(ROW) do { _Pragma("unroll") for (int ii = 0; ii < 5; ++ii) { \
        const int P = ii * 64 + lane, rho = P / 10, c = P - rho * 10, u = rho >> 4, qq = (rho >> 2) & 3, e = rho & 3; pv[ii] = (ROW) * 1280 + c * 16 + 3072 - (ii & 3) * 1024; } } while (0)
#define PO_DMA(rnd, src) do { const char* p_ = (src); set_m0(po_slot(rnd) + lw * 5120); GLDS(pv[0], p_, 0); GLDS(pv[1], p_, 1024); GLDS(pv[2], p_, 2048); GLDS(pv[3], p_, 3072); \
        set_m0(po_slot(rnd) + lw * 5120 + 4096); GLDS(pv[4], p_, 0); } while (0)
#define PO_REG(k, src) do { const char* p_ = (src); _Pragma("unroll") for (int ii = 0; ii < 5; ++ii) \
        asm volatile("global_load_dwordx4 %0, %1, %2" : "=v"(pr[k][ii]) : "v"(pv[ii]), "s"(p_ + (ii & 3) * 1024) : "memory"); } while (0)
    PO_VOFF(8 * qq + 4 * u + e);
    const char* pw = wd + (size_t)lw * 491520 + id.cb80 * 160 - 3072;
    const char* po = wo + (size_t)lw * 204800 + id.cb80 * 160 - 3072;
    const int prot = 3 * (id.rb64 & 3);
#define PO_W(rnd) (pw + ((rnd) + prot >= 12 ? (rnd) + prot - 12 : (rnd) + prot) * 40960)
    u32x4 pr[6][5];
    VM_BAR(6);
    SFOR(s, 8)
        if constexpr (s == 1) if (lw == 0) publish(flags, F_H + (rb * 2 + lane) * 32 + cb, lane < 2, tag);
        if constexpr (s < 2) { PO_DMA(2 * s, PO_W(2 * s)); PO_DMA(2 * s + 1, PO_W(2 * s + 1)); }
        if constexpr (s >= 2 && s < 5) { PO_REG(2 * s - 4, PO_W(2 * s + 2)); PO_REG(2 * s - 3, PO_W(2 * s + 3)); }
        if constexpr (s == 3) {
            if (lw == 1) wait_tags(flags, F_H + id.rb64 * 32 + (lane & 15), tag);
            if (lw == 2 && blockIdx.x == 0) {
#pragma unroll 1
                for (int k = 0; k < 4; ++k) wait_tags(flags, F_ACK + k * 64 + lane, tag);
                publish(flags, F_GEN, lane == 0, tag);
            }
        }
        if constexpr (s == 5) {
            unsigned dummy;
            asm volatile("global_load_dword %0, %1, %2\n\ts_waitcnt vmcnt(0)" : "=&v"(dummy) : "v"((lane < 50 ? lane : 49) * 128), "s"(wo + (size_t)(id.i * 4 + lw) * 6400) : "memory");
        }
        if constexpr (s == 7) PO_DMA(4, PO_W(4));
        VM_BAR(0);
        if constexpr (s < 6) KV_DMA(s + 2);
    SEND
    PO_DMA(5, PO_W(5));
    PO_VOFF(20 * (qq >> 1) + 4 * (qq & 1) + 8 * u + e);
    SFOR(q, 13)
        if constexpr (q == 0) BAR(); else if constexpr (q == 3) VM_BAR(8); else if constexpr (q == 11) { LGKM0(); VM_BAR(20); } else if constexpr (q == 12) { VM_BAR(0); } else BAR_L();
        if constexpr (q == 2) if (lw == 0) publish(flags, F_O + (id.qb * 4 + lane) * 32 + id.hd, lane < 4, tag);
        if constexpr (q >= 4 && q < 10) {
#pragma unroll
            for (int ii = 0; ii < 5; ++ii) { PIN(pr[q - 4][ii]); *(LAS u32x4*)(LP(po_slot(q + 2) + lw * 5120 + voff) + ii * 1024) = pr[q - 4][ii]; }
        }
        if constexpr (q == 8) PO_VOFF(20 * (4 * (qq >> 1) + 2 * (qq & 1) + u) + 16 + e);
        if constexpr (q < 10) {
            const char* p_ = po + (q < 8 ? (q >> 1) * 51200 : 0);
            set_m0(po_slot(12 + (q >> 1)) + lw * 5120);
            if constexpr (q & 1) { GLDS(pv[3], p_, 3072); set_m0(po_slot(12 + (q >> 1)) + lw * 5120 + 4096); GLDS(pv[4], p_, 0); }
            else { GLDS(pv[0], p_, 0); GLDS(pv[1], p_, 1024); GLDS(pv[2], p_, 2048); }
        }
        if constexpr (q == 9) if (lw == 1) wait_tags(flags, F_O + id.rb64 * 32 + r, tag);
    SEND
#undef KV_DMA
#undef PO_VOFF
#undef PO_DMA
#undef PO_REG
#undef PO_W
}

template <bool VT>
DI void qkv_phase(char* ws, rsrc_t rs, int rb, int cb, int w) {
    const int lane = lane_id(), wm = w >> 2, wn = w & 3, r = lane & 31, h = lane >> 5, xrow = wm * 64 + r;
    unsigned ax[4];
#pragma unroll
    for (int p = 0; p < 4; ++p) ax[p] = x_addr(xrow, p, h);
    const unsigned aw = wq_addr(lane, wn, VT);
    const unsigned rec = cb < 5 ? 1536 : 3072;
    unsigned off[2];
    if (!VT) {
#pragma unroll
        for (int o = 0; o < 2; ++o) {
            const int Gc = (cb % 5) * 16 + wn * 4 + o * 2 + h, P = Gc / 5, gp = Gc - P * 5;
            off[o] = (cb < 5 ? WS_Q : WS_KV) + (rb * 4 + wm * 2) * rec + r * 16 + (2 * P + (gp > 2)) * 64 * rec + (gp > 2 ? gp - 3 : gp) * 512 + (gp == 2);
            PIN(off[o]);
        }
    } else {
        const int col = (cb - 10) * 128 + wn * 32 + r, vh = col / 20, d = col - vh * 20;
        off[0] = cb < 15 ? WS_KV + (vh * 64 + rb * 4 + wm * 2) * 3072 + 1536 + h * 384 + d * 16 : OOB;
        PIN(off[0]);
    }
    f32x16 acc[2] = {};
    bf16x8 xA[4][2], wA[4], xB[4][2], wB[4];
#define Q_MM(X, W, p, i) do { acc[i] = VT ? MFMA16(X[p][i], W[p], acc[i]) : MFMA16(W[p], X[p][i], acc[i]); PIN(acc[i]); SBAR(); } while (0)
#define Q_RW(p, A) cat(ldstr(LP(A) + (p) * 4096), ldstr(LP(A) + (p) * 4096 + 1024))
#define Q_STAGE(XC, WC, XN, WN) do { BAR_L(); const unsigned a_ = aw + sl; \
        _Pragma("unroll") for (int p = 0; p < 4; ++p) { const unsigned b_ = ax[p] + sl; \
            XN[p][0] = lds128(LP(b_)); XN[p][1] = lds128(LP(b_) + 4096); if (p == 3) WN[3] = Q_RW(3, a_); SBAR(); Q_MM(XC, WC, p, 0); \
            if (p < 3) WN[p] = Q_RW(p, a_); SBAR(); Q_MM(XC, WC, p, 1); } \
        sl = sl == 81920 ? 0 : sl + 40960; } while (0)
    BAR();
    STAMP(0, 1);
#pragma unroll
    for (int p = 0; p < 4; ++p) { xA[p][0] = lds128(LP(ax[p])); xA[p][1] = lds128(LP(ax[p]) + 4096); wA[p] = Q_RW(p, aw); }
    unsigned sl = 40960;
#pragma unroll 1
    for (int g = 0; g < 8; g += 2) {
        Q_STAGE(xA, wA, xB, wB); Q_STAGE(xB, wB, xA, wA);
        if (g == 2) STAMP(5, 1);
        if (g == 4) STAMP(5, 2);
    }
    Q_STAGE(xA, wA, xB, wB);
    BAR_L();
    f32x4 e0[VT ? 8 : 2], e1[VT ? 8 : 2];
    float sa[2], sb[2];
    Q_MM(xB, wB, 0, 0); Q_MM(xB, wB, 0, 1);
    if (!VT) {
#pragma unroll
        for (int i = 0; i < 2; ++i) { sa[i] = *(LAS float*)LP(SCR + (xrow + 32 * i) * 4); sb[i] = *(LAS float*)LP(SCR + 512 + (xrow + 32 * i) * 4); }
#pragma unroll
        for (int o = 0; o < 2; ++o) { e0[o] = *(LAS f32x4*)LP(SCR + 1024 + (wn * 32 + o * 16 + 4 * h) * 4); e1[o] = *(LAS f32x4*)LP(SCR + 1024 + (wn * 32 + o * 16 + 8 + 4 * h) * 4); }
    } else {
        sa[0] = *(LAS float*)LP(SCR + 1024 + (wn * 32 + r) * 4);
#pragma unroll
        for (int k = 0; k < (VT ? 8 : 2); ++k) { e0[k] = *(LAS f32x4*)LP(SCR + (wm * 64 + 8 * k + 4 * h) * 4); e1[k] = *(LAS f32x4*)LP(SCR + 512 + (wm * 64 + 8 * k + 4 * h) * 4); }
    }
    SBAR();
    Q_MM(xB, wB, 1, 0); Q_MM(xB, wB, 2, 0); Q_MM(xB, wB, 3, 0); Q_MM(xB, wB, 1, 1); Q_MM(xB, wB, 2, 1); Q_MM(xB, wB, 3, 1);
#undef Q_MM
#undef Q_RW
#undef Q_STAGE
    STAMP(0, 2);
#pragma unroll
    for (int i = 0; i < 2; ++i) {
        const f32x16& c = acc[i];
#pragma unroll
        for (int o = 0; o < 2; ++o) {
            const f32x4 c0 = {c[8 * o], c[8 * o + 1], c[8 * o + 2], c[8 * o + 3]}, c1 = {c[8 * o + 4], c[8 * o + 5], c[8 * o + 6], c[8 * o + 7]};
            if (!VT) {
                const float a = cb < 5 ? sa[i] * (0.22360679774997896f * 1.4426950408889634f) : sa[i];
                const f32x4 v0 = (c0 + e0[o] * sb[i]) * a, v1 = (c1 + e1[o] * sb[i]) * a;
                const u32x4 ov = {pk(v0[0], v0[1]), pk(v0[2], v0[3]), pk(v1[0], v1[1]), pk(v1[2], v1[3])};
                wst128(rs, (off[o] & ~1u) + i * rec, ov);
                if (off[o] & 1) wst128(rs, off[o] - 1 + (64 + i) * rec, ov);
            } else {
                constexpr int M = VT ? 7 : 1;
                const f32x4 v0 = (c0 + e1[(4 * i + 2 * o) & M] * sa[0]) * e0[(4 * i + 2 * o) & M], v1 = (c1 + e1[(4 * i + 2 * o + 1) & M] * sa[0]) * e0[(4 * i + 2 * o + 1) & M];
                wst128(rs, off[0] + i * 3072 + o * 768, u32x4{pk(v0[0], v0[1]), pk(v0[2], v0[3]), pk(v1[0], v1[1]), pk(v1[2], v1[3])});
            }
        }
    }
}

extern "C" __global__ __launch_bounds__(768, 3) void k_block(const char* __restrict__ x, const char* __restrict__ wqkv, const char* __restrict__ wo,
                                                             const char* __restrict__ wg, const char* __restrict__ wu, const char* __restrict__ wd,
                                                             char* __restrict__ ws, char* __restrict__ out) {
    extern __shared__ __attribute__((aligned(16))) char smem[];
    const int b = blockIdx.x, j = b & 7, i = b >> 3;
    const int w = __builtin_amdgcn_readfirstlane(threadIdx.x >> 6);
    const rsrc_t rs = __builtin_amdgcn_make_buffer_rsrc(ws, 0, WS_END, 0x00020000);
    const int rb = 4 * (j >> 1) + (i & 3), cb = (j & 1) * 8 + (i >> 2);
    const int hd = j * 4 + (i >> 3), qb = i & 7;
    const int rb64 = j * 4 + (i & 3), cb80 = i >> 2;
    if (w >= 8) {
        const Ids id = {rb, cb, hd, qb, rb64, cb80, (((rb & 3) * 5 >> 1) + (cb & 7) + ((cb >> 2) & 1) + (j & 1) + (j >> 1)) % 10 * ROTM, i};
        helper_role(x, wqkv, wo, wg, wu, wd, ws, rs, (gu32*)(ws + WS_FLAG), id, w - 8);
        return;
    }
    { const unsigned long long t_ = __builtin_amdgcn_s_memrealtime(); if (w == 0) __hip_atomic_store((unsigned long long*)(ws + WS_DBG) + blockIdx.x, t_, __ATOMIC_RELAXED, __HIP_MEMORY_SCOPE_AGENT); }
    if (cb < 10) qkv_phase<false>(ws, rs, rb, cb, w); else qkv_phase<true>(ws, rs, rb, cb, w);
    STAMP(0, 3);

    bf16x8 q1; bf16x4 q2;
    {
        const int lane = lane_id(), tt = w & 3, fh = w >> 2, r = lane & 31, h = lane >> 5, xrow = tt * 32 + r;
        unsigned ax[4];
#pragma unroll
        for (int p = 0; p < 4; ++p) ax[p] = x_addr(xrow, p, h);
        const unsigned awA = gu_addr(lane, 3 * fh), awB = gu_addr(lane, 3 * fh + 1);
        f32x16 acc[3] = {};
        bf16x8 xf[3], wf[4][3], xA, wA[2], xB, wB[2];
#define G_RW(p, a, A, B) ((a) == 1 ? cat(ldstr(LP(B) + (p) * 6144), ldstr(LP(B) + (p) * 6144 + 768)) : cat(ldstr(LP(A) + (p) * 6144 + (a) * 32), ldstr(LP(A) + (p) * 6144 + (a) * 32 + 768)))
#define G_MM(X, W, a) do { acc[a] = MFMA16(W, X, acc[a]); PIN(acc[a]); SBAR(); } while (0)
#define G_STAGE(XC, WC, XN, WN) do { const unsigned a_ = awA + sl, c_ = awB + sl; \
        WN[0] = G_RW(3, 1, a_, c_); SBAR(); \
        _Pragma("unroll") for (int p = 0; p < 3; ++p) { \
            G_MM(xf[p], wf[p][0], 0); wf[p][0] = G_RW(p, 0, a_, c_); if (p == 0) XN = lds128(LP(ax[3] + sl)); if (p == 1) WN[1] = G_RW(3, 2, a_, c_); SBAR(); \
            G_MM(xf[p], wf[p][1], 1); wf[p][1] = G_RW(p, 1, a_, c_); SBAR(); \
            G_MM(xf[p], wf[p][2], 2); wf[p][2] = G_RW(p, 2, a_, c_); xf[p] = lds128(LP(ax[p] + sl)); SBAR(); } \
        G_MM(XC, wf[3][0], 0); wf[3][0] = G_RW(3, 0, a_, c_); SBAR(); G_MM(XC, WC[0], 1); G_MM(XC, WC[1], 2); \
        sl = sl == 81920 ? 0 : sl + 40960; } while (0)
        STAMP(1, 0);
#pragma unroll
        for (int p = 0; p < 3; ++p) {
            xf[p] = lds128(LP(ax[p] + 40960));
#pragma unroll
            for (int a = 0; a < 3; ++a) wf[p][a] = G_RW(p, a, awA + 40960, awB + 40960);
        }
        xA = lds128(LP(ax[3] + 40960)); wf[3][0] = G_RW(3, 0, awA + 40960, awB + 40960); wA[0] = G_RW(3, 1, awA + 40960, awB + 40960); wA[1] = G_RW(3, 2, awA + 40960, awB + 40960);
        unsigned sl = 81920;
#pragma unroll 1
        for (int g = 10; g < 18; g += 2) {
            BAR_L();
            G_STAGE(xA, wA, xB, wB);
            if (g == 12) VM0();
            BAR_L();
            G_STAGE(xB, wB, xA, wA);
            if (g == 12) STAMP(1, 1);
        }
        BAR_L();
        {
            const char* qp = ws + WS_Q + (hd * 64 + qb * 8 + w) * 1536;
            asm volatile("global_load_dwordx4 %0, %1, %2 sc1" : "=v"(q1) : "v"(lane * 16), "s"(qp) : "memory");
            asm volatile("global_load_dwordx2 %0, %1, %2 offset:1024 sc1" : "=v"(q2) : "v"(r * 16 + (hd & 1) * 8), "s"(qp) : "memory");
        }
        G_STAGE(xA, wA, xB, wB);
        const float ra = *(LAS float*)LP(SCR + xrow * 4), rm = *(LAS float*)LP(SCR + 512 + xrow * 4);
        f32x4 e0[6], e1[6];
        G_MM(xf[0], wf[0][0], 0); G_MM(xf[0], wf[0][1], 1); G_MM(xf[0], wf[0][2], 2); G_MM(xf[1], wf[1][0], 0); G_MM(xf[1], wf[1][1], 1); G_MM(xf[1], wf[1][2], 2);
#pragma unroll
        for (int t = 0; t < 6; ++t) { e0[t] = *(LAS f32x4*)LP(SCR + 1536 + (fh * 96 + (t >> 1) * 32 + 8 * (t & 1) + 4 * h) * 4); e1[t] = *(LAS f32x4*)LP(SCR + 1536 + (fh * 96 + (t >> 1) * 32 + 16 + 8 * (t & 1) + 4 * h) * 4); }
        SBAR();
        G_MM(xf[2], wf[2][0], 0); G_MM(xB, wf[3][0], 0); G_MM(xf[2], wf[2][1], 1); G_MM(xB, wB[0], 1); G_MM(xf[2], wf[2][2], 2); G_MM(xB, wB[1], 2);
#undef G_RW
#undef G_MM
#undef G_STAGE
        STAMP(1, 3);
        const float a2 = ra * ra;
        const unsigned hp = WS_H + ((rb * 4 + tt) * 96 + cb * 6 + 3 * fh) * 1024 + lane * 16;
#pragma unroll
        for (int a = 0; a < 3; ++a) {
            u32x4 o;
#pragma unroll
            for (int g = 0; g < 2; ++g) {
                const f32x16& c = acc[a];
                const f32x4 gv = f32x4{c[4 * g], c[4 * g + 1], c[4 * g + 2], c[4 * g + 3]} + e0[2 * a + g] * rm;
                const f32x4 uv = (f32x4{c[8 + 4 * g], c[8 + 4 * g + 1], c[8 + 4 * g + 2], c[8 + 4 * g + 3]} + e1[2 * a + g] * rm) * a2;
                o[2 * g] = pk(__builtin_fmaxf(gv[0], 0.f) * uv[0], __builtin_fmaxf(gv[1], 0.f) * uv[1]);
                o[2 * g + 1] = pk(__builtin_fmaxf(gv[2], 0.f) * uv[2], __builtin_fmaxf(gv[3], 0.f) * uv[3]);
            }
            wst128(rs, hp + a * 1024, o);
        }
    }
    STAMP(1, 4);

    bf16x8 hf[12][2];
    const int ptt = w & 1, pkq = w >> 1, prot = 3 * (rb64 & 3);
    unsigned ho;
#define H_REQ(HI) hf[((HI) >> 1) % 12][(HI) & 1] = __builtin_bit_cast(bf16x8, __builtin_amdgcn_raw_buffer_load_b128(rs, ho + ((HI) & 1) * 256, \
        (((HI) >> 1) + prot >= 12 ? ((HI) >> 1) + prot - 12 : ((HI) >> 1) + prot) * 2048, 16))
    {
        const int lane = lane_id(), r = lane & 31, h = lane >> 5;
        f32x16 o = {};
        float l;
        asm volatile("s_waitcnt vmcnt(3)" : "+v"(q1), "+v"(q2) :: "memory");
        if (h) q2 = bf16x4{0, 0, 0, 0};
        const unsigned aK = lane * 16, aK2 = h ? 1536 + 320 : 1024 + r * 16 + (hd & 1) * 8, aV = 1536 + h * 384 + (r < 21 ? r : 21) * 16;
        const f32x16 zero = {};
        BAR_L();
        STAMP(2, 0);
        constexpr unsigned B0 = KVB + 49152;
        bf16x8 k1 = lds128(LP(B0 + aK)); bf16x4 k2 = lds64(LP(B0 + aK2));
        f32x16 sA = MFMA8(k2, q2, MFMA16(k1, q1, zero));
        { const float m = max16(sA); if (h) q2[0] = (short)(pk(-m, 0.f) & 0xffffu); }
        sA = MFMA8(k2, q2, MFMA16(k1, q1, zero));
        k1 = lds128(LP(B0 + 3072 + aK)); k2 = lds64(LP(B0 + 3072 + aK2));
        bf16x8 v0 = lds128(LP(B0 + aV)), v1 = v0;
        f32x16 sB;
        u32x4 p0, p1 = {0u, 0u, 0u, 0u};
        float e12 = 0.f, e13 = 0.f, e14 = 0.f, e15 = 0.f;
#define EX(v) __builtin_amdgcn_exp2f(v)
#define ATT_STEP(SC, SN, K1A, K2A, V1A, V0A, HI) do { \
        SN = MFMA16(k1, q1, zero); SBAR(); \
        k1 = lds128(K1A); \
        p1[2] = pk(e12, e13); p1[3] = pk(e14, e15); PIN(p1); \
        float e0 = EX(SC[0]), e1 = EX(SC[1]), e2 = EX(SC[2]), e3 = EX(SC[3]); PIN(e0); PIN(e1); PIN(e2); PIN(e3); SBAR(); \
        SN = MFMA8(k2, q2, SN); SBAR(); \
        k2 = lds64(K2A); \
        if ((HI) >= 0) H_REQ(HI); \
        p0[0] = pk(e0, e1); p0[1] = pk(e2, e3); PIN(p0); \
        float e4 = EX(SC[4]), e5 = EX(SC[5]), e6 = EX(SC[6]), e7 = EX(SC[7]); PIN(e4); PIN(e5); PIN(e6); PIN(e7); SBAR(); \
        o = MFMA16(v1, __builtin_bit_cast(bf16x8, p1), o); SBAR(); \
        v1 = lds128(V1A); \
        p0[2] = pk(e4, e5); p0[3] = pk(e6, e7); PIN(p0); \
        float e8 = EX(SC[8]), e9 = EX(SC[9]), e10 = EX(SC[10]), e11 = EX(SC[11]); PIN(e8); PIN(e9); PIN(e10); PIN(e11); SBAR(); \
        o = MFMA16(v0, __builtin_bit_cast(bf16x8, p0), o); SBAR(); \
        v0 = lds128(V0A); \
        p1[0] = pk(e8, e9); p1[1] = pk(e10, e11); PIN(p1); \
        e12 = EX(SC[12]); e13 = EX(SC[13]); e14 = EX(SC[14]); e15 = EX(SC[15]); PIN(e12); PIN(e13); PIN(e14); PIN(e15); SBAR(); \
    } while (0)
#define ATT_T(SC, SN, t, HI) ATT_STEP(SC, SN, cK + ((t) + 2) * 3072, cK2 + ((t) + 2) * 3072, cV + (t) * 3072 + 768, cV + ((t) + 1) * 3072, HI)
#define ATT_STAGE(H0, WAITC) do { \
        const unsigned nxt = cur == KVB + 49152 ? KVB : cur + 24576; \
        LAS char *cK = LP(aK + cur), *cK2 = LP(aK2 + cur), *cV = LP(aV + cur); \
        ATT_T(sA, sB, 0, H0); ATT_T(sB, sA, 1, (H0) < 0 ? -1 : (H0) + 1); ATT_T(sA, sB, 2, (H0) < 0 ? -1 : (H0) + 2); ATT_T(sB, sA, 3, (H0) < 0 ? -1 : (H0) + 3); \
        ATT_T(sA, sB, 4, (H0) < 0 ? -1 : (H0) + 4); ATT_T(sB, sA, 5, (H0) < 0 ? -1 : (H0) + 5); \
        WAITC; \
        cK = LP(aK + nxt); cK2 = LP(aK2 + nxt); \
        ATT_STEP(sA, sB, cK, cK2, cV + 6 * 3072 + 768, cV + 7 * 3072, (H0) < 0 ? -1 : (H0) + 6); \
        ATT_STEP(sB, sA, cK + 3072, cK2 + 3072, cV + 7 * 3072 + 768, LP(aV + nxt), (H0) < 0 ? -1 : (H0) + 7); \
        cur = nxt; } while (0)
        unsigned cur = B0;
#pragma unroll 1
        for (int s = 0; s < 6; ++s) ATT_STAGE(-1, VM_BAR(0));
        ho = WS_H + ((rb64 * 2 + ptt) * 96 + pkq * 24 + h) * 1024 + ((lane >> 4) & 1) * 512 + (lane & 15) * 16;
        ATT_STAGE(0, BAR());
        ATT_STAGE(8, BAR());
        p1[2] = pk(e12, e13); p1[3] = pk(e14, e15);
        o = MFMA16(v1, __builtin_bit_cast(bf16x8, p1), o);
#undef ATT_STEP
#undef ATT_T
#undef ATT_STAGE
        STAMP(2, 3);
        { auto rr = __builtin_amdgcn_permlane32_swap(__float_as_uint(o[8]), __float_as_uint(o[8]), false, false); l = __uint_as_float(rr[1]); }
        float am = 0.f;
#pragma unroll
        for (int e = 0; e < 12; ++e) am = __builtin_fmaxf(am, __builtin_fabsf(o[e]));
        const bool ok = (l > 5.4e-20f) && (l < 1.8e19f) && (am < 3.0e38f);
        if (__builtin_expect(!__all(ok), 0)) {
            float m = -INFINITY;
            o = zero;
            if (h) q2[0] = 0;
#pragma unroll 1
            for (int t = 0; t < 64; ++t) {
                const unsigned rec = WS_KV + (hd * 64 + t) * 3072;
                const bf16x8 fk1 = __builtin_bit_cast(bf16x8, wld128(rs, rec + aK)); const bf16x4 fk2 = __builtin_bit_cast(bf16x4, wld64(rs, rec + aK2));
                const bf16x8 fv0 = __builtin_bit_cast(bf16x8, wld128(rs, rec + aV)), fv1 = __builtin_bit_cast(bf16x8, wld128(rs, rec + aV + 768));
                f32x16 sf = MFMA8(fk2, q2, MFMA16(fk1, q1, zero));
                const float mn = __builtin_fmaxf(m, max16(sf));
                const float al = EX(m - mn);
                m = mn;
                u32x4 f0, f1;
#pragma unroll
                for (int e = 0; e < 16; ++e) o[e] *= al;
#pragma unroll
                for (int e = 0; e < 4; ++e) { f0[e] = pk(EX(sf[2 * e] - mn), EX(sf[2 * e + 1] - mn)); f1[e] = pk(EX(sf[8 + 2 * e] - mn), EX(sf[8 + 2 * e + 1] - mn)); }
                o = MFMA16(fv0, __builtin_bit_cast(bf16x8, f0), o); o = MFMA16(fv1, __builtin_bit_cast(bf16x8, f1), o);
            }
            auto rr = __builtin_amdgcn_permlane32_swap(__float_as_uint(o[8]), __float_as_uint(o[8]), false, false); l = __uint_as_float(rr[1]);
        }
#undef EX
        const float inv = __builtin_amdgcn_rcpf(l);
        const unsigned op = WS_O + (qb * 8 + w) * 40960;
        u32x4 oa;
#pragma unroll
        for (int e = 0; e < 4; ++e) oa[e] = pk(o[2 * e] * inv, o[2 * e + 1] * inv);
        wst128(rs, op + hd * 1024 + lane * 16, oa);
        wst64(rs, h ? OOB : op + 32768 + hd * 256 + r * 8, u32x2{pk(o[8] * inv, o[9] * inv), pk(o[10] * inv, o[11] * inv)});
    }

    {
        const int lane = lane_id(), r16 = lane & 15, q4 = lane >> 4;
        const unsigned paw = pkq * 5120 + (4 * q4 + ((lane >> 2) & 3)) * 160 + (lane & 3) * 40, awH = paw + 61440, awT = paw + 114688;
        f32x4 acc[2][5] = {};
        bf16x8 pf[2][5];
        u32x2 xv[3];
#define PO_A(rnd) (po_slot(rnd) < 61440 ? LP(paw) + po_slot(rnd) : po_slot(rnd) < 114688 ? LP(awH) + (po_slot(rnd) - 61440) : LP(awT) + (po_slot(rnd) - 114688))
#define PO_RW(rnd, nt) pf[(rnd) & 1][nt] = cat(ldstr(PO_A(rnd) + (nt) * 8), ldstr(PO_A(rnd) + (nt) * 8 + 2560))
        const size_t xo = (size_t)(rb64 * 64 + ptt * 32 + r16) * 1280 + cb80 * 160 + q4 * 40;
#pragma unroll
        for (int nt = 0; nt < 5; ++nt) PO_RW(0, nt);
        SFOR(q, 17)
            if constexpr (q == 2) asm volatile("s_waitcnt vmcnt(2)" ::: "memory");
            if constexpr (q <= 12) BAR_L();
            if constexpr (q == 0) STAMP(4, 0);
            if constexpr (q == 6) STAMP(4, 1);
            if constexpr (q >= 1 && q < 5) { H_REQ(14 + 2 * q); H_REQ(15 + 2 * q); }
            if constexpr (q == 6) {
#pragma unroll
                for (int k = 0; k < 3; ++k) { const int Q = (pkq + 4 * k) % 10, t = Q >= 5, nt = Q - 5 * t; xv[k] = *(const u32x2*)(x + xo + t * 20480 + nt * 8); }
            }
            if constexpr (q == 10) {
                STAMP(4, 2);
                const unsigned op = WS_O + (rb64 * 2 + ptt) * 40960;
#pragma unroll
                for (int s = 0; s < 4; ++s)
#pragma unroll
                    for (int t = 0; t < 2; ++t) hf[s][t] = __builtin_bit_cast(bf16x8, wld128(rs, op + (8 * pkq + 2 * s + (q4 >> 1)) * 1024 + (q4 & 1) * 512 + (t * 16 + r16) * 16));
#pragma unroll
                for (int t = 0; t < 2; ++t) {
                    const unsigned a = op + 32768 + (8 * pkq + 2 * q4) * 256 + (t * 16 + r16) * 8;
                    hf[4][t] = cat(__builtin_bit_cast(bf16x4, wld64(rs, a)), __builtin_bit_cast(bf16x4, wld64(rs, a + 256)));
                }
            }
            SBAR();
#pragma unroll
            for (int nt = 0; nt < 5; ++nt) {
#pragma unroll
                for (int t = 0; t < 2; ++t) {
                    acc[t][nt] = MFMA16X16(pf[q & 1][nt], hf[q % 12][t], acc[t][nt]); PIN(acc[t][nt]); SBAR();
                    if constexpr (q < 16) if (2 * nt + t < 5) PO_RW(q + 1, (2 * nt + t) % 5);
                    SBAR();
                }
            }
            if constexpr (q == 15) BAR_L();
        SEND
#undef PO_A
#undef PO_RW
        STAMP(4, 3);
#pragma unroll
        for (int Q = 0; Q < 10; ++Q) *(LAS f32x4*)(LP(w * 10240 + lane * 16) + Q * 1024) = acc[Q / 5][Q % 5];
        BAR_L();
#pragma unroll
        for (int k = 0; k < 3; ++k) {
            const int Q = pkq + 4 * k, t = Q >= 5, nt = Q - 5 * t;
            if (Q < 10) {
                f32x4 v = f32x4{bflo(xv[k][0]), bfhi(xv[k][0]), bflo(xv[k][1]), bfhi(xv[k][1])};
#pragma unroll
                for (int p = 0; p < 4; ++p) v += *(LAS f32x4*)(LP(ptt * 10240 + Q * 1024 + lane * 16) + p * 20480);
                *(u32x2*)(out + xo + t * 20480 + nt * 8) = u32x2{pk(v[0], v[1]), pk(v[2], v[3])};
            }
        }
    }
#undef H_REQ
    if (blockIdx.x == 0 && w == 0) {
        const unsigned long long t0 = __builtin_amdgcn_s_memrealtime();
        unsigned long long mn = ~0ull, mx = 0;
        for (int k = 0; k < 256; ++k) { const unsigned long long v = __hip_atomic_load((unsigned long long*)(ws + WS_DBG) + k, __ATOMIC_RELAXED, __HIP_MEMORY_SCOPE_AGENT); mn = v < mn ? v : mn; mx = v > mx ? v : mx; }
        long long dt = MEAS == 0 ? 0 : MEAS == 1 ? (long long)(t0 - mn) : (long long)(mx - mn);
        if (dt < 0) dt = 0; if (dt > 10000) dt = 10000;
        while (__builtin_amdgcn_s_memrealtime() - t0 < (unsigned long long)(30000 + 4 * dt)) __builtin_amdgcn_s_sleep(1);
    }
}

extern "C" void kernel_launch(void* const* d_in, const int* in_sizes, int n_in, void* d_out, int out_size, void* d_ws, size_t ws_size,
                              hipStream_t stream) {
    (void)hipFuncSetAttribute((const void*)k_block, hipFuncAttributeMaxDynamicSharedMemorySize, 163840);
    k_block<<<256, 768, 163840, stream>>>((const char*)d_in[0], (const char*)d_in[1], (const char*)d_in[2], (const char*)d_in[4], (const char*)d_in[5],
                                          (const char*)d_in[6], (char*)d_ws, (char*)d_out);
}
```

```cpp
#include <hip/hip_runtime.h>
#include <hip/hip_bf16.h>
#include <stdint.h>

typedef __attribute__((ext_vector_type(8))) short bf16x8;
typedef __attribute__((ext_vector_type(4))) short bf16x4;
typedef __attribute__((ext_vector_type(16))) float f32x16;
typedef __attribute__((ext_vector_type(4))) float f32x4;
typedef __attribute__((ext_vector_type(2))) float f32x2;
typedef __attribute__((ext_vector_type(4))) unsigned u32x4;
typedef __attribute__((ext_vector_type(2))) unsigned u32x2;
typedef __attribute__((ext_vector_type(2))) __bf16 bf2;
typedef __attribute__((address_space(1))) unsigned gu32;
typedef __amdgpu_buffer_rsrc_t rsrc_t;
#define LAS __attribute__((address_space(3)))
#define DI __device__ __forceinline__

constexpr unsigned WS_Q = 0;
constexpr unsigned WS_KV = WS_Q + 32 * 64 * 1536;
constexpr unsigned WS_H = WS_KV + 32 * 64 * 3072;
constexpr unsigned WS_O = WS_H + 64 * 96 * 1024;
constexpr unsigned WS_ST = WS_O + 64 * 40 * 1024;
constexpr unsigned WS_CQ = WS_ST + 16 * 10 * 128 * 16;
constexpr unsigned WS_CG = WS_CQ + 16 * 10 * 128 * 8;
constexpr unsigned WS_FLAG = WS_CG + 16 * 10 * 192 * 8;
constexpr unsigned WS_END = WS_FLAG + 65536;
constexpr unsigned OOB = 0x7fffff00u;
constexpr int F_GEN = 0;
constexpr int F_ACK = 64;
constexpr int F_H = 1024;
constexpr int F_O = 3072;
constexpr int F_QKV = 8192;
constexpr size_t WS_DBG = 64u << 20;
#define STAMP(kid, i) do {} while (0)

#define MFMA16(a, b, c) __builtin_amdgcn_mfma_f32_32x32x16_bf16((a), (b), (c), 0, 0, 0)
#define MFMA8(a, b, c) __builtin_amdgcn_mfma_f32_32x32x8bf16_1k((a), (b), (c), 0, 0, 0)
#define MFMA16X16(a, b, c) __builtin_amdgcn_mfma_f32_16x16x32_bf16((a), (b), (c), 0, 0, 0)
#define LP(a) ((LAS char*)(uintptr_t)(a))
#define SBAR() __builtin_amdgcn_sched_barrier(0)
#define PIN(x) asm volatile("" : "+v"(x))
#define LGKM0() __builtin_amdgcn_s_waitcnt(0xC07F)
#define BAR() asm volatile("s_barrier" ::: "memory")
#define BAR_L() do { asm volatile("" ::: "memory"); LGKM0(); BAR(); } while (0)
#define VM_BAR(N) asm volatile("s_waitcnt vmcnt(" #N ")\n\ts_barrier" ::: "memory")
#define DRAIN_BAR() asm volatile("s_waitcnt vmcnt(0) lgkmcnt(0)\n\ts_barrier" ::: "memory")
#define VM_BAR2(sel, NA, NB) asm volatile("s_cmp_eq_u32 %0, 0\n\ts_cbranch_scc1 1f\n\ts_waitcnt vmcnt(" #NB ")\n1:\n\ts_waitcnt vmcnt(" #NA ")\n\ts_barrier" :: "s"((int)(sel)) : "memory", "scc")

DI void set_m0(unsigned lds) { asm volatile("s_mov_b32 m0, %0\n\ts_nop 0" :: "s"(lds) : "memory"); }
#define GLDS(voff, sbase, OFF) asm volatile("global_load_lds_dwordx4 %0, %1 offset:" #OFF :: "v"(voff), "s"(sbase) : "memory")
#define GLDS_SC1(voff, sbase, OFF) asm volatile("global_load_lds_dwordx4 %0, %1 offset:" #OFF " sc1" :: "v"(voff), "s"(sbase) : "memory")

DI int lane_id() { int l; asm volatile("v_mbcnt_lo_u32_b32 %0, -1, 0\n\tv_mbcnt_hi_u32_b32 %0, -1, %0" : "=v"(l)); return l; }
DI bf16x8 lds128(LAS char* p) { return *(LAS bf16x8*)p; }
DI bf16x4 lds64(LAS char* p) { return *(LAS bf16x4*)p; }
DI bf16x4 ldstr(LAS char* p) { return __builtin_amdgcn_ds_read_tr16_b64_v4i16((LAS bf16x4*)p); }
DI bf16x8 cat(bf16x4 a, bf16x4 b) { return __builtin_shufflevector(a, b, 0, 1, 2, 3, 4, 5, 6, 7); }
DI unsigned pk(float a, float b) { f32x2 f = {a, b}; return __builtin_bit_cast(unsigned, __builtin_convertvector(f, bf2)); }
DI float bflo(unsigned u) { return __uint_as_float(u << 16); }
DI float bfhi(unsigned u) { return __uint_as_float(u & 0xffff0000u); }
DI float dot2(unsigned a, unsigned b, float c) { return __builtin_amdgcn_fdot2_f32_bf16(__builtin_bit_cast(bf2, a), __builtin_bit_cast(bf2, b), c, false); }
DI float swap32_add(float v) { auto r = __builtin_amdgcn_permlane32_swap(__float_as_uint(v), __float_as_uint(v), false, false); return __uint_as_float(r[0]) + __uint_as_float(r[1]); }
DI float swap32_max(float v) { auto r = __builtin_amdgcn_permlane32_swap(__float_as_uint(v), __float_as_uint(v), false, false); return __builtin_fmaxf(__uint_as_float(r[0]), __uint_as_float(r[1])); }
DI float addf(float a, float b) { float r; asm("v_add_f32 %0, %1, %2" : "=v"(r) : "v"(a), "v"(b)); return r; }
DI float mulf(float a, float b) { float r; asm("v_mul_f32 %0, %1, %2" : "=v"(r) : "v"(a), "v"(b)); return r; }
DI float relu(float a) { float r; asm("v_max_f32 %0, 0, %1" : "=v"(r) : "v"(a)); return r; }
DI f32x4 add4(f32x4 a, f32x4 b) { return f32x4{addf(a[0], b[0]), addf(a[1], b[1]), addf(a[2], b[2]), addf(a[3], b[3])}; }
DI void wst128(rsrc_t rs, unsigned off, u32x4 v) { __builtin_amdgcn_raw_buffer_store_b128(v, rs, off, 0, 16); }
DI void wst64(rsrc_t rs, unsigned off, u32x2 v) { __builtin_amdgcn_raw_buffer_store_b64(v, rs, off, 0, 16); }
DI void wst32(rsrc_t rs, unsigned off, unsigned v) { __builtin_amdgcn_raw_buffer_store_b32(v, rs, off, 0, 16); }
DI u32x4 wld128(rsrc_t rs, unsigned off) { return __builtin_amdgcn_raw_buffer_load_b128(rs, off, 0, 16); }
DI u32x2 wld64(rsrc_t rs, unsigned off) { return __builtin_amdgcn_raw_buffer_load_b64(rs, off, 0, 16); }
DI unsigned wld32(rsrc_t rs, unsigned off) { return __builtin_amdgcn_raw_buffer_load_b32(rs, off, 0, 16); }
#define POLL_SLEEP 4
DI void wait_tags(gu32* flags, int word, unsigned tag) {
    for (int spin = 0; spin < (1 << 18); ++spin) {
        const unsigned v = __hip_atomic_load(flags + word, __ATOMIC_RELAXED, __HIP_MEMORY_SCOPE_AGENT);
        if (__all(v == tag)) break;
        __builtin_amdgcn_s_sleep(POLL_SLEEP);
    }
    asm volatile("" ::: "memory");
}
DI void wait_tags2(gu32* flags, int word0, int word1, unsigned tag) {
    for (int spin = 0; spin < (1 << 18); ++spin) {
        const unsigned v0 = __hip_atomic_load(flags + word0, __ATOMIC_RELAXED, __HIP_MEMORY_SCOPE_AGENT);
        const unsigned v1 = __hip_atomic_load(flags + word1, __ATOMIC_RELAXED, __HIP_MEMORY_SCOPE_AGENT);
        if (__all(v0 == tag && v1 == tag)) break;
        __builtin_amdgcn_s_sleep(POLL_SLEEP);
    }
    asm volatile("" ::: "memory");
}
DI void publish(gu32* flags, int word, bool on, unsigned tag) { if (on) __hip_atomic_store(flags + word, tag, __ATOMIC_RELAXED, __HIP_MEMORY_SCOPE_AGENT); }
#define TAG_OF(gen) ((unsigned)__builtin_amdgcn_readfirstlane(gen) + 1u)

DI float max16(const f32x16& s) {
    float a = __builtin_fmaxf(__builtin_fmaxf(s[0], s[1]), s[2]), b = __builtin_fmaxf(__builtin_fmaxf(s[3], s[4]), s[5]);
    a = __builtin_fmaxf(__builtin_fmaxf(a, s[6]), s[7]); b = __builtin_fmaxf(__builtin_fmaxf(b, s[8]), s[9]);
    a = __builtin_fmaxf(__builtin_fmaxf(a, s[10]), s[11]); b = __builtin_fmaxf(__builtin_fmaxf(b, s[12]), s[13]);
    a = __builtin_fmaxf(__builtin_fmaxf(a, s[14]), s[15]);
    return swap32_max(__builtin_fmaxf(a, b));
}

template <int I> struct IC { static constexpr int v = I; };
template <int I, int N, class F> DI void sfor(F&& f) { if constexpr (I < N) { f(IC<I>{}); sfor<I + 1, N>(f); } }
#define SFOR(var, N) sfor<0, N>([&](auto var##_) __attribute__((always_inline)) { constexpr int var = decltype(var##_)::v;
#define SEND });
#define VM0() __builtin_amdgcn_s_waitcnt(0x0F70)

constexpr unsigned WRB = 81920, SCR = 155648, KVB = 0;
constexpr unsigned po_slot_f(int r) {
    constexpr int m[17] = {0, 1, 2, 3, 4, 5, 0, 1, 2, 5, 0, 1, 6, 3, 4, 2, 5};
    return m[r] < 4 ? 73728u + m[r] * 20480u : m[r] == 4 ? 49152u : m[r] == 5 ? 0u : 24576u;
}
template <int R> struct PoSlot { static constexpr unsigned v = po_slot_f(R); };
#define po_slot(r) (PoSlot<(r)>::v)
DI int first_stage(int j, int cb) { return ((((j >> 1) + 2 * (j & 1) + ((cb >> 2) & 1)) & 3) * 5 + 1) >> 1; }
DI int kstage(int kt, int rk) { const int k = kt + rk; return k >= 10 ? k - 10 : k; }
#define GATHER2(S0, S1, G, OFF, STRIDE) do { unsigned bad_ = 0; \
        _Pragma("unroll") for (int k_ = 0; k_ < 5; ++k_) { PIN(G[k_]); bad_ |= (G[k_][1] ^ tag) | (G[k_][3] ^ tag); } \
        S0 = addf(addf(addf(addf(__uint_as_float(G[0][0]), __uint_as_float(G[1][0])), __uint_as_float(G[2][0])), __uint_as_float(G[3][0])), __uint_as_float(G[4][0])); \
        S1 = addf(addf(addf(addf(__uint_as_float(G[0][2]), __uint_as_float(G[1][2])), __uint_as_float(G[2][2])), __uint_as_float(G[3][2])), __uint_as_float(G[4][2])); \
        bool ok_ = bad_ == 0; \
        for (int spin_ = 0; spin_ < (1 << 18) && __builtin_expect(!__all(ok_), 0); ++spin_) { \
            __builtin_amdgcn_s_sleep(POLL_SLEEP); S0 = 0.f; S1 = 0.f; ok_ = true; \
            _Pragma("unroll") for (int k_ = 0; k_ < 5; ++k_) { const u32x4 g_ = wld128(rs, (OFF) + k_ * (STRIDE)); \
                S0 += __uint_as_float(g_[0]); S1 += __uint_as_float(g_[2]); ok_ = ok_ && g_[1] == tag && g_[3] == tag; } } \
        S0 = swap32_add(S0); S1 = swap32_add(S1); } while (0)
#define GATHER_REQ(G, OFF, STRIDE) do { _Pragma("unroll") for (int k_ = 0; k_ < 5; ++k_) \
        asm volatile("global_load_dwordx4 %0, %1, %2 sc1" : "=v"(G[k_]) : "v"(OFF), "s"(ws + k_ * (STRIDE)) : "memory"); } while (0)
DI float fsum32(const bf16x8 (&f)[4]) {
    float a = 0.f, b = 0.f;
#pragma unroll
    for (int p = 0; p < 4; ++p) { const u32x4 u = __builtin_bit_cast(u32x4, f[p]); a = dot2(u[0], 0x3F803F80u, a); b = dot2(u[1], 0x3F803F80u, b); a = dot2(u[2], 0x3F803F80u, a); b = dot2(u[3], 0x3F803F80u, b); }
    return swap32_add(a + b);
}
DI unsigned x_addr(int row, int p, int h) { return row * 128 + (((2 * p + h) ^ ((row >> 1) & 7)) << 4); }
DI unsigned wq_addr(int lane, int nt, bool vt) {
    const int c = lane & 3;
    return WRB + (lane >> 5) * 2048 + (nt >> 1) * 512 + ((lane >> 2) & 3) * 128 + (((nt ^ (lane >> 3)) & 1) << 6) + ((lane >> 4) & 1) * 32 + (vt ? c : ((c & 1) << 1) | (c >> 1)) * 8;
}
DI unsigned gu_addr(int lane, int nt) {
    const int c = lane & 3, gi = (lane >> 4) & 1;
    return WRB + gi * 3072 + (lane >> 5) * 1536 + ((lane >> 2) & 3) * 192 + (nt >> 1) * 64 + (((nt ^ gi) & 1) << 5) + (((c & 1) << 1) | (c >> 1)) * 8;
}
#define VMW(N) asm volatile("s_waitcnt vmcnt(" #N ")" ::: "memory")
struct Ids { int rb, cb, hd, qb, rb64, cb80, rk, i, sx, sw; };

#define LOADER_SETUP() \
        const unsigned vA0 = (lane >> 3) * 1280 + (((lane & 7) ^ (lane >> 4)) << 4), vA1 = (vA0 ^ 64) + 10240 - 1024; \
        const unsigned vW0 = ((lane >> 3) & 3) * 3840 + (lane >> 5) * 128 + (((lane & 7) ^ ((lane >> 2) & 4)) << 4), vW1 = vW0 + 15360 - 1024; \
        const char* xa = x + (size_t)(rb * 128 + lw * 64) * 1280; \
        const char* wb = wqkv + (size_t)lw * 32 * 3840 + (cb < 15 ? cb : 14) * 256
#define H_4(V0, V1, P, STEP) do { GLDS(V0, P, 0); GLDS(V1, P, 1024); GLDS(V0, (P) + (STEP) - 2048, 2048); GLDS(V1, (P) + (STEP) - 2048, 3072); } while (0)
#define H_X(slot, ka) do { const char* p_ = xa + (ka) * 128; set_m0((slot) + lw * 8192); H_4(vA0, vA1, p_, 20480); set_m0((slot) + lw * 8192 + 4096); H_4(vA0, vA1, p_ + 40960, 20480); } while (0)
#define H_Q(xs, wsl, ka) do { H_X(xs, ka); const char* q_ = wb + (ka) * 245760; set_m0(WRB + (wsl) + lw * 8192); H_4(vW0, vW1, q_, 30720); \
        set_m0(WRB + (wsl) + lw * 8192 + 4096); H_4(vW0, vW1, q_ + 61440, 30720); } while (0)
#define NEXT_X(v) ((v) == 65536 ? 0u : (v) + 16384)
#define NEXT_W(v) ((v) == 49152 ? 0u : (v) + 24576)
DI void helper_role(const char* x, const char* wqkv, const char* wo, const char* wg, const char* wu, const char* wd, char* ws, rsrc_t rs, gu32* flags,
                    const Ids id, int lw) {
    const int w = lw;
#define HPRIO 3
    __builtin_amdgcn_s_setprio(HPRIO);
    unsigned gen, tag;
    asm volatile("global_load_dword %0, %1, %2 sc1" : "=v"(gen) : "v"(0), "s"(flags + F_GEN) : "memory");
    const int lane = lane_id(), r = lane & 31, h = lane >> 5;
    const int rb = id.rb, cb = id.cb, rk = id.rk, cbl = cb < 15 ? cb : 14;
    const unsigned voff = lane * 16;
    if (lw < 2) {
        LOADER_SETUP();
        H_Q(16384, 24576, rk + 1);
        SBAR();
        unsigned gv[3];
#pragma unroll
        for (int p = 0; p < 3; ++p) { const int P = 64 * p + lane, row = P / 12; gv[p] = row * 3072 + (((P - row * 12) ^ (2 * lw)) << 4) + 2048 - p * 1024; }
        const char* gb = (lw ? wu : wg) + cb * 192 - 2048;
#define H_G(wsl, ka) do { const char* q_ = gb + (size_t)(ka) * 196608; _Pragma("unroll") for (int st_ = 0; st_ < 4; ++st_) { \
        set_m0(WRB + (wsl) + st_ * 6144 + lw * 3072); GLDS(gv[0], q_ + st_ * 49152, 0); GLDS(gv[1], q_ + st_ * 49152, 1024); GLDS(gv[2], q_ + st_ * 49152, 2048); } } while (0)
        VM_BAR(16);
        H_Q(32768, 49152, kstage(2, rk));
        unsigned xs = 49152, wsl = 0; int ka = kstage(3, rk);
#pragma unroll 1
        for (int g = 0; g < 7; ++g) { VM_BAR(16); H_Q(xs, wsl, ka); xs = NEXT_X(xs); wsl = NEXT_W(wsl); ka = ka == 9 ? 0 : ka + 1; }
        VM_BAR(16);
        ka = kstage(5, rk);
#pragma unroll 1
        for (int g = 7; g < 12; ++g) { H_G(wsl, ka); wsl = NEXT_W(wsl); ka = ka == 9 ? 0 : ka + 1; VM_BAR(12); }
#pragma unroll 1
        for (int g = 12; g < 17; ++g) { H_X(xs, ka); H_G(wsl, ka); xs += 16384; wsl = NEXT_W(wsl); ka = ka == 9 ? 0 : ka + 1; VM_BAR(20); }
        VM_BAR(0);
        asm volatile("v_readfirstlane_b32 %0, %1" : "=s"(tag) : "v"(gen));
        tag += 1;
#undef H_G
    } else {
        const int sv = lw - 2;
        VM_BAR(0);
        asm volatile("v_readfirstlane_b32 %0, %1" : "=s"(tag) : "v"(gen));
        tag += 1;
        u32x4 gs[2][5], gc[5];
        const unsigned so = WS_ST + ((rb * 10 + h * 5) * 128 + sv * 64 + r) * 16, co = WS_CQ + ((cbl * 10 + h * 5) * 128 + sv * 64 + 2 * r) * 8;
        const unsigned cg = WS_CG + ((cb * 10 + h * 5) * 192 + sv * 64 + 2 * r) * 8;
        unsigned wsl = 0;
        bf16x8 fw[3][4];
#pragma unroll 1
        for (int g = 0; g < 4; ++g) {
            if (g == id.sw + 1 && cb < 15) {
                const float c0 = fsum32(fw[0]);
                const float c1 = fsum32(fw[1]);
                wst64(rs, WS_CQ + ((cb * 10 + kstage(id.sw, rk)) * 128 + sv * 64 + lane) * 8, u32x2{__float_as_uint(h ? c1 : c0), tag});
            }
            if ((g == id.sx || g == id.sx + 1) && id.sx < 3) {
                const int row = sv * 64 + (g - id.sx) * 32 + r;
                float sx = 0.f, sq = 0.f;
#pragma unroll
                for (int p = 0; p < 4; ++p) {
                    const u32x4 f = __builtin_bit_cast(u32x4, lds128(LP(x_addr(row, p, h) + id.sx * 16384)));
#pragma unroll
                    for (int e = 0; e < 4; ++e) { sx = dot2(f[e], 0x3F803F80u, sx); sq = dot2(f[e], f[e], sq); }
                }
                sx = swap32_add(sx); sq = swap32_add(sq);
                wst128(rs, h == 0 ? WS_ST + ((rb * 10 + kstage(id.sx, rk)) * 128 + row) * 16 : OOB, u32x4{__float_as_uint(sx), tag, __float_as_uint(sq), tag});
            }
            if (g == id.sw) {
#pragma unroll
                for (int t = 0; t < 2; ++t) {
                    const unsigned a = wq_addr(lane, sv * 2 + t, cb >= 10) + wsl;
#pragma unroll
                    for (int p = 0; p < 4; ++p) fw[t][p] = cat(ldstr(LP(a) + p * 4096), ldstr(LP(a) + p * 4096 + 1024));
                }
            }
            BAR_L();
            wsl = NEXT_W(wsl);
        }
        {
            const int vh = ((cb - 10) * 128 + 19) / 20 + (lane >> 3);
            const unsigned v = (lane & 1) ? 0u : 0x3F803F80u;
            const unsigned o = (cb >= 10 && cb < 15 && vh * 20 < (cb - 9) * 128) ? WS_KV + (vh * 64 + rb * 4 + sv * 2) * 3072 + 1536 + ((lane >> 1) & 3) * 384 + 320 + (lane & 1) * 16 : OOB;
            wst128(rs, o, u32x4{v, v, v, v}); wst128(rs, o + 3072, u32x4{v, v, v, v});
        }
        BAR();
        GATHER_REQ(gs[0], so, 2048);
        BAR();
        GATHER_REQ(gs[1], so + 512, 2048);
        BAR();
        GATHER_REQ(gc, co, 1024);
        BAR();
        float s0, s1;
        VMW(5);
#pragma unroll
        for (int t = 0; t < 2; ++t) {
            GATHER2(s0, s1, gs[t], so + t * 512, 2048);
            const float mu = s0 * (1.f / 640.f);
            *(LAS float*)LP(SCR + h * 512 + (sv * 64 + t * 32 + r) * 4) = h ? -mu : __builtin_amdgcn_rsqf(__builtin_fmaxf(__builtin_fmaf(-mu, mu, s1 * (1.f / 640.f)), 0.f) + 1e-5f);
        }
        BAR();
        VMW(0);
        GATHER2(s0, s1, gc, co, 1024);
        *(LAS f32x2*)LP(SCR + 1024 + (sv * 64 + 2 * r) * 4) = f32x2{s0, s1};
        BAR_L();
        wsl = 24576;
#pragma unroll 1
        for (int g = 0; g < 4; ++g) {
            if (g == id.sw + 1) {
                float c[3];
#pragma unroll
                for (int t = 0; t < 3; ++t) c[t] = fsum32(fw[t]);
                const unsigned o = WS_CG + ((cb * 10 + kstage(5 + id.sw, rk)) * 192 + sv * 96 + lane) * 8;
                wst64(rs, o, u32x2{__float_as_uint(h ? c[1] : c[0]), tag});
                wst64(rs, h == 0 ? o + 512 : OOB, u32x2{__float_as_uint(c[2]), tag});
            }
            if (g == id.sw) {
#pragma unroll
                for (int t = 0; t < 3; ++t) {
                    const unsigned a = gu_addr(lane, 3 * sv + t) + wsl;
#pragma unroll
                    for (int p = 0; p < 4; ++p) fw[t][p] = cat(ldstr(LP(a) + p * 6144), ldstr(LP(a) + p * 6144 + 768));
                }
            }
            BAR_L();
            wsl = NEXT_W(wsl);
        }
        BAR();
        if (sv == 0) {
            const int ty = cb / 5, cc = cb - ty * 5, gg = (r >> 1) * 5 + (r & 1) * 2 + 2 * h;
            const int blk = ty == 2 ? (r * 20 + 19 * h) >> 7 : gg >> 4;
            publish(flags, F_QKV + r * 128 + ((ty == 0 ? 4 : ty == 1 ? 0 : 2) + h) * 16 + rb, ty < 3 && blk == cc, tag);
            publish(flags, F_ACK + blockIdx.x, lane == 0, tag);
        }
        GATHER_REQ(gc, cg, 1536);
        BAR();
        GATHER_REQ(gs[0], cg - sv * 512 + 1024, 1536);
        BAR(); BAR();
        {
            VMW(0);
            GATHER2(s0, s1, gc, cg, 1536);
            *(LAS f32x2*)LP(SCR + 1536 + (sv * 64 + 2 * r) * 4) = f32x2{s0, s1};
            if (sv == 0) { GATHER2(s0, s1, gs[0], cg + 1024, 1536); *(LAS f32x2*)LP(SCR + 1536 + (128 + 2 * r) * 4) = f32x2{s0, s1}; }
        }
        BAR_L();
    }
    wait_tags2(flags, F_QKV + id.hd * 128 + lane, F_QKV + id.hd * 128 + 64 + (lane & 1) * 16 + id.qb * 2 + ((lane >> 1) & 1), tag);
    { const char* p_ = ws + WS_Q + (id.hd * 64 + id.qb * 8 + lw * 2) * 1536; set_m0(49152 + lw * 3072); GLDS_SC1(voff, p_, 0); GLDS_SC1(voff, p_, 1024); GLDS_SC1(voff, p_, 2048); }
    const char* kv = ws + WS_KV + (size_t)id.hd * 196608 + lw * 6144;
#define KV_DMA(s) do { const char* p_ = kv + (s) * 24576; set_m0(KVB + (s) % 3 * 24576 + lw * 6144); GLDS_SC1(voff, p_, 0); GLDS_SC1(voff, p_, 1024); GLDS_SC1(voff, p_, 2048); \
        set_m0(KVB + (s) % 3 * 24576 + lw * 6144 + 3072); GLDS_SC1(voff, p_ + 3072, 0); GLDS_SC1(voff, p_ + 3072, 1024); GLDS_SC1(voff, p_ + 3072, 2048); } while (0)
    KV_DMA(0); KV_DMA(1);
    unsigned pv[5];
#define PO_VOFF(ROW) do { _Pragma("unroll") for (int ii = 0; ii < 5; ++ii) { \
        const int P = ii * 64 + lane, rho = P / 10, c = P - rho * 10, u = rho >> 4, qq = (rho >> 2) & 3, e = rho & 3; pv[ii] = (ROW) * 1280 + c * 16 + 3072 - (ii & 3) * 1024; } } while (0)
#define PO_DMA(rnd, src) do { const char* p_ = (src); set_m0(po_slot(rnd) + lw * 5120); GLDS(pv[0], p_, 0); GLDS(pv[1], p_, 1024); GLDS(pv[2], p_, 2048); GLDS(pv[3], p_, 3072); \
        set_m0(po_slot(rnd) + lw * 5120 + 4096); GLDS(pv[4], p_, 0); } while (0)
#define PO_REG(k, src) do { const char* p_ = (src); _Pragma("unroll") for (int ii = 0; ii < 5; ++ii) \
        asm volatile("global_load_dwordx4 %0, %1, %2" : "=v"(pr[k][ii]) : "v"(pv[ii]), "s"(p_ + (ii & 3) * 1024) : "memory"); } while (0)
    PO_VOFF(8 * qq + 4 * u + e);
    const char* pw = wd + (size_t)lw * 491520 + id.cb80 * 160 - 3072;
    const char* po = wo + (size_t)lw * 204800 + id.cb80 * 160 - 3072;
    const int prot = 3 * (id.rb64 & 3);
#define PO_W(rnd) (pw + ((rnd) + prot >= 12 ? (rnd) + prot - 12 : (rnd) + prot) * 40960)
    u32x4 pr[6][5];
    *(LAS unsigned*)LP(SCR + 4092) = 0u;
    LGKM0(); VM_BAR(6);
    SFOR(s, 8)
        if constexpr (s == 1) if (lw == 0) publish(flags, F_H + (rb * 2 + lane) * 32 + cb, lane < 2, tag);
        if constexpr (s < 2) { PO_DMA(2 * s, PO_W(2 * s)); PO_DMA(2 * s + 1, PO_W(2 * s + 1)); }
        if constexpr (s >= 2 && s < 5) { PO_REG(2 * s - 4, PO_W(2 * s + 2)); PO_REG(2 * s - 3, PO_W(2 * s + 3)); }
        if constexpr (s == 3) {
            if (lw == 1) wait_tags(flags, F_H + id.rb64 * 32 + (lane & 15), tag);
            if (lw == 2 && blockIdx.x == 0) {
#pragma unroll 1
                for (int k = 0; k < 4; ++k) wait_tags(flags, F_ACK + k * 64 + lane, tag);
                publish(flags, F_GEN, lane == 0, tag);
            }
        }
        if constexpr (s == 5) {
            unsigned dummy;
            asm volatile("global_load_dword %0, %1, %2\n\ts_waitcnt vmcnt(0)" : "=&v"(dummy) : "v"((lane < 50 ? lane : 49) * 128), "s"(wo + (size_t)(id.i * 4 + lw) * 6400) : "memory");
        }
        if constexpr (s == 7) PO_DMA(4, PO_W(4));
        VM_BAR(0);
        if constexpr (s < 6) KV_DMA(s + 2);
    SEND
    PO_DMA(5, PO_W(5));
    PO_VOFF(20 * (qq >> 1) + 4 * (qq & 1) + 8 * u + e);
#define PO_PUT(rnd) do { _Pragma("unroll") for (int ii = 0; ii < 5; ++ii) { PIN(pr[(rnd) - 6][ii]); *(LAS u32x4*)(LP(po_slot(rnd) + lw * 5120 + voff) + ii * 1024) = pr[(rnd) - 6][ii]; } } while (0)
    BAR();
    PO_PUT(6); PO_DMA(12, po);
    LGKM0(); VM_BAR(5);
    PO_PUT(7); PO_PUT(8); PO_DMA(13, po + 51200);
    if (lw == 0) {
        for (int spin = 0; spin < (1 << 22) && __builtin_amdgcn_readfirstlane(*(volatile LAS unsigned*)LP(SCR + 4092)) != 8; ++spin) __builtin_amdgcn_s_sleep(1);
        publish(flags, F_O + (id.qb * 4 + lane) * 32 + id.hd, lane < 4, tag);
    }
    BAR_L();
    PO_PUT(9); PO_PUT(10);
    if (lw == 1) wait_tags(flags, F_O + id.rb64 * 32 + r, tag);
    PO_DMA(14, po + 2 * 51200);
    BAR_L();
    PO_PUT(11); PO_DMA(15, po + 3 * 51200);
    PO_VOFF(20 * (4 * (qq >> 1) + 2 * (qq & 1) + u) + 16 + e);
    LGKM0(); VM_BAR(15);
    PO_DMA(16, po);
    VM_BAR(10);
    VM_BAR(0);
#undef PO_PUT
#undef KV_DMA
#undef PO_VOFF
#undef PO_DMA
#undef PO_REG
#undef PO_W
}

template <bool VT>
DI void qkv_phase(char* ws, rsrc_t rs, int rb, int cb, int w) {
    const int lane = lane_id(), wm = w >> 2, wn = w & 3, r = lane & 31, h = lane >> 5, xrow = wm * 64 + r;
    unsigned ax[4];
#pragma unroll
    for (int p = 0; p < 4; ++p) ax[p] = x_addr(xrow, p, h);
    const unsigned aw = wq_addr(lane, wn, VT);
    const unsigned rec = cb < 5 ? 1536 : 3072;
    unsigned off[2];
    if (!VT) {
#pragma unroll
        for (int o = 0; o < 2; ++o) {
            const int Gc = (cb % 5) * 16 + wn * 4 + o * 2 + h, P = Gc / 5, gp = Gc - P * 5;
            off[o] = (cb < 5 ? WS_Q : WS_KV) + (rb * 4 + wm * 2) * rec + r * 16 + (2 * P + (gp > 2)) * 64 * rec + (gp > 2 ? gp - 3 : gp) * 512 + (gp == 2);
            PIN(off[o]);
        }
    } else {
        const int col = (cb - 10) * 128 + wn * 32 + r, vh = col / 20, d = col - vh * 20;
        off[0] = cb < 15 ? WS_KV + (vh * 64 + rb * 4 + wm * 2) * 3072 + 1536 + h * 384 + d * 16 : OOB;
        PIN(off[0]);
    }
    f32x16 acc[2] = {};
    bf16x8 xA[4][2], wA[4], xB[4][2], wB[4];
#define Q_MM(X, W, p, i) do { acc[i] = VT ? MFMA16(X[p][i], W[p], acc[i]) : MFMA16(W[p], X[p][i], acc[i]); PIN(acc[i]); SBAR(); } while (0)
#define Q_RW(p, A) cat(ldstr(LP(A) + (p) * 4096), ldstr(LP(A) + (p) * 4096 + 1024))
#define Q_STAGE(XC, WC, XN, WN) do { BAR_L(); const unsigned a_ = aw + wsl; \
        _Pragma("unroll") for (int p = 0; p < 4; ++p) { const unsigned b_ = ax[p] + xs; \
            XN[p][0] = lds128(LP(b_)); XN[p][1] = lds128(LP(b_) + 4096); if (p == 3) WN[3] = Q_RW(3, a_); SBAR(); Q_MM(XC, WC, p, 0); \
            if (p < 3) WN[p] = Q_RW(p, a_); SBAR(); Q_MM(XC, WC, p, 1); } \
        xs = NEXT_X(xs); wsl = NEXT_W(wsl); } while (0)
    BAR();
    STAMP(0, 1);
#pragma unroll
    for (int p = 0; p < 4; ++p) { xA[p][0] = lds128(LP(ax[p])); xA[p][1] = lds128(LP(ax[p]) + 4096); wA[p] = Q_RW(p, aw); }
    unsigned xs = 16384, wsl = 24576;
#pragma unroll 1
    for (int g = 0; g < 8; g += 2) {
        Q_STAGE(xA, wA, xB, wB); Q_STAGE(xB, wB, xA, wA);
        if (g == 2) STAMP(5, 1);
        if (g == 4) STAMP(5, 2);
    }
    STAMP(5, 3);
    Q_STAGE(xA, wA, xB, wB);
    BAR_L();
    STAMP(5, 4);
    f32x4 e0[VT ? 8 : 2], e1[VT ? 8 : 2];
    float sa[2], sb[2];
    Q_MM(xB, wB, 0, 0); Q_MM(xB, wB, 0, 1);
    if (!VT) {
#pragma unroll
        for (int i = 0; i < 2; ++i) { sa[i] = *(LAS float*)LP(SCR + (xrow + 32 * i) * 4); sb[i] = *(LAS float*)LP(SCR + 512 + (xrow + 32 * i) * 4); }
#pragma unroll
        for (int o = 0; o < 2; ++o) { e0[o] = *(LAS f32x4*)LP(SCR + 1024 + (wn * 32 + o * 16 + 4 * h) * 4); e1[o] = *(LAS f32x4*)LP(SCR + 1024 + (wn * 32 + o * 16 + 8 + 4 * h) * 4); }
    } else {
        sa[0] = *(LAS float*)LP(SCR + 1024 + (wn * 32 + r) * 4);
#pragma unroll
        for (int k = 0; k < (VT ? 8 : 2); ++k) { e0[k] = *(LAS f32x4*)LP(SCR + (wm * 64 + 8 * k + 4 * h) * 4); e1[k] = *(LAS f32x4*)LP(SCR + 512 + (wm * 64 + 8 * k + 4 * h) * 4); }
    }
    SBAR();
    Q_MM(xB, wB, 1, 0); Q_MM(xB, wB, 2, 0); Q_MM(xB, wB, 3, 0); Q_MM(xB, wB, 1, 1); Q_MM(xB, wB, 2, 1); Q_MM(xB, wB, 3, 1);
#undef Q_MM
#undef Q_RW
#undef Q_STAGE
    STAMP(0, 2);
#pragma unroll
    for (int i = 0; i < 2; ++i) {
        const f32x16& c = acc[i];
#pragma unroll
        for (int o = 0; o < 2; ++o) {
            const f32x4 c0 = {c[8 * o], c[8 * o + 1], c[8 * o + 2], c[8 * o + 3]}, c1 = {c[8 * o + 4], c[8 * o + 5], c[8 * o + 6], c[8 * o + 7]};
            if (!VT) {
                const float a = cb < 5 ? sa[i] * (0.22360679774997896f * 1.4426950408889634f) : sa[i];
                const f32x4 v0 = (c0 + e0[o] * sb[i]) * a, v1 = (c1 + e1[o] * sb[i]) * a;
                const u32x4 ov = {pk(v0[0], v0[1]), pk(v0[2], v0[3]), pk(v1[0], v1[1]), pk(v1[2], v1[3])};
                wst128(rs, (off[o] & ~1u) + i * rec, ov);
                if (off[o] & 1) wst128(rs, off[o] - 1 + (64 + i) * rec, ov);
            } else {
                constexpr int M = VT ? 7 : 1;
                const f32x4 v0 = (c0 + e1[(4 * i + 2 * o) & M] * sa[0]) * e0[(4 * i + 2 * o) & M], v1 = (c1 + e1[(4 * i + 2 * o + 1) & M] * sa[0]) * e0[(4 * i + 2 * o + 1) & M];
                wst128(rs, off[0] + i * 3072 + o * 768, u32x4{pk(v0[0], v0[1]), pk(v0[2], v0[3]), pk(v1[0], v1[1]), pk(v1[2], v1[3])});
            }
        }
    }
}

extern "C" __global__ __launch_bounds__(768, 3) void k_block(const char* __restrict__ x, const char* __restrict__ wqkv, const char* __restrict__ wo,
                                                             const char* __restrict__ wg, const char* __restrict__ wu, const char* __restrict__ wd,
                                                             char* __restrict__ ws, char* __restrict__ out) {
    extern __shared__ __attribute__((aligned(16))) char smem[];
    const int b = blockIdx.x, j = b & 7, i = b >> 3;
    const int w = __builtin_amdgcn_readfirstlane(threadIdx.x >> 6);
    const rsrc_t rs = __builtin_amdgcn_make_buffer_rsrc(ws, 0, WS_END, 0x00020000);
    const int rb = 4 * (j >> 1) + (i & 3), cb = (j & 1) * 8 + (i >> 2);
    const int hd = j * 4 + (i >> 3), qb = i & 7;
    const int rb64 = j * 4 + (i & 3), cb80 = i >> 2;
    if (w == 8 || w == 9) {
        const int lane = lane_id(), lw = w - 8, rk = first_stage(j, cb);
        LOADER_SETUP();
        H_Q(0, 0, rk);
    }
    if (w >= 8) {
        const Ids id = {rb, cb, hd, qb, rb64, cb80, first_stage(j, cb), i, cb & 3, rb & 3};
        helper_role(x, wqkv, wo, wg, wu, wd, ws, rs, (gu32*)(ws + WS_FLAG), id, w - 8);
        return;
    }
    STAMP(0, 0);
    if (cb < 10) qkv_phase<false>(ws, rs, rb, cb, w); else qkv_phase<true>(ws, rs, rb, cb, w);
    STAMP(0, 3);

    {
        const int lane = lane_id(), tt = w & 3, fh = w >> 2, r = lane & 31, h = lane >> 5, xrow = tt * 32 + r;
        unsigned ax[4];
#pragma unroll
        for (int p = 0; p < 4; ++p) ax[p] = x_addr(xrow, p, h);
        const unsigned awA = gu_addr(lane, 3 * fh), awB = gu_addr(lane, 3 * fh + 1);
        f32x16 acc[3] = {};
        bf16x8 xf[3], wf[4][3], xA, wA[2], xB, wB[2];
#define G_RW(p, a, A, B) ((a) == 1 ? cat(ldstr(LP(B) + (p) * 6144), ldstr(LP(B) + (p) * 6144 + 768)) : cat(ldstr(LP(A) + (p) * 6144 + (a) * 32), ldstr(LP(A) + (p) * 6144 + (a) * 32 + 768)))
#define G_MM(X, W, a) do { acc[a] = MFMA16(W, X, acc[a]); PIN(acc[a]); SBAR(); } while (0)
#define G_STAGE(XC, WC, XN, WN) do { const unsigned a_ = awA + wsl, c_ = awB + wsl; \
        WN[0] = G_RW(3, 1, a_, c_); SBAR(); \
        _Pragma("unroll") for (int p = 0; p < 3; ++p) { \
            G_MM(xf[p], wf[p][0], 0); wf[p][0] = G_RW(p, 0, a_, c_); if (p == 0) XN = lds128(LP(ax[3] + xs)); if (p == 1) WN[1] = G_RW(3, 2, a_, c_); SBAR(); \
            G_MM(xf[p], wf[p][1], 1); wf[p][1] = G_RW(p, 1, a_, c_); SBAR(); \
            G_MM(xf[p], wf[p][2], 2); wf[p][2] = G_RW(p, 2, a_, c_); xf[p] = lds128(LP(ax[p] + xs)); SBAR(); } \
        G_MM(XC, wf[3][0], 0); wf[3][0] = G_RW(3, 0, a_, c_); SBAR(); G_MM(XC, WC[0], 1); G_MM(XC, WC[1], 2); \
        xs = NEXT_X(xs); wsl = NEXT_W(wsl); } while (0)
        STAMP(1, 0);
#pragma unroll
        for (int p = 0; p < 3; ++p) {
            xf[p] = lds128(LP(ax[p]));
#pragma unroll
            for (int a = 0; a < 3; ++a) wf[p][a] = G_RW(p, a, awA + 24576, awB + 24576);
        }
        xA = lds128(LP(ax[3])); wf[3][0] = G_RW(3, 0, awA + 24576, awB + 24576); wA[0] = G_RW(3, 1, awA + 24576, awB + 24576); wA[1] = G_RW(3, 2, awA + 24576, awB + 24576);
        unsigned xs = 16384, wsl = 49152;
#pragma unroll 1
        for (int g = 10; g < 18; g += 2) {
            BAR_L();
            STAMP(3, (g - 10) >> 1);
            G_STAGE(xA, wA, xB, wB);
            BAR_L();
            G_STAGE(xB, wB, xA, wA);
            if (g == 12) VM0();
        }
        STAMP(3, 4);
        BAR_L();
        STAMP(3, 5);
        G_STAGE(xA, wA, xB, wB);
        const float ra = *(LAS float*)LP(SCR + xrow * 4), rm = *(LAS float*)LP(SCR + 512 + xrow * 4);
        f32x4 e0[6], e1[6];
        G_MM(xf[0], wf[0][0], 0); G_MM(xf[0], wf[0][1], 1); G_MM(xf[0], wf[0][2], 2); G_MM(xf[1], wf[1][0], 0); G_MM(xf[1], wf[1][1], 1); G_MM(xf[1], wf[1][2], 2);
#pragma unroll
        for (int t = 0; t < 6; ++t) { e0[t] = *(LAS f32x4*)LP(SCR + 1536 + (fh * 96 + (t >> 1) * 32 + 8 * (t & 1) + 4 * h) * 4); e1[t] = *(LAS f32x4*)LP(SCR + 1536 + (fh * 96 + (t >> 1) * 32 + 16 + 8 * (t & 1) + 4 * h) * 4); }
        SBAR();
        G_MM(xf[2], wf[2][0], 0); G_MM(xB, wf[3][0], 0); G_MM(xf[2], wf[2][1], 1); G_MM(xB, wB[0], 1); G_MM(xf[2], wf[2][2], 2); G_MM(xB, wB[1], 2);
#undef G_RW
#undef G_MM
#undef G_STAGE
        STAMP(1, 3);
        const float a2 = ra * ra;
        const unsigned hp = WS_H + ((rb * 4 + tt) * 96 + cb * 6 + 3 * fh) * 1024 + lane * 16;
#pragma unroll
        for (int a = 0; a < 3; ++a) {
            u32x4 o;
#pragma unroll
            for (int g = 0; g < 2; ++g) {
                const f32x16& c = acc[a];
                const f32x4 gv = f32x4{c[4 * g], c[4 * g + 1], c[4 * g + 2], c[4 * g + 3]} + e0[2 * a + g] * rm;
                const f32x4 uv = (f32x4{c[8 + 4 * g], c[8 + 4 * g + 1], c[8 + 4 * g + 2], c[8 + 4 * g + 3]} + e1[2 * a + g] * rm) * a2;
                o[2 * g] = pk(__builtin_fmaxf(gv[0], 0.f) * uv[0], __builtin_fmaxf(gv[1], 0.f) * uv[1]);
                o[2 * g + 1] = pk(__builtin_fmaxf(gv[2], 0.f) * uv[2], __builtin_fmaxf(gv[3], 0.f) * uv[3]);
            }
            wst128(rs, hp + a * 1024, o);
        }
    }
    STAMP(1, 4);

    bf16x8 hf[12][2];
    const int ptt = w & 1, pkq = w >> 1, prot = 3 * (rb64 & 3);
    unsigned ho;
#define H_REQ(HI) hf[((HI) >> 1) % 12][(HI) & 1] = __builtin_bit_cast(bf16x8, __builtin_amdgcn_raw_buffer_load_b128(rs, ho + ((HI) & 1) * 256, \
        (((HI) >> 1) + prot >= 12 ? ((HI) >> 1) + prot - 12 : ((HI) >> 1) + prot) * 2048, 16))
    {
        const int lane = lane_id(), r = lane & 31, h = lane >> 5;
        f32x16 o = {};
        float l;
        const unsigned aK = lane * 16, aK2 = h ? 1536 + 320 : 1024 + r * 16 + (hd & 1) * 8, aV = 1536 + h * 384 + (r < 21 ? r : 21) * 16;
        const f32x16 zero = {};
        BAR_L();
        STAMP(2, 0);
        const bf16x8 q1 = lds128(LP(49152 + w * 1536 + lane * 16));
        bf16x4 q2 = lds64(LP(49152 + w * 1536 + 1024 + r * 16 + (hd & 1) * 8));
        if (h) q2 = bf16x4{0, 0, 0, 0};
        constexpr unsigned B0 = KVB;
        bf16x8 k1 = lds128(LP(B0 + aK)); bf16x4 k2 = lds64(LP(B0 + aK2));
        f32x16 sA = MFMA8(k2, q2, MFMA16(k1, q1, zero));
        { const float m = max16(sA); if (h) q2[0] = (short)(pk(-m, 0.f) & 0xffffu); }
        sA = MFMA8(k2, q2, MFMA16(k1, q1, zero));
        k1 = lds128(LP(B0 + 3072 + aK)); k2 = lds64(LP(B0 + 3072 + aK2));
        bf16x8 v0 = lds128(LP(B0 + aV)), v1 = v0;
        f32x16 sB;
        u32x4 p0, p1 = {0u, 0u, 0u, 0u};
        float e12 = 0.f, e13 = 0.f, e14 = 0.f, e15 = 0.f;
#define EX(v) __builtin_amdgcn_exp2f(v)
#define ATT_STEP(SC, SN, K1A, K2A, V1A, V0A, HI) do { \
        SN = MFMA16(k1, q1, zero); SBAR(); \
        k1 = lds128(K1A); \
        p1[2] = pk(e12, e13); p1[3] = pk(e14, e15); PIN(p1); \
        float e0 = EX(SC[0]), e1 = EX(SC[1]), e2 = EX(SC[2]), e3 = EX(SC[3]); PIN(e0); PIN(e1); PIN(e2); PIN(e3); SBAR(); \
        SN = MFMA8(k2, q2, SN); SBAR(); \
        k2 = lds64(K2A); \
        if ((HI) >= 0) H_REQ(HI); \
        p0[0] = pk(e0, e1); p0[1] = pk(e2, e3); PIN(p0); \
        float e4 = EX(SC[4]), e5 = EX(SC[5]), e6 = EX(SC[6]), e7 = EX(SC[7]); PIN(e4); PIN(e5); PIN(e6); PIN(e7); SBAR(); \
        o = MFMA16(v1, __builtin_bit_cast(bf16x8, p1), o); SBAR(); \
        v1 = lds128(V1A); \
        p0[2] = pk(e4, e5); p0[3] = pk(e6, e7); PIN(p0); \
        float e8 = EX(SC[8]), e9 = EX(SC[9]), e10 = EX(SC[10]), e11 = EX(SC[11]); PIN(e8); PIN(e9); PIN(e10); PIN(e11); SBAR(); \
        o = MFMA16(v0, __builtin_bit_cast(bf16x8, p0), o); SBAR(); \
        v0 = lds128(V0A); \
        p1[0] = pk(e8, e9); p1[1] = pk(e10, e11); PIN(p1); \
        e12 = EX(SC[12]); e13 = EX(SC[13]); e14 = EX(SC[14]); e15 = EX(SC[15]); PIN(e12); PIN(e13); PIN(e14); PIN(e15); SBAR(); \
    } while (0)
#define ATT_T(SC, SN, t, HI) ATT_STEP(SC, SN, cK + ((t) + 2) * 3072, cK2 + ((t) + 2) * 3072, cV + (t) * 3072 + 768, cV + ((t) + 1) * 3072, HI)
#define ATT_STAGE(H0, WAITC) do { \
        const unsigned nxt = cur == KVB + 49152 ? KVB : cur + 24576; \
        LAS char *cK = LP(aK + cur), *cK2 = LP(aK2 + cur), *cV = LP(aV + cur); \
        ATT_T(sA, sB, 0, H0); ATT_T(sB, sA, 1, (H0) < 0 ? -1 : (H0) + 1); ATT_T(sA, sB, 2, (H0) < 0 ? -1 : (H0) + 2); ATT_T(sB, sA, 3, (H0) < 0 ? -1 : (H0) + 3); \
        ATT_T(sA, sB, 4, (H0) < 0 ? -1 : (H0) + 4); ATT_T(sB, sA, 5, (H0) < 0 ? -1 : (H0) + 5); \
        WAITC; \
        cK = LP(aK + nxt); cK2 = LP(aK2 + nxt); \
        ATT_STEP(sA, sB, cK, cK2, cV + 6 * 3072 + 768, cV + 7 * 3072, (H0) < 0 ? -1 : (H0) + 6); \
        ATT_STEP(sB, sA, cK + 3072, cK2 + 3072, cV + 7 * 3072 + 768, LP(aV + nxt), (H0) < 0 ? -1 : (H0) + 7); \
        cur = nxt; } while (0)
        unsigned cur = B0;
#pragma unroll 1
        for (int s = 0; s < 6; ++s) ATT_STAGE(-1, VM_BAR(0));
        ho = WS_H + ((rb64 * 2 + ptt) * 96 + pkq * 24 + h) * 1024 + ((lane >> 4) & 1) * 512 + (lane & 15) * 16;
        ATT_STAGE(0, BAR());
        ATT_STAGE(8, BAR());
        p1[2] = pk(e12, e13); p1[3] = pk(e14, e15);
        o = MFMA16(v1, __builtin_bit_cast(bf16x8, p1), o);
#undef ATT_STEP
#undef ATT_T
#undef ATT_STAGE
        STAMP(2, 3);
        { auto rr = __builtin_amdgcn_permlane32_swap(__float_as_uint(o[8]), __float_as_uint(o[8]), false, false); l = __uint_as_float(rr[1]); }
        float am = 0.f;
#pragma unroll
        for (int e = 0; e < 12; ++e) am = __builtin_fmaxf(am, __builtin_fabsf(o[e]));
        const bool ok = (l > 5.4e-20f) && (l < 1.8e19f) && (am < 3.0e38f);
        if (__builtin_expect(!__all(ok), 0)) {
            float m = -INFINITY;
            o = zero;
            if (h) q2[0] = 0;
#pragma unroll 1
            for (int t = 0; t < 64; ++t) {
                const unsigned rec = WS_KV + (hd * 64 + t) * 3072;
                const bf16x8 fk1 = __builtin_bit_cast(bf16x8, wld128(rs, rec + aK)); const bf16x4 fk2 = __builtin_bit_cast(bf16x4, wld64(rs, rec + aK2));
                const bf16x8 fv0 = __builtin_bit_cast(bf16x8, wld128(rs, rec + aV)), fv1 = __builtin_bit_cast(bf16x8, wld128(rs, rec + aV + 768));
                f32x16 sf = MFMA8(fk2, q2, MFMA16(fk1, q1, zero));
                const float mn = __builtin_fmaxf(m, max16(sf));
                const float al = EX(m - mn);
                m = mn;
                u32x4 f0, f1;
#pragma unroll
                for (int e = 0; e < 16; ++e) o[e] *= al;
#pragma unroll
                for (int e = 0; e < 4; ++e) { f0[e] = pk(EX(sf[2 * e] - mn), EX(sf[2 * e + 1] - mn)); f1[e] = pk(EX(sf[8 + 2 * e] - mn), EX(sf[8 + 2 * e + 1] - mn)); }
                o = MFMA16(fv0, __builtin_bit_cast(bf16x8, f0), o); o = MFMA16(fv1, __builtin_bit_cast(bf16x8, f1), o);
            }
            auto rr = __builtin_amdgcn_permlane32_swap(__float_as_uint(o[8]), __float_as_uint(o[8]), false, false); l = __uint_as_float(rr[1]);
        }
#undef EX
        const float inv = __builtin_amdgcn_rcpf(l);
        const unsigned op = WS_O + (qb * 8 + w) * 40960;
        u32x4 oa;
#pragma unroll
        for (int e = 0; e < 4; ++e) oa[e] = pk(o[2 * e] * inv, o[2 * e + 1] * inv);
        wst128(rs, op + hd * 1024 + lane * 16, oa);
        wst64(rs, h ? OOB : op + 32768 + hd * 256 + r * 8, u32x2{pk(o[8] * inv, o[9] * inv), pk(o[10] * inv, o[11] * inv)});
    }

    {
        const int lane = lane_id(), r16 = lane & 15, q4 = lane >> 4;
        const unsigned paw = pkq * 5120 + (4 * q4 + ((lane >> 2) & 3)) * 160 + (lane & 3) * 40, awH = paw + 49152, awT = paw + 114688;
        f32x4 acc[2][5] = {};
        bf16x8 pf[2][5];
        u32x4 xv[2];
#define PO_A(rnd) (po_slot(rnd) < 49152 ? LP(paw) + po_slot(rnd) : po_slot(rnd) < 114688 ? LP(awH) + (po_slot(rnd) - 49152) : LP(awT) + (po_slot(rnd) - 114688))
#define PO_RW(rnd, nt) pf[(rnd) & 1][nt] = cat(ldstr(PO_A(rnd) + (nt) * 8), ldstr(PO_A(rnd) + (nt) * 8 + 2560))
        unsigned fo[2], fl[2];
#pragma unroll
        for (int nt = 0; nt < 5; ++nt) PO_RW(0, nt);
        SFOR(q, 17)
            if constexpr (q == 5)
                asm volatile("s_waitcnt vmcnt(8)\n\ts_mov_b64 exec, 1\n\tds_add_u32 %0, %1\n\ts_mov_b64 exec, -1" :: "v"(SCR + 4092), "v"(1u) : "memory");
            if constexpr (q == 0 || (q >= 4 && q <= 14 && !(q & 1))) BAR_L();
            if constexpr (q == 0) STAMP(4, 0);
            if constexpr (q == 6) STAMP(4, 1);
            if constexpr (q >= 1 && q < 5) { H_REQ(14 + 2 * q); H_REQ(15 + 2 * q); }
            if constexpr (q == 6) {
#pragma unroll
                for (int k = 0; k < 2; ++k) {
                    const int it = (k ? 512 + (w & 1) * 64 : w * 64) + lane, row = it / 10, c = it - row * 10;
                    fo[k] = (rb64 * 64 + row) * 1280 + cb80 * 160 + c * 16; fl[k] = row * 336 + c * 32;
                    xv[k] = *(const u32x4*)(x + fo[k]);
                }
            }
            if constexpr (q == 8) {
                STAMP(4, 2);
                const unsigned op = WS_O + (rb64 * 2 + ptt) * 40960;
#pragma unroll
                for (int s = 0; s < 4; ++s)
#pragma unroll
                    for (int t = 0; t < 2; ++t) hf[s][t] = __builtin_bit_cast(bf16x8, wld128(rs, op + (8 * pkq + 2 * s + (q4 >> 1)) * 1024 + (q4 & 1) * 512 + (t * 16 + r16) * 16));
#pragma unroll
                for (int t = 0; t < 2; ++t) {
                    const unsigned a = op + 32768 + (8 * pkq + 2 * q4) * 256 + (t * 16 + r16) * 8;
                    hf[4][t] = cat(__builtin_bit_cast(bf16x4, wld64(rs, a)), __builtin_bit_cast(bf16x4, wld64(rs, a + 256)));
                }
            }
            SBAR();
#pragma unroll
            for (int nt = 0; nt < 5; ++nt) {
#pragma unroll
                for (int t = 0; t < 2; ++t) {
                    acc[t][nt] = MFMA16X16(pf[q & 1][nt], hf[q % 12][t], acc[t][nt]); PIN(acc[t][nt]); SBAR();
                    if constexpr (q < 16) if (2 * nt + t < 5) PO_RW(q + 1, (2 * nt + t) % 5);
                    SBAR();
                }
            }
            if constexpr (q == 15) BAR_L();
        SEND
#undef PO_A
#undef PO_RW
        STAMP(4, 3);
#pragma unroll
        for (int Q = 0; Q < 10; ++Q) *(LAS f32x4*)(LP(pkq * 21504 + (ptt * 32 + r16) * 336 + q4 * 80) + (Q / 5) * 5376 + (Q % 5) * 16) = acc[Q / 5][Q % 5];
        BAR_L();
#pragma unroll
        for (int k = 0; k < 2; ++k) {
            if (k == 0 || w < 2) {
                f32x4 v0 = {bflo(xv[k][0]), bfhi(xv[k][0]), bflo(xv[k][1]), bfhi(xv[k][1])}, v1 = {bflo(xv[k][2]), bfhi(xv[k][2]), bflo(xv[k][3]), bfhi(xv[k][3])};
#pragma unroll
                for (int p = 0; p < 4; ++p) { v0 += *(LAS f32x4*)(LP(fl[k]) + p * 21504); v1 += *(LAS f32x4*)(LP(fl[k]) + p * 21504 + 16); }
                const u32x4 ov = {pk(v0[0], v0[1]), pk(v0[2], v0[3]), pk(v1[0], v1[1]), pk(v1[2], v1[3])};
                *(u32x4*)(out + fo[k]) = ov;
            }
        }
    }
#undef H_REQ
    STAMP(4, 4);
}

extern "C" void kernel_launch(void* const* d_in, const int* in_sizes, int n_in, void* d_out, int out_size, void* d_ws, size_t ws_size,
                              hipStream_t stream) {
    (void)hipFuncSetAttribute((const void*)k_block, hipFuncAttributeMaxDynamicSharedMemorySize, 163840);
    k_block<<<256, 768, 163840, stream>>>((const char*)d_in[0], (const char*)d_in[1], (const char*)d_in[2], (const char*)d_in[4], (const char*)d_in[5],
                                          (const char*)d_in[6], (char*)d_ws, (char*)d_out);
}
```

```cpp
#include <hip/hip_runtime.h>
#include <hip/hip_bf16.h>
#include <stdint.h>

typedef __attribute__((ext_vector_type(8))) short bf16x8;
typedef __attribute__((ext_vector_type(4))) short bf16x4;
typedef __attribute__((ext_vector_type(16))) float f32x16;
typedef __attribute__((ext_vector_type(4))) float f32x4;
typedef __attribute__((ext_vector_type(2))) float f32x2;
typedef __attribute__((ext_vector_type(4))) unsigned u32x4;
typedef __attribute__((ext_vector_type(2))) unsigned u32x2;
typedef __attribute__((ext_vector_type(2))) __bf16 bf2;
typedef __attribute__((address_space(1))) unsigned gu32;
typedef __amdgpu_buffer_rsrc_t rsrc_t;
#define LAS __attribute__((address_space(3)))
#define DI __device__ __forceinline__

constexpr unsigned WS_Q = 0;
constexpr unsigned WS_KV = WS_Q + 32 * 64 * 1536;
constexpr unsigned WS_H = WS_KV + 32 * 64 * 3072;
constexpr unsigned WS_O = WS_H + 64 * 96 * 1024;
constexpr unsigned WS_ST = WS_O + 64 * 40 * 1024;
constexpr unsigned WS_CQ = WS_ST + 16 * 10 * 128 * 16;
constexpr unsigned WS_CG = WS_CQ + 16 * 10 * 128 * 8;
constexpr unsigned WS_FLAG = WS_CG + 16 * 10 * 192 * 8;
constexpr unsigned WS_END = WS_FLAG + 65536;
constexpr unsigned OOB = 0x7fffff00u;
constexpr int F_GEN = 0;
constexpr int F_ACK = 64;
constexpr int F_H = 1024;
constexpr int F_O = 3072;
constexpr int F_QKV = 8192;
constexpr size_t WS_DBG = 64u << 20;
#define STAMP(kid, i) do {} while (0)

#define MFMA16(a, b, c) __builtin_amdgcn_mfma_f32_32x32x16_bf16((a), (b), (c), 0, 0, 0)
#define MFMA8(a, b, c) __builtin_amdgcn_mfma_f32_32x32x8bf16_1k((a), (b), (c), 0, 0, 0)
#define MFMA16X16(a, b, c) __builtin_amdgcn_mfma_f32_16x16x32_bf16((a), (b), (c), 0, 0, 0)
#define LP(a) ((LAS char*)(uintptr_t)(a))
#define SBAR() __builtin_amdgcn_sched_barrier(0)
#define PIN(x) asm volatile("" : "+v"(x))
#define LGKM0() __builtin_amdgcn_s_waitcnt(0xC07F)
#define BAR() asm volatile("s_barrier" ::: "memory")
#define BAR_L() do { asm volatile("" ::: "memory"); LGKM0(); BAR(); } while (0)
#define VM_BAR(N) asm volatile("s_waitcnt vmcnt(" #N ")\n\ts_barrier" ::: "memory")
#define DRAIN_BAR() asm volatile("s_waitcnt vmcnt(0) lgkmcnt(0)\n\ts_barrier" ::: "memory")
#define VM_BAR2(sel, NA, NB) asm volatile("s_cmp_eq_u32 %0, 0\n\ts_cbranch_scc1 1f\n\ts_waitcnt vmcnt(" #NB ")\n1:\n\ts_waitcnt vmcnt(" #NA ")\n\ts_barrier" :: "s"((int)(sel)) : "memory", "scc")

DI void set_m0(unsigned lds) { asm volatile("s_mov_b32 m0, %0\n\ts_nop 0" :: "s"(lds) : "memory"); }
#define GLDS(voff, sbase, OFF) asm volatile("global_load_lds_dwordx4 %0, %1 offset:" #OFF :: "v"(voff), "s"(sbase) : "memory")
#define GLDS_SC1(voff, sbase, OFF) asm volatile("global_load_lds_dwordx4 %0, %1 offset:" #OFF " sc1" :: "v"(voff), "s"(sbase) : "memory")

DI int lane_id() { int l; asm volatile("v_mbcnt_lo_u32_b32 %0, -1, 0\n\tv_mbcnt_hi_u32_b32 %0, -1, %0" : "=v"(l)); return l; }
DI bf16x8 lds128(LAS char* p) { return *(LAS bf16x8*)p; }
DI bf16x4 lds64(LAS char* p) { return *(LAS bf16x4*)p; }
DI bf16x4 ldstr(LAS char* p) { return __builtin_amdgcn_ds_read_tr16_b64_v4i16((LAS bf16x4*)p); }
DI bf16x8 cat(bf16x4 a, bf16x4 b) { return __builtin_shufflevector(a, b, 0, 1, 2, 3, 4, 5, 6, 7); }
DI unsigned pk(float a, float b) { f32x2 f = {a, b}; return __builtin_bit_cast(unsigned, __builtin_convertvector(f, bf2)); }
DI float bflo(unsigned u) { return __uint_as_float(u << 16); }
DI float bfhi(unsigned u) { return __uint_as_float(u & 0xffff0000u); }
DI float dot2(unsigned a, unsigned b, float c) { return __builtin_amdgcn_fdot2_f32_bf16(__builtin_bit_cast(bf2, a), __builtin_bit_cast(bf2, b), c, false); }
DI float swap32_add(float v) { auto r = __builtin_amdgcn_permlane32_swap(__float_as_uint(v), __float_as_uint(v), false, false); return __uint_as_float(r[0]) + __uint_as_float(r[1]); }
DI float swap32_max(float v) { auto r = __builtin_amdgcn_permlane32_swap(__float_as_uint(v), __float_as_uint(v), false, false); return __builtin_fmaxf(__uint_as_float(r[0]), __uint_as_float(r[1])); }
DI float addf(float a, float b) { float r; asm("v_add_f32 %0, %1, %2" : "=v"(r) : "v"(a), "v"(b)); return r; }
DI float mulf(float a, float b) { float r; asm("v_mul_f32 %0, %1, %2" : "=v"(r) : "v"(a), "v"(b)); return r; }
DI float relu(float a) { float r; asm("v_max_f32 %0, 0, %1" : "=v"(r) : "v"(a)); return r; }
DI f32x4 add4(f32x4 a, f32x4 b) { return f32x4{addf(a[0], b[0]), addf(a[1], b[1]), addf(a[2], b[2]), addf(a[3], b[3])}; }
DI void wst128(rsrc_t rs, unsigned off, u32x4 v) { __builtin_amdgcn_raw_buffer_store_b128(v, rs, off, 0, 16); }
DI void wst64(rsrc_t rs, unsigned off, u32x2 v) { __builtin_amdgcn_raw_buffer_store_b64(v, rs, off, 0, 16); }
DI void wst32(rsrc_t rs, unsigned off, unsigned v) { __builtin_amdgcn_raw_buffer_store_b32(v, rs, off, 0, 16); }
DI u32x4 wld128(rsrc_t rs, unsigned off) { return __builtin_amdgcn_raw_buffer_load_b128(rs, off, 0, 16); }
DI u32x2 wld64(rsrc_t rs, unsigned off) { return __builtin_amdgcn_raw_buffer_load_b64(rs, off, 0, 16); }
DI unsigned wld32(rsrc_t rs, unsigned off) { return __builtin_amdgcn_raw_buffer_load_b32(rs, off, 0, 16); }
#define POLL_SLEEP 4
DI void wait_tags(gu32* flags, int word, unsigned tag) {
    for (int spin = 0; spin < (1 << 18); ++spin) {
        const unsigned v = __hip_atomic_load(flags + word, __ATOMIC_RELAXED, __HIP_MEMORY_SCOPE_AGENT);
        if (__all(v == tag)) break;
        __builtin_amdgcn_s_sleep(POLL_SLEEP);
    }
    asm volatile("" ::: "memory");
}
DI void wait_tags2(gu32* flags, int word0, int word1, unsigned tag) {
    for (int spin = 0; spin < (1 << 18); ++spin) {
        const unsigned v0 = __hip_atomic_load(flags + word0, __ATOMIC_RELAXED, __HIP_MEMORY_SCOPE_AGENT);
        const unsigned v1 = __hip_atomic_load(flags + word1, __ATOMIC_RELAXED, __HIP_MEMORY_SCOPE_AGENT);
        if (__all(v0 == tag && v1 == tag)) break;
        __builtin_amdgcn_s_sleep(POLL_SLEEP);
    }
    asm volatile("" ::: "memory");
}
DI void publish(gu32* flags, int word, bool on, unsigned tag) { if (on) __hip_atomic_store(flags + word, tag, __ATOMIC_RELAXED, __HIP_MEMORY_SCOPE_AGENT); }
#define TAG_OF(gen) ((unsigned)__builtin_amdgcn_readfirstlane(gen) + 1u)

DI float max16(const f32x16& s) {
    float a = __builtin_fmaxf(__builtin_fmaxf(s[0], s[1]), s[2]), b = __builtin_fmaxf(__builtin_fmaxf(s[3], s[4]), s[5]);
    a = __builtin_fmaxf(__builtin_fmaxf(a, s[6]), s[7]); b = __builtin_fmaxf(__builtin_fmaxf(b, s[8]), s[9]);
    a = __builtin_fmaxf(__builtin_fmaxf(a, s[10]), s[11]); b = __builtin_fmaxf(__builtin_fmaxf(b, s[12]), s[13]);
    a = __builtin_fmaxf(__builtin_fmaxf(a, s[14]), s[15]);
    return swap32_max(__builtin_fmaxf(a, b));
}

template <int I> struct IC { static constexpr int v = I; };
template <int I, int N, class F> DI void sfor(F&& f) { if constexpr (I < N) { f(IC<I>{}); sfor<I + 1, N>(f); } }
#define SFOR(var, N) sfor<0, N>([&](auto var##_) __attribute__((always_inline)) { constexpr int var = decltype(var##_)::v;
#define SEND });
#define VM0() __builtin_amdgcn_s_waitcnt(0x0F70)

constexpr unsigned WRB = 81920, SCR = 155648, KVB = 0;
constexpr unsigned po_slot_f(int r) {
    constexpr int m[17] = {0, 1, 2, 3, 4, 5, 0, 1, 2, 5, 0, 1, 6, 3, 4, 2, 5};
    return m[r] < 4 ? 73728u + m[r] * 20480u : m[r] == 4 ? 49152u : m[r] == 5 ? 0u : 24576u;
}
template <int R> struct PoSlot { static constexpr unsigned v = po_slot_f(R); };
#define po_slot(r) (PoSlot<(r)>::v)
DI int first_stage(int j, int cb) { return ((((j >> 1) + 2 * (j & 1) + ((cb >> 2) & 1)) & 3) * 5 + 1) >> 1; }
DI int kstage(int kt, int rk) { const int k = kt + rk; return k >= 10 ? k - 10 : k; }
#define GATHER2(S0, S1, G, OFF, STRIDE) do { unsigned bad_ = 0; \
        _Pragma("unroll") for (int k_ = 0; k_ < 5; ++k_) { PIN(G[k_]); bad_ |= (G[k_][1] ^ tag) | (G[k_][3] ^ tag); } \
        S0 = addf(addf(addf(addf(__uint_as_float(G[0][0]), __uint_as_float(G[1][0])), __uint_as_float(G[2][0])), __uint_as_float(G[3][0])), __uint_as_float(G[4][0])); \
        S1 = addf(addf(addf(addf(__uint_as_float(G[0][2]), __uint_as_float(G[1][2])), __uint_as_float(G[2][2])), __uint_as_float(G[3][2])), __uint_as_float(G[4][2])); \
        bool ok_ = bad_ == 0; \
        for (int spin_ = 0; spin_ < (1 << 18) && __builtin_expect(!__all(ok_), 0); ++spin_) { \
            __builtin_amdgcn_s_sleep(POLL_SLEEP); S0 = 0.f; S1 = 0.f; ok_ = true; \
            _Pragma("unroll") for (int k_ = 0; k_ < 5; ++k_) { const u32x4 g_ = wld128(rs, (OFF) + k_ * (STRIDE)); \
                S0 += __uint_as_float(g_[0]); S1 += __uint_as_float(g_[2]); ok_ = ok_ && g_[1] == tag && g_[3] == tag; } } \
        S0 = swap32_add(S0); S1 = swap32_add(S1); } while (0)
#define GATHER_REQ(G, OFF, STRIDE) do { _Pragma("unroll") for (int k_ = 0; k_ < 5; ++k_) \
        asm volatile("global_load_dwordx4 %0, %1, %2 sc1" : "=v"(G[k_]) : "v"(OFF), "s"(ws + k_ * (STRIDE)) : "memory"); } while (0)
DI float fsum32(const bf16x8 (&f)[4]) {
    float a = 0.f, b = 0.f;
#pragma unroll
    for (int p = 0; p < 4; ++p) { const u32x4 u = __builtin_bit_cast(u32x4, f[p]); a = dot2(u[0], 0x3F803F80u, a); b = dot2(u[1], 0x3F803F80u, b); a = dot2(u[2], 0x3F803F80u, a); b = dot2(u[3], 0x3F803F80u, b); }
    return swap32_add(a + b);
}
DI unsigned x_addr(int row, int p, int h) { return row * 128 + (((2 * p + h) ^ ((row >> 1) & 7)) << 4); }
DI unsigned wq_addr(int lane, int nt, bool vt) {
    const int c = lane & 3;
    return WRB + (lane >> 5) * 2048 + (nt >> 1) * 512 + ((lane >> 2) & 3) * 128 + (((nt ^ (lane >> 3)) & 1) << 6) + ((lane >> 4) & 1) * 32 + (vt ? c : ((c & 1) << 1) | (c >> 1)) * 8;
}
DI unsigned gu_addr(int lane, int nt) {
    const int c = lane & 3, gi = (lane >> 4) & 1;
    return WRB + gi * 3072 + (lane >> 5) * 1536 + ((lane >> 2) & 3) * 192 + (nt >> 1) * 64 + (((nt ^ gi) & 1) << 5) + (((c & 1) << 1) | (c >> 1)) * 8;
}
#define VMW(N) asm volatile("s_waitcnt vmcnt(" #N ")" ::: "memory")
struct Ids { int rb, cb, hd, qb, rb64, cb80, rk, i, sx, sw; };

#define LOADER_SETUP() \
        const unsigned vA0 = (lane >> 3) * 1280 + (((lane & 7) ^ (lane >> 4)) << 4), vA1 = (vA0 ^ 64) + 10240 - 1024; \
        const unsigned vW0 = ((lane >> 3) & 3) * 3840 + (lane >> 5) * 128 + (((lane & 7) ^ ((lane >> 2) & 4)) << 4), vW1 = vW0 + 15360 - 1024; \
        const char* xa = x + (size_t)(rb * 128 + lw * 64) * 1280; \
        const char* wb = wqkv + (size_t)lw * 32 * 3840 + (cb < 15 ? cb : 14) * 256
#define H_4(V0, V1, P, STEP) do { GLDS(V0, P, 0); GLDS(V1, P, 1024); GLDS(V0, (P) + (STEP) - 2048, 2048); GLDS(V1, (P) + (STEP) - 2048, 3072); } while (0)
#define H_X(slot, ka) do { const char* p_ = xa + (ka) * 128; set_m0((slot) + lw * 8192); H_4(vA0, vA1, p_, 20480); set_m0((slot) + lw * 8192 + 4096); H_4(vA0, vA1, p_ + 40960, 20480); } while (0)
#define H_Q(xs, wsl, ka) do { H_X(xs, ka); const char* q_ = wb + (ka) * 245760; set_m0(WRB + (wsl) + lw * 8192); H_4(vW0, vW1, q_, 30720); \
        set_m0(WRB + (wsl) + lw * 8192 + 4096); H_4(vW0, vW1, q_ + 61440, 30720); } while (0)
#define NEXT_X(v) ((v) == 65536 ? 0u : (v) + 16384)
#define NEXT_W(v) ((v) == 49152 ? 0u : (v) + 24576)
DI void helper_role(const char* x, const char* wqkv, const char* wo, const char* wg, const char* wu, const char* wd, char* ws, rsrc_t rs, gu32* flags,
                    const Ids id, int lw) {
    const int w = lw;
#define HPRIO 3
    __builtin_amdgcn_s_setprio(HPRIO);
    unsigned gen, tag;
    asm volatile("global_load_dword %0, %1, %2 sc1" : "=v"(gen) : "v"(0), "s"(flags + F_GEN) : "memory");
    const int lane = lane_id(), r = lane & 31, h = lane >> 5;
    const int rb = id.rb, cb = id.cb, rk = id.rk, cbl = cb < 15 ? cb : 14;
    const unsigned voff = lane * 16;
    if (lw < 2) {
        LOADER_SETUP();
        H_Q(16384, 24576, rk + 1);
        SBAR();
        unsigned gv[3];
#pragma unroll
        for (int p = 0; p < 3; ++p) { const int P = 64 * p + lane, row = P / 12; gv[p] = row * 3072 + (((P - row * 12) ^ (2 * lw)) << 4) + 2048 - p * 1024; }
        const char* gb = (lw ? wu : wg) + cb * 192 - 2048;
#define H_G(wsl, ka) do { const char* q_ = gb + (size_t)(ka) * 196608; _Pragma("unroll") for (int st_ = 0; st_ < 4; ++st_) { \
        set_m0(WRB + (wsl) + st_ * 6144 + lw * 3072); GLDS(gv[0], q_ + st_ * 49152, 0); GLDS(gv[1], q_ + st_ * 49152, 1024); GLDS(gv[2], q_ + st_ * 49152, 2048); } } while (0)
        VM_BAR(16);
        H_Q(32768, 49152, kstage(2, rk));
        unsigned xs = 49152, wsl = 0; int ka = kstage(3, rk);
#pragma unroll 1
        for (int g = 0; g < 7; ++g) { VM_BAR(16); H_Q(xs, wsl, ka); xs = NEXT_X(xs); wsl = NEXT_W(wsl); ka = ka == 9 ? 0 : ka + 1; }
        VM_BAR(16);
        ka = kstage(5, rk);
#pragma unroll 1
        for (int g = 7; g < 12; ++g) { H_G(wsl, ka); wsl = NEXT_W(wsl); ka = ka == 9 ? 0 : ka + 1; VM_BAR(12); }
#pragma unroll 1
        for (int g = 12; g < 17; ++g) { H_X(xs, ka); H_G(wsl, ka); xs += 16384; wsl = NEXT_W(wsl); ka = ka == 9 ? 0 : ka + 1; VM_BAR(20); }
        VM_BAR(0);
        asm volatile("v_readfirstlane_b32 %0, %1" : "=s"(tag) : "v"(gen));
        tag += 1;
#undef H_G
    } else {
        const int sv = lw - 2;
        VM_BAR(0);
        asm volatile("v_readfirstlane_b32 %0, %1" : "=s"(tag) : "v"(gen));
        tag += 1;
        u32x4 gs[2][5], gc[5];
        const unsigned so = WS_ST + ((rb * 10 + h * 5) * 128 + sv * 64 + r) * 16, co = WS_CQ + ((cbl * 10 + h * 5) * 128 + sv * 64 + 2 * r) * 8;
        const unsigned cg = WS_CG + ((cb * 10 + h * 5) * 192 + sv * 64 + 2 * r) * 8;
        unsigned wsl = 0;
        bf16x8 fw[3][4];
#pragma unroll 1
        for (int g = 0; g < 4; ++g) {
            if (g == id.sw + 1 && cb < 15) {
                const float c0 = fsum32(fw[0]);
                const float c1 = fsum32(fw[1]);
                wst64(rs, WS_CQ + ((cb * 10 + kstage(id.sw, rk)) * 128 + sv * 64 + lane) * 8, u32x2{__float_as_uint(h ? c1 : c0), tag});
            }
            if ((g == id.sx || g == id.sx + 1) && id.sx < 3) {
                const int row = sv * 64 + (g - id.sx) * 32 + r;
                float sx = 0.f, sq = 0.f;
#pragma unroll
                for (int p = 0; p < 4; ++p) {
                    const u32x4 f = __builtin_bit_cast(u32x4, lds128(LP(x_addr(row, p, h) + id.sx * 16384)));
#pragma unroll
                    for (int e = 0; e < 4; ++e) { sx = dot2(f[e], 0x3F803F80u, sx); sq = dot2(f[e], f[e], sq); }
                }
                sx = swap32_add(sx); sq = swap32_add(sq);
                wst128(rs, h == 0 ? WS_ST + ((rb * 10 + kstage(id.sx, rk)) * 128 + row) * 16 : OOB, u32x4{__float_as_uint(sx), tag, __float_as_uint(sq), tag});
            }
            if (g == id.sw) {
#pragma unroll
                for (int t = 0; t < 2; ++t) {
                    const unsigned a = wq_addr(lane, sv * 2 + t, cb >= 10) + wsl;
#pragma unroll
                    for (int p = 0; p < 4; ++p) fw[t][p] = cat(ldstr(LP(a) + p * 4096), ldstr(LP(a) + p * 4096 + 1024));
                }
            }
            BAR_L();
            wsl = NEXT_W(wsl);
        }
        {
            const int vh = ((cb - 10) * 128 + 19) / 20 + (lane >> 3);
            const unsigned v = (lane & 1) ? 0u : 0x3F803F80u;
            const unsigned o = (cb >= 10 && cb < 15 && vh * 20 < (cb - 9) * 128) ? WS_KV + (vh * 64 + rb * 4 + sv * 2) * 3072 + 1536 + ((lane >> 1) & 3) * 384 + 320 + (lane & 1) * 16 : OOB;
            wst128(rs, o, u32x4{v, v, v, v}); wst128(rs, o + 3072, u32x4{v, v, v, v});
        }
        BAR();
        GATHER_REQ(gs[0], so, 2048);
        BAR();
        GATHER_REQ(gs[1], so + 512, 2048);
        BAR();
        GATHER_REQ(gc, co, 1024);
        BAR();
        float s0, s1;
        VMW(5);
#pragma unroll
        for (int t = 0; t < 2; ++t) {
            GATHER2(s0, s1, gs[t], so + t * 512, 2048);
            const float mu = s0 * (1.f / 640.f);
            *(LAS float*)LP(SCR + h * 512 + (sv * 64 + t * 32 + r) * 4) = h ? -mu : __builtin_amdgcn_rsqf(__builtin_fmaxf(__builtin_fmaf(-mu, mu, s1 * (1.f / 640.f)), 0.f) + 1e-5f);
        }
        BAR();
        VMW(0);
        GATHER2(s0, s1, gc, co, 1024);
        *(LAS f32x2*)LP(SCR + 1024 + (sv * 64 + 2 * r) * 4) = f32x2{s0, s1};
        BAR_L();
        wsl = 24576;
#pragma unroll 1
        for (int g = 0; g < 4; ++g) {
            if (g == id.sw + 1) {
                float c[3];
#pragma unroll
                for (int t = 0; t < 3; ++t) c[t] = fsum32(fw[t]);
                const unsigned o = WS_CG + ((cb * 10 + kstage(5 + id.sw, rk)) * 192 + sv * 96 + lane) * 8;
                wst64(rs, o, u32x2{__float_as_uint(h ? c[1] : c[0]), tag});
                wst64(rs, h == 0 ? o + 512 : OOB, u32x2{__float_as_uint(c[2]), tag});
            }
            if (g == id.sw) {
#pragma unroll
                for (int t = 0; t < 3; ++t) {
                    const unsigned a = gu_addr(lane, 3 * sv + t) + wsl;
#pragma unroll
                    for (int p = 0; p < 4; ++p) fw[t][p] = cat(ldstr(LP(a) + p * 6144), ldstr(LP(a) + p * 6144 + 768));
                }
            }
            BAR_L();
            wsl = NEXT_W(wsl);
        }
        BAR();
        if (sv == 0) {
            const int ty = cb / 5, cc = cb - ty * 5, gg = (r >> 1) * 5 + (r & 1) * 2 + 2 * h;
            const int blk = ty == 2 ? (r * 20 + 19 * h) >> 7 : gg >> 4;
            publish(flags, F_QKV + r * 128 + ((ty == 0 ? 4 : ty == 1 ? 0 : 2) + h) * 16 + rb, ty < 3 && blk == cc, tag);
            publish(flags, F_ACK + blockIdx.x, lane == 0, tag);
        }
        GATHER_REQ(gc, cg, 1536);
        BAR();
        GATHER_REQ(gs[0], cg - sv * 512 + 1024, 1536);
        BAR(); BAR();
        {
            VMW(0);
            GATHER2(s0, s1, gc, cg, 1536);
            *(LAS f32x2*)LP(SCR + 1536 + (sv * 64 + 2 * r) * 4) = f32x2{s0, s1};
            if (sv == 0) { GATHER2(s0, s1, gs[0], cg + 1024, 1536); *(LAS f32x2*)LP(SCR + 1536 + (128 + 2 * r) * 4) = f32x2{s0, s1}; }
        }
        BAR_L();
    }
    wait_tags2(flags, F_QKV + id.hd * 128 + lane, F_QKV + id.hd * 128 + 64 + (lane & 1) * 16 + id.qb * 2 + ((lane >> 1) & 1), tag);
    { const char* p_ = ws + WS_Q + (id.hd * 64 + id.qb * 8 + lw * 2) * 1536; set_m0(49152 + lw * 3072); GLDS_SC1(voff, p_, 0); GLDS_SC1(voff, p_, 1024); GLDS_SC1(voff, p_, 2048); }
    const char* kv = ws + WS_KV + (size_t)id.hd * 196608 + lw * 6144;
#define KV_DMA(s) do { const char* p_ = kv + (s) * 24576; set_m0(KVB + (s) % 3 * 24576 + lw * 6144); GLDS_SC1(voff, p_, 0); GLDS_SC1(voff, p_, 1024); GLDS_SC1(voff, p_, 2048); \
        set_m0(KVB + (s) % 3 * 24576 + lw * 6144 + 3072); GLDS_SC1(voff, p_ + 3072, 0); GLDS_SC1(voff, p_ + 3072, 1024); GLDS_SC1(voff, p_ + 3072, 2048); } while (0)
    KV_DMA(0); KV_DMA(1);
    unsigned pv[5];
#define PO_VOFF(ROW) do { _Pragma("unroll") for (int ii = 0; ii < 5; ++ii) { \
        const int P = ii * 64 + lane, rho = P / 10, c = P - rho * 10, u = rho >> 4, qq = (rho >> 2) & 3, e = rho & 3; pv[ii] = (ROW) * 1280 + c * 16 + 3072 - (ii & 3) * 1024; } } while (0)
#define PO_DMA(rnd, src) do { const char* p_ = (src); set_m0(po_slot(rnd) + lw * 5120); GLDS(pv[0], p_, 0); GLDS(pv[1], p_, 1024); GLDS(pv[2], p_, 2048); GLDS(pv[3], p_, 3072); \
        set_m0(po_slot(rnd) + lw * 5120 + 4096); GLDS(pv[4], p_, 0); } while (0)
#define PO_REG(k, src) do { const char* p_ = (src); _Pragma("unroll") for (int ii = 0; ii < 5; ++ii) \
        asm volatile("global_load_dwordx4 %0, %1, %2" : "=v"(pr[k][ii]) : "v"(pv[ii]), "s"(p_ + (ii & 3) * 1024) : "memory"); } while (0)
    PO_VOFF(8 * qq + 4 * u + e);
    const char* pw = wd + (size_t)lw * 491520 + id.cb80 * 160 - 3072;
    const char* po = wo + (size_t)lw * 204800 + id.cb80 * 160 - 3072;
    const int prot = 3 * (id.rb64 & 3);
#define PO_W(rnd) (pw + ((rnd) + prot >= 12 ? (rnd) + prot - 12 : (rnd) + prot) * 40960)
    u32x4 pr[6][5];
    *(LAS unsigned*)LP(SCR + 4092) = 0u;
    LGKM0(); VM_BAR(6);
    SFOR(s, 8)
        if constexpr (s == 1) if (lw == 0) publish(flags, F_H + (rb * 2 + lane) * 32 + cb, lane < 2, tag);
        if constexpr (s < 2) { PO_DMA(2 * s, PO_W(2 * s)); PO_DMA(2 * s + 1, PO_W(2 * s + 1)); }
        if constexpr (s >= 2 && s < 5) { PO_REG(2 * s - 4, PO_W(2 * s + 2)); PO_REG(2 * s - 3, PO_W(2 * s + 3)); }
        if constexpr (s == 3) {
            if (lw == 1) wait_tags(flags, F_H + id.rb64 * 32 + (lane & 15), tag);
            if (lw == 2 && blockIdx.x == 0) {
                for (int spin = 0; spin < (1 << 18); ++spin) {
                    const u32x4 a = wld128(rs, WS_FLAG + F_ACK * 4 + lane * 16);
                    if (__all(a[0] == tag && a[1] == tag && a[2] == tag && a[3] == tag)) break;
                    __builtin_amdgcn_s_sleep(POLL_SLEEP);
                }
                publish(flags, F_GEN, lane == 0, tag);
            }
        }
        if constexpr (s == 5) {
            unsigned dummy;
            asm volatile("global_load_dword %0, %1, %2\n\ts_waitcnt vmcnt(0)" : "=&v"(dummy) : "v"((lane < 50 ? lane : 49) * 128), "s"(wo + (size_t)(id.i * 4 + lw) * 6400) : "memory");
        }
        if constexpr (s == 7) PO_DMA(4, PO_W(4));
        VM_BAR(0);
        if constexpr (s < 6) KV_DMA(s + 2);
    SEND
    PO_DMA(5, PO_W(5));
    PO_VOFF(20 * (qq >> 1) + 4 * (qq & 1) + 8 * u + e);
#define PO_PUT(rnd) do { _Pragma("unroll") for (int ii = 0; ii < 5; ++ii) { PIN(pr[(rnd) - 6][ii]); *(LAS u32x4*)(LP(po_slot(rnd) + lw * 5120 + voff) + ii * 1024) = pr[(rnd) - 6][ii]; } } while (0)
    BAR();
    PO_PUT(6); PO_DMA(12, po);
    LGKM0(); VM_BAR(5);
    PO_PUT(7); PO_PUT(8); PO_DMA(13, po + 51200);
    if (lw == 0) {
        for (int spin = 0; spin < (1 << 22) && __builtin_amdgcn_readfirstlane(*(volatile LAS unsigned*)LP(SCR + 4092)) != 8; ++spin) __builtin_amdgcn_s_sleep(1);
        publish(flags, F_O + (id.qb * 4 + lane) * 32 + id.hd, lane < 4, tag);
    }
    BAR_L();
    PO_PUT(9); PO_PUT(10);
    if (lw == 1) wait_tags(flags, F_O + id.rb64 * 32 + r, tag);
    PO_DMA(14, po + 2 * 51200);
    BAR_L();
    PO_PUT(11); PO_DMA(15, po + 3 * 51200);
    PO_VOFF(20 * (4 * (qq >> 1) + 2 * (qq & 1) + u) + 16 + e);
    LGKM0(); VM_BAR(15);
    PO_DMA(16, po);
    VM_BAR(10);
    VM_BAR(0);
#undef PO_PUT
#undef KV_DMA
#undef PO_VOFF
#undef PO_DMA
#undef PO_REG
#undef PO_W
}

template <bool VT>
DI void qkv_phase(char* ws, rsrc_t rs, int rb, int cb, int w) {
    const int lane = lane_id(), wm = w >> 2, wn = w & 3, r = lane & 31, h = lane >> 5, xrow = wm * 64 + r;
    unsigned ax[4];
#pragma unroll
    for (int p = 0; p < 4; ++p) ax[p] = x_addr(xrow, p, h);
    const unsigned aw = wq_addr(lane, wn, VT);
    const unsigned rec = cb < 5 ? 1536 : 3072;
    unsigned off[2];
    if (!VT) {
#pragma unroll
        for (int o = 0; o < 2; ++o) {
            const int Gc = (cb % 5) * 16 + wn * 4 + o * 2 + h, P = Gc / 5, gp = Gc - P * 5;
            off[o] = (cb < 5 ? WS_Q : WS_KV) + (rb * 4 + wm * 2) * rec + r * 16 + (2 * P + (gp > 2)) * 64 * rec + (gp > 2 ? gp - 3 : gp) * 512 + (gp == 2);
            PIN(off[o]);
        }
    } else {
        const int col = (cb - 10) * 128 + wn * 32 + r, vh = col / 20, d = col - vh * 20;
        off[0] = cb < 15 ? WS_KV + (vh * 64 + rb * 4 + wm * 2) * 3072 + 1536 + h * 384 + d * 16 : OOB;
        PIN(off[0]);
    }
    f32x16 acc[2] = {};
    bf16x8 xA[4][2], wA[4], xB[4][2], wB[4];
#define Q_MM(X, W, p, i) do { acc[i] = VT ? MFMA16(X[p][i], W[p], acc[i]) : MFMA16(W[p], X[p][i], acc[i]); PIN(acc[i]); SBAR(); } while (0)
#define Q_RW(p, A) cat(ldstr(LP(A) + (p) * 4096), ldstr(LP(A) + (p) * 4096 + 1024))
#define Q_STAGE(XC, WC, XN, WN) do { BAR_L(); const unsigned a_ = aw + wsl; \
        _Pragma("unroll") for (int p = 0; p < 4; ++p) { const unsigned b_ = ax[p] + xs; \
            XN[p][0] = lds128(LP(b_)); XN[p][1] = lds128(LP(b_) + 4096); if (p == 3) WN[3] = Q_RW(3, a_); SBAR(); Q_MM(XC, WC, p, 0); \
            if (p < 3) WN[p] = Q_RW(p, a_); SBAR(); Q_MM(XC, WC, p, 1); } \
        xs = NEXT_X(xs); wsl = NEXT_W(wsl); } while (0)
    BAR();
    STAMP(0, 1);
#pragma unroll
    for (int p = 0; p < 4; ++p) { xA[p][0] = lds128(LP(ax[p])); xA[p][1] = lds128(LP(ax[p]) + 4096); wA[p] = Q_RW(p, aw); }
    unsigned xs = 16384, wsl = 24576;
#pragma unroll 1
    for (int g = 0; g < 8; g += 2) {
        Q_STAGE(xA, wA, xB, wB); Q_STAGE(xB, wB, xA, wA);
        if (g == 2) STAMP(5, 1);
        if (g == 4) STAMP(5, 2);
    }
    STAMP(5, 3);
    Q_STAGE(xA, wA, xB, wB);
    BAR_L();
    STAMP(5, 4);
    f32x4 e0[VT ? 8 : 2], e1[VT ? 8 : 2];
    float sa[2], sb[2];
    Q_MM(xB, wB, 0, 0); Q_MM(xB, wB, 0, 1);
    if (!VT) {
#pragma unroll
        for (int i = 0; i < 2; ++i) { sa[i] = *(LAS float*)LP(SCR + (xrow + 32 * i) * 4); sb[i] = *(LAS float*)LP(SCR + 512 + (xrow + 32 * i) * 4); }
#pragma unroll
        for (int o = 0; o < 2; ++o) { e0[o] = *(LAS f32x4*)LP(SCR + 1024 + (wn * 32 + o * 16 + 4 * h) * 4); e1[o] = *(LAS f32x4*)LP(SCR + 1024 + (wn * 32 + o * 16 + 8 + 4 * h) * 4); }
    } else {
        sa[0] = *(LAS float*)LP(SCR + 1024 + (wn * 32 + r) * 4);
#pragma unroll
        for (int k = 0; k < (VT ? 8 : 2); ++k) { e0[k] = *(LAS f32x4*)LP(SCR + (wm * 64 + 8 * k + 4 * h) * 4); e1[k] = *(LAS f32x4*)LP(SCR + 512 + (wm * 64 + 8 * k + 4 * h) * 4); }
    }
    SBAR();
    Q_MM(xB, wB, 1, 0); Q_MM(xB, wB, 2, 0); Q_MM(xB, wB, 3, 0); Q_MM(xB, wB, 1, 1); Q_MM(xB, wB, 2, 1); Q_MM(xB, wB, 3, 1);
#undef Q_MM
#undef Q_RW
#undef Q_STAGE
    STAMP(0, 2);
#pragma unroll
    for (int i = 0; i < 2; ++i) {
        const f32x16& c = acc[i];
#pragma unroll
        for (int o = 0; o < 2; ++o) {
            const f32x4 c0 = {c[8 * o], c[8 * o + 1], c[8 * o + 2], c[8 * o + 3]}, c1 = {c[8 * o + 4], c[8 * o + 5], c[8 * o + 6], c[8 * o + 7]};
            if (!VT) {
                const float a = cb < 5 ? sa[i] * (0.22360679774997896f * 1.4426950408889634f) : sa[i];
                const f32x4 v0 = (c0 + e0[o] * sb[i]) * a, v1 = (c1 + e1[o] * sb[i]) * a;
                const u32x4 ov = {pk(v0[0], v0[1]), pk(v0[2], v0[3]), pk(v1[0], v1[1]), pk(v1[2], v1[3])};
                wst128(rs, (off[o] & ~1u) + i * rec, ov);
                if (off[o] & 1) wst128(rs, off[o] - 1 + (64 + i) * rec, ov);
            } else {
                constexpr int M = VT ? 7 : 1;
                const f32x4 v0 = (c0 + e1[(4 * i + 2 * o) & M] * sa[0]) * e0[(4 * i + 2 * o) & M], v1 = (c1 + e1[(4 * i + 2 * o + 1) & M] * sa[0]) * e0[(4 * i + 2 * o + 1) & M];
                wst128(rs, off[0] + i * 3072 + o * 768, u32x4{pk(v0[0], v0[1]), pk(v0[2], v0[3]), pk(v1[0], v1[1]), pk(v1[2], v1[3])});
            }
        }
    }
}

extern "C" __global__ __launch_bounds__(768, 3) void k_block(const char* __restrict__ x, const char* __restrict__ wqkv, const char* __restrict__ wo,
                                                             const char* __restrict__ wg, const char* __restrict__ wu, const char* __restrict__ wd,
                                                             char* __restrict__ ws, char* __restrict__ out) {
    extern __shared__ __attribute__((aligned(16))) char smem[];
    const int b = blockIdx.x, j = b & 7, i = b >> 3;
    const int w = __builtin_amdgcn_readfirstlane(threadIdx.x >> 6);
    const rsrc_t rs = __builtin_amdgcn_make_buffer_rsrc(ws, 0, WS_END, 0x00020000);
    const int rb = 4 * (j >> 1) + (i & 3), cb = (j & 1) * 8 + (i >> 2);
    const int hd = j * 4 + (i >> 3), qb = i & 7;
    const int rb64 = j * 4 + (i & 3), cb80 = i >> 2;
    if (w == 8 || w == 9) {
        const int lane = lane_id(), lw = w - 8, rk = first_stage(j, cb);
        LOADER_SETUP();
        H_Q(0, 0, rk);
    }
    if (w >= 8) {
        const Ids id = {rb, cb, hd, qb, rb64, cb80, first_stage(j, cb), i, cb & 3, rb & 3};
        helper_role(x, wqkv, wo, wg, wu, wd, ws, rs, (gu32*)(ws + WS_FLAG), id, w - 8);
        return;
    }
    STAMP(0, 0);
    if (cb < 10) qkv_phase<false>(ws, rs, rb, cb, w); else qkv_phase<true>(ws, rs, rb, cb, w);
    STAMP(0, 3);

    {
        const int lane = lane_id(), tt = w & 3, fh = w >> 2, r = lane & 31, h = lane >> 5, xrow = tt * 32 + r;
        unsigned ax[4];
#pragma unroll
        for (int p = 0; p < 4; ++p) ax[p] = x_addr(xrow, p, h);
        const unsigned awA = gu_addr(lane, 3 * fh), awB = gu_addr(lane, 3 * fh + 1);
        f32x16 acc[3] = {};
        bf16x8 xf[3], wf[4][3], xA, wA[2], xB, wB[2];
#define G_RW(p, a, A, B) ((a) == 1 ? cat(ldstr(LP(B) + (p) * 6144), ldstr(LP(B) + (p) * 6144 + 768)) : cat(ldstr(LP(A) + (p) * 6144 + (a) * 32), ldstr(LP(A) + (p) * 6144 + (a) * 32 + 768)))
#define G_MM(X, W, a) do { acc[a] = MFMA16(W, X, acc[a]); PIN(acc[a]); SBAR(); } while (0)
#define G_STAGE(XC, WC, XN, WN) do { const unsigned a_ = awA + wsl, c_ = awB + wsl; \
        WN[0] = G_RW(3, 1, a_, c_); SBAR(); \
        _Pragma("unroll") for (int p = 0; p < 3; ++p) { \
            G_MM(xf[p], wf[p][0], 0); wf[p][0] = G_RW(p, 0, a_, c_); if (p == 0) XN = lds128(LP(ax[3] + xs)); if (p == 1) WN[1] = G_RW(3, 2, a_, c_); SBAR(); \
            G_MM(xf[p], wf[p][1], 1); wf[p][1] = G_RW(p, 1, a_, c_); SBAR(); \
            G_MM(xf[p], wf[p][2], 2); wf[p][2] = G_RW(p, 2, a_, c_); xf[p] = lds128(LP(ax[p] + xs)); SBAR(); } \
        G_MM(XC, wf[3][0], 0); wf[3][0] = G_RW(3, 0, a_, c_); SBAR(); G_MM(XC, WC[0], 1); G_MM(XC, WC[1], 2); \
        xs = NEXT_X(xs); wsl = NEXT_W(wsl); } while (0)
        STAMP(1, 0);
#pragma unroll
        for (int p = 0; p < 3; ++p) {
            xf[p] = lds128(LP(ax[p]));
#pragma unroll
            for (int a = 0; a < 3; ++a) wf[p][a] = G_RW(p, a, awA + 24576, awB + 24576);
        }
        xA = lds128(LP(ax[3])); wf[3][0] = G_RW(3, 0, awA + 24576, awB + 24576); wA[0] = G_RW(3, 1, awA + 24576, awB + 24576); wA[1] = G_RW(3, 2, awA + 24576, awB + 24576);
        unsigned xs = 16384, wsl = 49152;
#pragma unroll 1
        for (int g = 10; g < 18; g += 2) {
            BAR_L();
            STAMP(3, (g - 10) >> 1);
            G_STAGE(xA, wA, xB, wB);
            BAR_L();
            G_STAGE(xB, wB, xA, wA);
            if (g == 12) VM0();
        }
        STAMP(3, 4);
        BAR_L();
        STAMP(3, 5);
        G_STAGE(xA, wA, xB, wB);
        const float ra = *(LAS float*)LP(SCR + xrow * 4), rm = *(LAS float*)LP(SCR + 512 + xrow * 4);
        f32x4 e0[6], e1[6];
        G_MM(xf[0], wf[0][0], 0); G_MM(xf[0], wf[0][1], 1); G_MM(xf[0], wf[0][2], 2); G_MM(xf[1], wf[1][0], 0); G_MM(xf[1], wf[1][1], 1); G_MM(xf[1], wf[1][2], 2);
#pragma unroll
        for (int t = 0; t < 6; ++t) { e0[t] = *(LAS f32x4*)LP(SCR + 1536 + (fh * 96 + (t >> 1) * 32 + 8 * (t & 1) + 4 * h) * 4); e1[t] = *(LAS f32x4*)LP(SCR + 1536 + (fh * 96 + (t >> 1) * 32 + 16 + 8 * (t & 1) + 4 * h) * 4); }
        SBAR();
        G_MM(xf[2], wf[2][0], 0); G_MM(xB, wf[3][0], 0); G_MM(xf[2], wf[2][1], 1); G_MM(xB, wB[0], 1); G_MM(xf[2], wf[2][2], 2); G_MM(xB, wB[1], 2);
#undef G_RW
#undef G_MM
#undef G_STAGE
        STAMP(1, 3);
        const float a2 = ra * ra;
        const unsigned hp = WS_H + ((rb * 4 + tt) * 96 + cb * 6 + 3 * fh) * 1024 + lane * 16;
#pragma unroll
        for (int a = 0; a < 3; ++a) {
            u32x4 o;
#pragma unroll
            for (int g = 0; g < 2; ++g) {
                const f32x16& c = acc[a];
                const f32x4 gv = f32x4{c[4 * g], c[4 * g + 1], c[4 * g + 2], c[4 * g + 3]} + e0[2 * a + g] * rm;
                const f32x4 uv = (f32x4{c[8 + 4 * g], c[8 + 4 * g + 1], c[8 + 4 * g + 2], c[8 + 4 * g + 3]} + e1[2 * a + g] * rm) * a2;
                o[2 * g] = pk(__builtin_fmaxf(gv[0], 0.f) * uv[0], __builtin_fmaxf(gv[1], 0.f) * uv[1]);
                o[2 * g + 1] = pk(__builtin_fmaxf(gv[2], 0.f) * uv[2], __builtin_fmaxf(gv[3], 0.f) * uv[3]);
            }
            wst128(rs, hp + a * 1024, o);
        }
    }
    STAMP(1, 4);

    bf16x8 hf[12][2];
    const int ptt = w & 1, pkq = w >> 1, prot = 3 * (rb64 & 3);
    unsigned ho;
#define H_REQ(HI) hf[((HI) >> 1) % 12][(HI) & 1] = __builtin_bit_cast(bf16x8, __builtin_amdgcn_raw_buffer_load_b128(rs, ho + ((HI) & 1) * 256, \
        (((HI) >> 1) + prot >= 12 ? ((HI) >> 1) + prot - 12 : ((HI) >> 1) + prot) * 2048, 16))
    {
        const int lane = lane_id(), r = lane & 31, h = lane >> 5;
        f32x16 o = {};
        float l;
        const unsigned aK = lane * 16, aK2 = h ? 1536 + 320 : 1024 + r * 16 + (hd & 1) * 8, aV = 1536 + h * 384 + (r < 21 ? r : 21) * 16;
        const f32x16 zero = {};
        BAR_L();
        STAMP(2, 0);
        const bf16x8 q1 = lds128(LP(49152 + w * 1536 + lane * 16));
        bf16x4 q2 = lds64(LP(49152 + w * 1536 + 1024 + r * 16 + (hd & 1) * 8));
        if (h) q2 = bf16x4{0, 0, 0, 0};
        constexpr unsigned B0 = KVB;
        bf16x8 k1 = lds128(LP(B0 + aK)); bf16x4 k2 = lds64(LP(B0 + aK2));
        f32x16 sA = MFMA8(k2, q2, MFMA16(k1, q1, zero));
        { const float m = max16(sA); if (h) q2[0] = (short)(pk(-m, 0.f) & 0xffffu); }
        sA = MFMA8(k2, q2, MFMA16(k1, q1, zero));
        k1 = lds128(LP(B0 + 3072 + aK)); k2 = lds64(LP(B0 + 3072 + aK2));
        bf16x8 v0 = lds128(LP(B0 + aV)), v1 = v0;
        f32x16 sB;
        u32x4 p0, p1 = {0u, 0u, 0u, 0u};
        float e12 = 0.f, e13 = 0.f, e14 = 0.f, e15 = 0.f;
#define EX(v) __builtin_amdgcn_exp2f(v)
#define ATT_STEP(SC, SN, K1A, K2A, V1A, V0A, HI) do { \
        SN = MFMA16(k1, q1, zero); SBAR(); \
        k1 = lds128(K1A); \
        p1[2] = pk(e12, e13); p1[3] = pk(e14, e15); PIN(p1); \
        float e0 = EX(SC[0]), e1 = EX(SC[1]), e2 = EX(SC[2]), e3 = EX(SC[3]); PIN(e0); PIN(e1); PIN(e2); PIN(e3); SBAR(); \
        SN = MFMA8(k2, q2, SN); SBAR(); \
        k2 = lds64(K2A); \
        if ((HI) >= 0) H_REQ(HI); \
        p0[0] = pk(e0, e1); p0[1] = pk(e2, e3); PIN(p0); \
        float e4 = EX(SC[4]), e5 = EX(SC[5]), e6 = EX(SC[6]), e7 = EX(SC[7]); PIN(e4); PIN(e5); PIN(e6); PIN(e7); SBAR(); \
        o = MFMA16(v1, __builtin_bit_cast(bf16x8, p1), o); SBAR(); \
        v1 = lds128(V1A); \
        p0[2] = pk(e4, e5); p0[3] = pk(e6, e7); PIN(p0); \
        float e8 = EX(SC[8]), e9 = EX(SC[9]), e10 = EX(SC[10]), e11 = EX(SC[11]); PIN(e8); PIN(e9); PIN(e10); PIN(e11); SBAR(); \
        o = MFMA16(v0, __builtin_bit_cast(bf16x8, p0), o); SBAR(); \
        v0 = lds128(V0A); \
        p1[0] = pk(e8, e9); p1[1] = pk(e10, e11); PIN(p1); \
        e12 = EX(SC[12]); e13 = EX(SC[13]); e14 = EX(SC[14]); e15 = EX(SC[15]); PIN(e12); PIN(e13); PIN(e14); PIN(e15); SBAR(); \
    } while (0)
#define ATT_T(SC, SN, t, HI) ATT_STEP(SC, SN, cK + ((t) + 2) * 3072, cK2 + ((t) + 2) * 3072, cV + (t) * 3072 + 768, cV + ((t) + 1) * 3072, HI)
#define ATT_STAGE(H0, WAITC) do { \
        const unsigned nxt = cur == KVB + 49152 ? KVB : cur + 24576; \
        LAS char *cK = LP(aK + cur), *cK2 = LP(aK2 + cur), *cV = LP(aV + cur); \
        ATT_T(sA, sB, 0, H0); ATT_T(sB, sA, 1, (H0) < 0 ? -1 : (H0) + 1); ATT_T(sA, sB, 2, (H0) < 0 ? -1 : (H0) + 2); ATT_T(sB, sA, 3, (H0) < 0 ? -1 : (H0) + 3); \
        ATT_T(sA, sB, 4, (H0) < 0 ? -1 : (H0) + 4); ATT_T(sB, sA, 5, (H0) < 0 ? -1 : (H0) + 5); \
        WAITC; \
        cK = LP(aK + nxt); cK2 = LP(aK2 + nxt); \
        ATT_STEP(sA, sB, cK, cK2, cV + 6 * 3072 + 768, cV + 7 * 3072, (H0) < 0 ? -1 : (H0) + 6); \
        ATT_STEP(sB, sA, cK + 3072, cK2 + 3072, cV + 7 * 3072 + 768, LP(aV + nxt), (H0) < 0 ? -1 : (H0) + 7); \
        cur = nxt; } while (0)
        unsigned cur = B0;
#pragma unroll 1
        for (int s = 0; s < 6; ++s) ATT_STAGE(-1, VM_BAR(0));
        ho = WS_H + ((rb64 * 2 + ptt) * 96 + pkq * 24 + h) * 1024 + ((lane >> 4) & 1) * 512 + (lane & 15) * 16;
        ATT_STAGE(0, BAR());
        ATT_STAGE(8, BAR());
        p1[2] = pk(e12, e13); p1[3] = pk(e14, e15);
        o = MFMA16(v1, __builtin_bit_cast(bf16x8, p1), o);
#undef ATT_STEP
#undef ATT_T
#undef ATT_STAGE
        STAMP(2, 3);
        { auto rr = __builtin_amdgcn_permlane32_swap(__float_as_uint(o[8]), __float_as_uint(o[8]), false, false); l = __uint_as_float(rr[1]); }
        float am = 0.f;
#pragma unroll
        for (int e = 0; e < 12; ++e) am = __builtin_fmaxf(am, __builtin_fabsf(o[e]));
        const bool ok = (l > 5.4e-20f) && (l < 1.8e19f) && (am < 3.0e38f);
        if (__builtin_expect(!__all(ok), 0)) {
            float m = -INFINITY;
            o = zero;
            if (h) q2[0] = 0;
#pragma unroll 1
            for (int t = 0; t < 64; ++t) {
                const unsigned rec = WS_KV + (hd * 64 + t) * 3072;
                const bf16x8 fk1 = __builtin_bit_cast(bf16x8, wld128(rs, rec + aK)); const bf16x4 fk2 = __builtin_bit_cast(bf16x4, wld64(rs, rec + aK2));
                const bf16x8 fv0 = __builtin_bit_cast(bf16x8, wld128(rs, rec + aV)), fv1 = __builtin_bit_cast(bf16x8, wld128(rs, rec + aV + 768));
                f32x16 sf = MFMA8(fk2, q2, MFMA16(fk1, q1, zero));
                const float mn = __builtin_fmaxf(m, max16(sf));
                const float al = EX(m - mn);
                m = mn;
                u32x4 f0, f1;
#pragma unroll
                for (int e = 0; e < 16; ++e) o[e] *= al;
#pragma unroll
                for (int e = 0; e < 4; ++e) { f0[e] = pk(EX(sf[2 * e] - mn), EX(sf[2 * e + 1] - mn)); f1[e] = pk(EX(sf[8 + 2 * e] - mn), EX(sf[8 + 2 * e + 1] - mn)); }
                o = MFMA16(fv0, __builtin_bit_cast(bf16x8, f0), o); o = MFMA16(fv1, __builtin_bit_cast(bf16x8, f1), o);
            }
            auto rr = __builtin_amdgcn_permlane32_swap(__float_as_uint(o[8]), __float_as_uint(o[8]), false, false); l = __uint_as_float(rr[1]);
        }
#undef EX
        const float inv = __builtin_amdgcn_rcpf(l);
        const unsigned op = WS_O + (qb * 8 + w) * 40960;
        u32x4 oa;
#pragma unroll
        for (int e = 0; e < 4; ++e) oa[e] = pk(o[2 * e] * inv, o[2 * e + 1] * inv);
        wst128(rs, op + hd * 1024 + lane * 16, oa);
        wst64(rs, h ? OOB : op + 32768 + hd * 256 + r * 8, u32x2{pk(o[8] * inv, o[9] * inv), pk(o[10] * inv, o[11] * inv)});
    }

    {
        const int lane = lane_id(), r16 = lane & 15, q4 = lane >> 4;
        const unsigned paw = pkq * 5120 + (4 * q4 + ((lane >> 2) & 3)) * 160 + (lane & 3) * 40, awH = paw + 49152, awT = paw + 114688;
        f32x4 acc[2][5] = {};
        bf16x8 pf[2][5];
        u32x4 xv[2];
#define PO_A(rnd) (po_slot(rnd) < 49152 ? LP(paw) + po_slot(rnd) : po_slot(rnd) < 114688 ? LP(awH) + (po_slot(rnd) - 49152) : LP(awT) + (po_slot(rnd) - 114688))
#define PO_RW(rnd, nt) pf[(rnd) & 1][nt] = cat(ldstr(PO_A(rnd) + (nt) * 8), ldstr(PO_A(rnd) + (nt) * 8 + 2560))
        unsigned fo[2], fl[2];
#pragma unroll
        for (int nt = 0; nt < 5; ++nt) PO_RW(0, nt);
        SFOR(q, 17)
            if constexpr (q == 5)
                asm volatile("s_waitcnt vmcnt(8)\n\ts_mov_b64 exec, 1\n\tds_add_u32 %0, %1\n\ts_mov_b64 exec, -1" :: "v"(SCR + 4092), "v"(1u) : "memory");
            if constexpr (q == 0 || (q >= 4 && q <= 14 && !(q & 1))) BAR_L();
            if constexpr (q == 0) STAMP(4, 0);
            if constexpr (q == 6) STAMP(4, 1);
            if constexpr (q >= 1 && q < 5) { H_REQ(14 + 2 * q); H_REQ(15 + 2 * q); }
            if constexpr (q == 6) {
#pragma unroll
                for (int k = 0; k < 2; ++k) {
                    const int it = (k ? 512 + (w & 1) * 64 : w * 64) + lane, row = it / 10, c = it - row * 10;
                    fo[k] = (rb64 * 64 + row) * 1280 + cb80 * 160 + c * 16; fl[k] = row * 336 + c * 32;
                    xv[k] = *(const u32x4*)(x + fo[k]);
                }
            }
            if constexpr (q == 8) {
                STAMP(4, 2);
                const unsigned op = WS_O + (rb64 * 2 + ptt) * 40960;
#pragma unroll
                for (int s = 0; s < 4; ++s)
#pragma unroll
                    for (int t = 0; t < 2; ++t) hf[s][t] = __builtin_bit_cast(bf16x8, wld128(rs, op + (8 * pkq + 2 * s + (q4 >> 1)) * 1024 + (q4 & 1) * 512 + (t * 16 + r16) * 16));
#pragma unroll
                for (int t = 0; t < 2; ++t) {
                    const unsigned a = op + 32768 + (8 * pkq + 2 * q4) * 256 + (t * 16 + r16) * 8;
                    hf[4][t] = cat(__builtin_bit_cast(bf16x4, wld64(rs, a)), __builtin_bit_cast(bf16x4, wld64(rs, a + 256)));
                }
            }
            SBAR();
#pragma unroll
            for (int nt = 0; nt < 5; ++nt) {
#pragma unroll
                for (int t = 0; t < 2; ++t) {
                    acc[t][nt] = MFMA16X16(pf[q & 1][nt], hf[q % 12][t], acc[t][nt]); PIN(acc[t][nt]); SBAR();
                    if constexpr (q < 16) if (2 * nt + t < 5) PO_RW(q + 1, (2 * nt + t) % 5);
                    SBAR();
                }
            }
            if constexpr (q == 15) BAR_L();
        SEND
#undef PO_A
#undef PO_RW
        STAMP(4, 3);
#pragma unroll
        for (int Q = 0; Q < 10; ++Q) *(LAS f32x4*)(LP(pkq * 21504 + (ptt * 32 + r16) * 336 + q4 * 80) + (Q / 5) * 5376 + (Q % 5) * 16) = acc[Q / 5][Q % 5];
        BAR_L();
#pragma unroll
        for (int k = 0; k < 2; ++k) {
            if (k == 0 || w < 2) {
                f32x4 v0 = {bflo(xv[k][0]), bfhi(xv[k][0]), bflo(xv[k][1]), bfhi(xv[k][1])}, v1 = {bflo(xv[k][2]), bfhi(xv[k][2]), bflo(xv[k][3]), bfhi(xv[k][3])};
#pragma unroll
                for (int p = 0; p < 4; ++p) { v0 += *(LAS f32x4*)(LP(fl[k]) + p * 21504); v1 += *(LAS f32x4*)(LP(fl[k]) + p * 21504 + 16); }
                const u32x4 ov = {pk(v0[0], v0[1]), pk(v0[2], v0[3]), pk(v1[0], v1[1]), pk(v1[2], v1[3])};
                *(u32x4*)(out + fo[k]) = ov;
            }
        }
    }
#undef H_REQ
    STAMP(4, 4);
}

extern "C" void kernel_launch(void* const* d_in, const int* in_sizes, int n_in, void* d_out, int out_size, void* d_ws, size_t ws_size,
                              hipStream_t stream) {
    (void)hipFuncSetAttribute((const void*)k_block, hipFuncAttributeMaxDynamicSharedMemorySize, 163840);
    k_block<<<256, 768, 163840, stream>>>((const char*)d_in[0], (const char*)d_in[1], (const char*)d_in[2], (const char*)d_in[4], (const char*)d_in[5],
                                          (const char*)d_in[6], (char*)d_ws, (char*)d_out);
}
```

```cpp
#include <hip/hip_runtime.h>
#include <hip/hip_bf16.h>
#include <stdint.h>

typedef __attribute__((ext_vector_type(8))) short bf16x8;
typedef __attribute__((ext_vector_type(4))) short bf16x4;
typedef __attribute__((ext_vector_type(16))) float f32x16;
typedef __attribute__((ext_vector_type(4))) float f32x4;
typedef __attribute__((ext_vector_type(2))) float f32x2;
typedef __attribute__((ext_vector_type(4))) unsigned u32x4;
typedef __attribute__((ext_vector_type(2))) unsigned u32x2;
typedef __attribute__((ext_vector_type(2))) __bf16 bf2;
typedef __attribute__((address_space(1))) unsigned gu32;
typedef __amdgpu_buffer_rsrc_t rsrc_t;
#define LAS __attribute__((address_space(3)))
#define DI __device__ __forceinline__

constexpr unsigned WS_Q = 0;
constexpr unsigned WS_KV = WS_Q + 32 * 64 * 1536;
constexpr unsigned WS_H = WS_KV + 32 * 64 * 3072;
constexpr unsigned WS_O = WS_H + 64 * 96 * 1024;
constexpr unsigned WS_ST = WS_O + 64 * 40 * 1024;
constexpr unsigned WS_CQ = WS_ST + 16 * 10 * 128 * 16;
constexpr unsigned WS_CG = WS_CQ + 16 * 10 * 128 * 8;
constexpr unsigned WS_FLAG = WS_CG + 16 * 10 * 192 * 8;
constexpr unsigned WS_END = WS_FLAG + 65536;
constexpr unsigned OOB = 0x7fffff00u;
constexpr int F_GEN = 0;
constexpr int F_ACK = 64;
constexpr int F_H = 1024;
constexpr int F_O = 3072;
constexpr int F_QKV = 8192;
constexpr size_t WS_DBG = 64u << 20;
#define STAMP(kid, i) do {} while (0)

#define MFMA16(a, b, c) __builtin_amdgcn_mfma_f32_32x32x16_bf16((a), (b), (c), 0, 0, 0)
#define MFMA8(a, b, c) __builtin_amdgcn_mfma_f32_32x32x8bf16_1k((a), (b), (c), 0, 0, 0)
#define MFMA16X16(a, b, c) __builtin_amdgcn_mfma_f32_16x16x32_bf16((a), (b), (c), 0, 0, 0)
#define LP(a) ((LAS char*)(uintptr_t)(a))
#define SBAR() __builtin_amdgcn_sched_barrier(0)
#define PIN(x) asm volatile("" : "+v"(x))
#define LGKM0() __builtin_amdgcn_s_waitcnt(0xC07F)
#define BAR() asm volatile("s_barrier" ::: "memory")
#define BAR_L() do { asm volatile("" ::: "memory"); LGKM0(); BAR(); } while (0)
#define VM_BAR(N) asm volatile("s_waitcnt vmcnt(" #N ")\n\ts_barrier" ::: "memory")

DI void set_m0(unsigned lds) { asm volatile("s_mov_b32 m0, %0\n\ts_nop 0" :: "s"(lds) : "memory"); }
#define GLDS(voff, sbase, OFF) asm volatile("global_load_lds_dwordx4 %0, %1 offset:" #OFF :: "v"(voff), "s"(sbase) : "memory")
#define GLDS_SC1(voff, sbase, OFF) asm volatile("global_load_lds_dwordx4 %0, %1 offset:" #OFF " sc1" :: "v"(voff), "s"(sbase) : "memory")

DI int lane_id() { int l; asm volatile("v_mbcnt_lo_u32_b32 %0, -1, 0\n\tv_mbcnt_hi_u32_b32 %0, -1, %0" : "=v"(l)); return l; }
DI bf16x8 lds128(LAS char* p) { return *(LAS bf16x8*)p; }
DI bf16x4 lds64(LAS char* p) { return *(LAS bf16x4*)p; }
DI bf16x4 ldstr(LAS char* p) { return __builtin_amdgcn_ds_read_tr16_b64_v4i16((LAS bf16x4*)p); }
DI bf16x8 cat(bf16x4 a, bf16x4 b) { return __builtin_shufflevector(a, b, 0, 1, 2, 3, 4, 5, 6, 7); }
DI unsigned pk(float a, float b) { f32x2 f = {a, b}; return __builtin_bit_cast(unsigned, __builtin_convertvector(f, bf2)); }
DI float bflo(unsigned u) { return __uint_as_float(u << 16); }
DI float bfhi(unsigned u) { return __uint_as_float(u & 0xffff0000u); }
DI float dot2(unsigned a, unsigned b, float c) { return __builtin_amdgcn_fdot2_f32_bf16(__builtin_bit_cast(bf2, a), __builtin_bit_cast(bf2, b), c, false); }
DI float swap32_add(float v) { auto r = __builtin_amdgcn_permlane32_swap(__float_as_uint(v), __float_as_uint(v), false, false); return __uint_as_float(r[0]) + __uint_as_float(r[1]); }
DI float swap32_max(float v) { auto r = __builtin_amdgcn_permlane32_swap(__float_as_uint(v), __float_as_uint(v), false, false); return __builtin_fmaxf(__uint_as_float(r[0]), __uint_as_float(r[1])); }
DI float addf(float a, float b) { float r; asm("v_add_f32 %0, %1, %2" : "=v"(r) : "v"(a), "v"(b)); return r; }
DI float mulf(float a, float b) { float r; asm("v_mul_f32 %0, %1, %2" : "=v"(r) : "v"(a), "v"(b)); return r; }
DI float fmaf1(float a, float b, float c) { float r; asm("v_fma_f32 %0, %1, %2, %3" : "=v"(r) : "v"(a), "v"(b), "v"(c)); return r; }
DI void wst128(rsrc_t rs, unsigned off, u32x4 v) { __builtin_amdgcn_raw_buffer_store_b128(v, rs, off, 0, 16); }
DI void wst64(rsrc_t rs, unsigned off, u32x2 v) { __builtin_amdgcn_raw_buffer_store_b64(v, rs, off, 0, 16); }
DI u32x4 wld128(rsrc_t rs, unsigned off) { return __builtin_amdgcn_raw_buffer_load_b128(rs, off, 0, 16); }
DI u32x2 wld64(rsrc_t rs, unsigned off) { return __builtin_amdgcn_raw_buffer_load_b64(rs, off, 0, 16); }
DI unsigned wld32(rsrc_t rs, unsigned off) { return __builtin_amdgcn_raw_buffer_load_b32(rs, off, 0, 16); }
#define POLL_SLEEP() do { __builtin_amdgcn_s_sleep(4); asm volatile("" ::: "memory"); } while (0)
DI void wait_tags(gu32* flags, int word, unsigned tag) {
    for (int spin = 0; spin < (1 << 18); ++spin) {
        const unsigned v = __hip_atomic_load(flags + word, __ATOMIC_RELAXED, __HIP_MEMORY_SCOPE_AGENT);
        if (__all(v == tag)) break;
        POLL_SLEEP();
    }
    asm volatile("" ::: "memory");
}
DI void wait_tags2(gu32* flags, int word0, int word1, unsigned tag) {
    for (int spin = 0; spin < (1 << 18); ++spin) {
        const unsigned v0 = __hip_atomic_load(flags + word0, __ATOMIC_RELAXED, __HIP_MEMORY_SCOPE_AGENT);
        const unsigned v1 = __hip_atomic_load(flags + word1, __ATOMIC_RELAXED, __HIP_MEMORY_SCOPE_AGENT);
        if (__all(v0 == tag && v1 == tag)) break;
        POLL_SLEEP();
    }
    asm volatile("" ::: "memory");
}
DI void publish(gu32* flags, int word, bool on, unsigned tag) { if (on) __hip_atomic_store(flags + word, tag, __ATOMIC_RELAXED, __HIP_MEMORY_SCOPE_AGENT); }

DI float max16(const f32x16& s) {
    float a = __builtin_fmaxf(__builtin_fmaxf(s[0], s[1]), s[2]), b = __builtin_fmaxf(__builtin_fmaxf(s[3], s[4]), s[5]);
    a = __builtin_fmaxf(__builtin_fmaxf(a, s[6]), s[7]); b = __builtin_fmaxf(__builtin_fmaxf(b, s[8]), s[9]);
    a = __builtin_fmaxf(__builtin_fmaxf(a, s[10]), s[11]); b = __builtin_fmaxf(__builtin_fmaxf(b, s[12]), s[13]);
    a = __builtin_fmaxf(__builtin_fmaxf(a, s[14]), s[15]);
    return swap32_max(__builtin_fmaxf(a, b));
}

template <int I> struct IC { static constexpr int v = I; };
template <int I, int N, class F> DI void sfor(F&& f) { if constexpr (I < N) { f(IC<I>{}); sfor<I + 1, N>(f); } }
#define SFOR(var, N) sfor<0, N>([&](auto var##_) __attribute__((always_inline)) { constexpr int var = decltype(var##_)::v;
#define SEND });
#define VM0() __builtin_amdgcn_s_waitcnt(0x0F70)

constexpr unsigned WRB = 81920, SCR = 155648, KVB = 0;
constexpr unsigned po_slot_f(int r) {
    constexpr int m[17] = {0, 1, 2, 3, 4, 5, 0, 1, 2, 5, 0, 1, 6, 3, 4, 2, 5};
    return m[r] < 4 ? 73728u + m[r] * 20480u : m[r] == 4 ? 49152u : m[r] == 5 ? 0u : 24576u;
}
template <int R> struct PoSlot { static constexpr unsigned v = po_slot_f(R); };
#define po_slot(r) (PoSlot<(r)>::v)
DI int first_stage(int j, int cb) { return ((((j >> 1) + 2 * (j & 1) + ((cb >> 2) & 1)) & 3) * 5 + 1) >> 1; }
DI int kstage(int kt, int rk) { const int k = kt + rk; return k >= 10 ? k - 10 : k; }
#define GATHER2(S0, S1, G, OFF, STRIDE) do { unsigned bad_ = 0; \
        _Pragma("unroll") for (int k_ = 0; k_ < 5; ++k_) { PIN(G[k_]); bad_ |= (G[k_][1] ^ tag) | (G[k_][3] ^ tag); } \
        S0 = addf(addf(addf(addf(__uint_as_float(G[0][0]), __uint_as_float(G[1][0])), __uint_as_float(G[2][0])), __uint_as_float(G[3][0])), __uint_as_float(G[4][0])); \
        S1 = addf(addf(addf(addf(__uint_as_float(G[0][2]), __uint_as_float(G[1][2])), __uint_as_float(G[2][2])), __uint_as_float(G[3][2])), __uint_as_float(G[4][2])); \
        bool ok_ = bad_ == 0; \
        for (int spin_ = 0; spin_ < (1 << 18) && __builtin_expect(!__all(ok_), 0); ++spin_) { \
            POLL_SLEEP(); S0 = 0.f; S1 = 0.f; ok_ = true; \
            _Pragma("unroll") for (int k_ = 0; k_ < 5; ++k_) { const u32x4 g_ = wld128(rs, (OFF) + k_ * (STRIDE)); \
                S0 += __uint_as_float(g_[0]); S1 += __uint_as_float(g_[2]); ok_ = ok_ && g_[1] == tag && g_[3] == tag; } } \
        S0 = swap32_add(S0); S1 = swap32_add(S1); } while (0)
#define GATHER_REQ(G, OFF, STRIDE) do { _Pragma("unroll") for (int k_ = 0; k_ < 5; ++k_) \
        asm volatile("global_load_dwordx4 %0, %1, %2 sc1" : "=v"(G[k_]) : "v"(OFF), "s"(ws + k_ * (STRIDE)) : "memory"); } while (0)
DI unsigned x_addr(int row, int p, int h) { return row * 128 + (((2 * p + h) ^ ((row >> 1) & 7)) << 4); }
DI unsigned wq_addr(int lane, int nt, bool vt) {
    const int c = lane & 3;
    return WRB + (lane >> 5) * 2048 + (nt >> 1) * 512 + ((lane >> 2) & 3) * 128 + (((nt ^ (lane >> 3)) & 1) << 6) + ((lane >> 4) & 1) * 32 + (vt ? c : ((c & 1) << 1) | (c >> 1)) * 8;
}
DI unsigned gu_addr(int lane, int nt) {
    const int c = lane & 3, gi = (lane >> 4) & 1;
    return WRB + gi * 3072 + (lane >> 5) * 1536 + ((lane >> 2) & 3) * 192 + (nt >> 1) * 64 + (((nt ^ gi) & 1) << 5) + (((c & 1) << 1) | (c >> 1)) * 8;
}
#define VMW(N) asm volatile("s_waitcnt vmcnt(" #N ")" ::: "memory")
#define NQG 3
constexpr int UQ = 2 * NQG, UG = NQG + 10;
constexpr int g_stage(int u) { return u - UG < 5 ? 5 + u - UG : NQG + u - UG - 5; }
#define XB(L) (((L) + NQG) % 5 * 16384)
constexpr int w_count(int u) { return u < UQ ? 10 : u < UG ? 8 : u < 20 ? 12 : 0; }
constexpr int x_lo(int g) { return g < UQ - 4 ? g + 4 : g <= UG - 4 ? 2 * (g + 4 - NQG) : g < UG + 2 ? 20 : 20 + 2 * (g - UG - 2) < 30 - 2 * NQG ? 20 + 2 * (g - UG - 2) : 30 - 2 * NQG; }
constexpr int req_count(int g) { return g < 0 ? 18 : 4 * (x_lo(g + 1) - x_lo(g)) + w_count(g + 3); }
template <int N> DI void vm_bar() { asm volatile("s_waitcnt vmcnt(%0)\n\ts_barrier" :: "n"(N) : "memory"); }
struct Ids { int rb, cb, hd, qb, rb64, cb80, rk, i, sx, sw; };

#define LOADER_SETUP() \
        const unsigned vA0 = (lane >> 3) * 1280 + (((lane & 7) ^ (lane >> 4)) << 4), vA1 = (vA0 ^ 64) + 10240 - 1024; \
        const unsigned vW0 = ((lane >> 3) & 3) * 3840 + (lane >> 5) * 128 + (((lane & 7) ^ ((lane >> 2) & 4)) << 4), vW1 = vW0 + 15360 - 1024; \
        const char* xa = x + (size_t)(rb * 128 + lw * 32) * 1280; \
        const char* wb = wqkv + (size_t)lw * 16 * 3840 + (cb < 15 ? cb : 14) * 256
#define H_4(V0, V1, P, STEP) do { GLDS(V0, P, 0); GLDS(V1, P, 1024); GLDS(V0, (P) + (STEP) - 2048, 2048); GLDS(V1, (P) + (STEP) - 2048, 3072); } while (0)
#define H_X(v) do { set_m0(XB((v) >> 1) + ((v) & 1) * 8192 + lw * 4096); H_4(vA0, vA1, xa + kstage((v) < 20 ? (v) >> 1 : ((v) >> 1) - 10 + NQG, rk) * 128 + ((v) & 1) * 81920, 20480); } while (0)
#define H_Q(v, dst) do { set_m0(WRB + (dst) + lw * 4096); H_4(vW0, vW1, wb + (kstage((v) >> 1, rk) * 2 + ((v) & 1)) * 122880, 30720); } while (0)
#define NEXT_X(v) ((v) == 65536 ? 0u : (v) + 16384)
#define NEXT_W(v) ((v) == 49152 ? 0u : (v) + 24576)
DI void helper_role(const char* x, const char* wqkv, const char* wo, const char* wg, const char* wu, const char* wd, char* ws, rsrc_t rs, gu32* flags,
                    const Ids id, int lw) {
    const int w = lw;
    __builtin_amdgcn_s_setprio(3);
    unsigned gen, tag;
    asm volatile("global_load_dword %0, %1, %2 sc1" : "=v"(gen) : "v"(0), "s"(flags + F_GEN) : "memory");
    const int lane = lane_id(), r = lane & 31, h = lane >> 5;
    const int rb = id.rb, cb = id.cb, rk = id.rk, cbl = cb < 15 ? cb : 14;
    const unsigned voff = lane * 16;
    if (lw < 2) {
        LOADER_SETUP();
        unsigned gv[3];
#pragma unroll
        for (int p = 0; p < 3; ++p) { const int P = 64 * p + lane, row = P / 12; gv[p] = row * 3072 + (((P - row * 12) ^ (2 * lw)) << 4) + 2048 - p * 1024; }
        const char* gb = (lw ? wu : wg) + cb * 192 - 2048;
#define H_G(v, dst) do { const char* q_ = gb + (size_t)(kstage((v) >> 1, rk) * 2 + ((v) & 1)) * 98304; _Pragma("unroll") for (int st_ = 0; st_ < 2; ++st_) { \
        set_m0(WRB + (dst) + st_ * 6144 + lw * 3072); GLDS(gv[0], q_ + st_ * 49152, 0); GLDS(gv[1], q_ + st_ * 49152, 1024); GLDS(gv[2], q_ + st_ * 49152, 2048); } } while (0)
#define H_W(u) do { constexpr unsigned d_ = (u) % 3 * 24576; \
        if constexpr ((u) < UQ) { H_Q(u, d_); H_G(u, d_ + 8192); } else if constexpr ((u) < UG) { H_Q(2 * ((u) - NQG), d_); H_Q(2 * ((u) - NQG) + 1, d_ + 8192); } \
        else { H_G(2 * g_stage(u), d_); H_G(2 * g_stage(u) + 1, d_ + 12288); } } while (0)
        H_G(0, 8192); H_W(1);
        VM_BAR(10);
        H_X(2); H_X(3); H_W(2);
        SFOR(g, 19)
            vm_bar<req_count(g - 1)>();
            if constexpr (x_lo(g + 1) > x_lo(g)) H_X(x_lo(g));
            if constexpr (x_lo(g + 1) > x_lo(g) + 1) H_X(x_lo(g) + 1);
            if constexpr (g < 17) H_W(g + 3);
        SEND
        asm volatile("v_readfirstlane_b32 %0, %1" : "=s"(tag) : "v"(gen));
        tag = tag * 2654435761u + 0x9E3779B9u;
#undef H_G
#undef H_W
    } else {
        const int sv = lw - 2;
        unsigned dummy;
        {
            const char* pc; int o0, sz;
            asm volatile("s_getpc_b64 %0\n1:\n\ts_mov_b32 %1, k_block-1b\n\ts_mov_b32 %2, k_end-k_block" : "=s"(pc), "=s"(o0), "=s"(sz));
            const int o = ((id.i * 2 + sv) * 6 + (lane < 6 ? lane : 5)) * 128;
            asm volatile("global_load_dword %0, %1, %2" : "=v"(dummy) : "v"(o < sz ? o : sz), "s"(pc + o0) : "memory");
        }
        VM_BAR(1);
        asm volatile("v_readfirstlane_b32 %0, %1" : "=s"(tag) : "v"(gen));
        tag = tag * 2654435761u + 0x9E3779B9u;
        unsigned wsl = 0;
        bf16x8 fw[5][2];
        float cs[5] = {0.f, 0.f, 0.f, 0.f, 0.f};
#pragma unroll 1
        for (int g = 0; g < 7; ++g) {
            if (g == 2 * id.sw + 1 || g == 2 * id.sw + 2) {
#pragma unroll
                for (int p = 0; p < 2; ++p)
#pragma unroll
                    for (int e = 0; e < 4; ++e)
#pragma unroll
                        for (int t = 0; t < 5; ++t) cs[t] = dot2(__builtin_bit_cast(u32x4, fw[t][p])[e], 0x3F803F80u, cs[t]);
                if (g == 2 * id.sw + 2) {
#pragma unroll
                    for (int t = 0; t < 5; ++t) cs[t] = swap32_add(cs[t]);
                    const int ka = kstage(id.sw, rk);
                    const unsigned o = WS_CG + ((cb * 10 + ka) * 192 + sv * 96 + lane) * 8;
                    wst64(rs, cb < 15 ? WS_CQ + ((cb * 10 + ka) * 128 + sv * 64 + lane) * 8 : OOB, u32x2{__float_as_uint(h ? cs[1] : cs[0]), tag});
                    wst64(rs, o, u32x2{__float_as_uint(h ? cs[3] : cs[2]), tag});
                    wst64(rs, h == 0 ? o + 512 : OOB, u32x2{__float_as_uint(cs[4]), tag});
                }
            }
            if ((g >> 1) == id.sx && id.sx < 3) {
                const int row = sv * 64 + (g & 1) * 32 + r;
                float sx = 0.f, sq = 0.f;
#pragma unroll
                for (int p = 0; p < 4; ++p) {
                    const u32x4 f = __builtin_bit_cast(u32x4, lds128(LP(x_addr(row, p, h) + XB(id.sx))));
#pragma unroll
                    for (int e = 0; e < 4; ++e) { sx = dot2(f[e], 0x3F803F80u, sx); sq = dot2(f[e], f[e], sq); }
                }
                sx = swap32_add(sx); sq = swap32_add(sq);
                wst128(rs, h == 0 ? WS_ST + ((rb * 10 + kstage(id.sx, rk)) * 128 + row) * 16 : OOB, u32x4{__float_as_uint(sx), tag, __float_as_uint(sq), tag});
            }
            if ((g >> 1) == id.sw) {
#pragma unroll
                for (int t = 0; t < 5; ++t) {
                    const unsigned a = (t < 2 ? wq_addr(lane, sv * 2 + t, cb >= 10) : gu_addr(lane, 3 * sv + t - 2) + 8192) + wsl;
#pragma unroll
                    for (int p = 0; p < 2; ++p) fw[t][p] = t < 2 ? cat(ldstr(LP(a) + p * 4096), ldstr(LP(a) + p * 4096 + 1024)) : cat(ldstr(LP(a) + p * 6144), ldstr(LP(a) + p * 6144 + 768));
                }
            }
            BAR_L();
            wsl = NEXT_W(wsl);
        }
        {
            const int vh = ((cb - 10) * 128 + 19) / 20 + (lane >> 3);
            const unsigned v = (lane & 1) ? 0u : 0x3F803F80u;
            const unsigned o = (cb >= 10 && cb < 15 && vh * 20 < (cb - 9) * 128) ? WS_KV + (vh * 64 + rb * 4 + sv * 2) * 3072 + 1536 + ((lane >> 1) & 3) * 384 + 320 + (lane & 1) * 16 : OOB;
            wst128(rs, o, u32x4{v, v, v, v}); wst128(rs, o + 3072, u32x4{v, v, v, v});
        }
        u32x4 gs[2][5], gc[5];
        const unsigned so = WS_ST + ((rb * 10 + h * 5) * 128 + sv * 64 + r) * 16, co = WS_CQ + ((cbl * 10 + h * 5) * 128 + sv * 64 + 2 * r) * 8;
        const unsigned cg = WS_CG + ((cb * 10 + h * 5) * 192 + sv * 64 + 2 * r) * 8, cg2 = cg - sv * 512 + 1024;
        float s0, s1;
        SFOR(g_, 12)
            constexpr int g = g_ + 7;
            BAR_L();
            if constexpr (g == UG - 6) GATHER_REQ(gs[0], so, 2048);
            if constexpr (g == UG - 5) GATHER_REQ(gs[1], so + 512, 2048);
            if constexpr (g == UG - 4) GATHER_REQ(gc, co, 1024);
            if constexpr (g == UG - 3 || g == UG - 2) {
                constexpr int t = g - (UG - 3);
                if (t) VMW(5); else { VMW(10); PIN(dummy); }
                GATHER2(s0, s1, gs[t], so + t * 512, 2048);
                const float mu = s0 * (1.f / 640.f);
                *(LAS float*)LP(SCR + h * 512 + (sv * 64 + t * 32 + r) * 4) = h ? -mu : __builtin_amdgcn_rsqf(__builtin_fmaxf(__builtin_fmaf(-mu, mu, s1 * (1.f / 640.f)), 0.f) + 1e-5f);
            }
            if constexpr (g == UG - 1) {
                VMW(0);
                GATHER2(s0, s1, gc, co, 1024);
                *(LAS f32x2*)LP(SCR + 1024 + (sv * 64 + 2 * r) * 4) = f32x2{s0, s1};
            }
            if constexpr (g == 14) { GATHER_REQ(gc, cg, 1536); GATHER_REQ(gs[0], cg2, 1536); }
            if constexpr (g == 16) {
                VMW(0);
                GATHER2(s0, s1, gc, cg, 1536);
                *(LAS f32x2*)LP(SCR + 1536 + (sv * 64 + 2 * r) * 4) = f32x2{s0, s1};
            }
            if constexpr (g == 17) if (sv == 0) { GATHER2(s0, s1, gs[0], cg2, 1536); *(LAS f32x2*)LP(SCR + 1536 + (128 + 2 * r) * 4) = f32x2{s0, s1}; }
            if constexpr (g == UG + 3) if (sv == 1) {
                const int ty = cb / 5, cc = cb - ty * 5, gg = (r >> 1) * 5 + (r & 1) * 2 + 2 * h;
                const int blk = ty == 2 ? (r * 20 + 19 * h) >> 7 : gg >> 4;
                publish(flags, F_QKV + r * 128 + ((ty == 0 ? 4 : ty == 1 ? 0 : 2) + h) * 16 + rb, ty < 3 && blk == cc, tag);
                publish(flags, F_ACK + blockIdx.x, lane == 0, tag);
            }
        SEND
    }
    wait_tags2(flags, F_QKV + id.hd * 128 + lane, F_QKV + id.hd * 128 + 64 + (lane & 1) * 16 + id.qb * 2 + ((lane >> 1) & 1), tag);
    { const char* p_ = ws + WS_Q + (id.hd * 64 + id.qb * 8 + lw * 2) * 1536; set_m0(49152 + lw * 3072); GLDS_SC1(voff, p_, 0); GLDS_SC1(voff, p_, 1024); GLDS_SC1(voff, p_, 2048); }
    const char* kv = ws + WS_KV + (size_t)id.hd * 196608 + lw * 6144;
#define KV_DMA(s) do { const char* p_ = kv + (s) * 24576; set_m0(KVB + (s) % 3 * 24576 + lw * 6144); GLDS_SC1(voff, p_, 0); GLDS_SC1(voff, p_, 1024); GLDS_SC1(voff, p_, 2048); \
        set_m0(KVB + (s) % 3 * 24576 + lw * 6144 + 3072); GLDS_SC1(voff, p_ + 3072, 0); GLDS_SC1(voff, p_ + 3072, 1024); GLDS_SC1(voff, p_ + 3072, 2048); } while (0)
    KV_DMA(0);
    unsigned pv[5];
#define PO_VOFF(ROW) do { _Pragma("unroll") for (int ii = 0; ii < 5; ++ii) { \
        const int P = ii * 64 + lane, rho = P / 10, c = P - rho * 10, u = rho >> 4, qq = (rho >> 2) & 3, e = rho & 3; pv[ii] = (ROW) * 1280 + c * 16 + 3072 - (ii & 3) * 1024; } } while (0)
#define PO_DMA(rnd, src) do { const char* p_ = (src); set_m0(po_slot(rnd) + lw * 5120); GLDS(pv[0], p_, 0); GLDS(pv[1], p_, 1024); GLDS(pv[2], p_, 2048); GLDS(pv[3], p_, 3072); \
        set_m0(po_slot(rnd) + lw * 5120 + 4096); GLDS(pv[4], p_, 0); } while (0)
#define PO_REG(k, src) do { const char* p_ = (src); _Pragma("unroll") for (int ii = 0; ii < 5; ++ii) \
        asm volatile("global_load_dwordx4 %0, %1, %2" : "=v"(pr[k][ii]) : "v"(pv[ii]), "s"(p_ + (ii & 3) * 1024) : "memory"); } while (0)
    PO_VOFF(8 * qq + 4 * u + e);
    const char* pw = wd + (size_t)lw * 491520 + id.cb80 * 160 - 3072;
    const char* po = wo + (size_t)lw * 204800 + id.cb80 * 160 - 3072;
    const int prot = 3 * (id.rb64 & 3);
#define PO_W(rnd) (pw + ((rnd) + prot >= 12 ? (rnd) + prot - 12 : (rnd) + prot) * 40960)
    u32x4 pr[6][5];
    VM_BAR(0);
    KV_DMA(1);
    SFOR(s, 8)
        if constexpr (s == 1) if (lw == 0) publish(flags, F_H + (rb * 2 + lane) * 32 + cb, lane < 2, tag);
        if constexpr (s < 2) { PO_DMA(2 * s, PO_W(2 * s)); PO_DMA(2 * s + 1, PO_W(2 * s + 1)); }
        if constexpr (s >= 2 && s < 5) { PO_REG(2 * s - 4, PO_W(2 * s + 2)); PO_REG(2 * s - 3, PO_W(2 * s + 3)); }
        if constexpr (s == 3) {
            if (lw == 1) wait_tags(flags, F_H + id.rb64 * 32 + (lane & 15), tag);
            if (lw == 2 && blockIdx.x == 0) {
                for (int spin = 0; spin < (1 << 18); ++spin) {
                    const u32x4 a = wld128(rs, WS_FLAG + F_ACK * 4 + lane * 16);
                    if (__all(a[0] == tag && a[1] == tag && a[2] == tag && a[3] == tag)) break;
                    POLL_SLEEP();
                }
                publish(flags, F_GEN, lane == 0, tag);
            }
        }
        if constexpr (s == 5) {
            unsigned dummy;
            asm volatile("global_load_dword %0, %1, %2\n\ts_waitcnt vmcnt(0)" : "=&v"(dummy) : "v"((lane < 50 ? lane : 49) * 128), "s"(wo + (size_t)(id.i * 4 + lw) * 6400) : "memory");
        }
        if constexpr (s == 5) if (lw == 3) {
            const int k = 8 + (lane >> 4) + prot;
            unsigned d = wld32(rs, WS_H + ((id.rb64 * 2 + (id.cb80 & 1)) * 96 + (id.cb80 >> 1) * 24 + (k >= 12 ? k - 12 : k) * 2) * 1024 + (lane & 15) * 128);
            PIN(d);
        }
        if constexpr (s == 7) PO_DMA(4, PO_W(4));
        VM_BAR(0);
        if constexpr (s < 6) KV_DMA(s + 2);
    SEND
    PO_DMA(5, PO_W(5));
    PO_VOFF(20 * (qq >> 1) + 4 * (qq & 1) + 8 * u + e);
#define PO_PUT(rnd) do { _Pragma("unroll") for (int ii = 0; ii < 5; ++ii) { PIN(pr[(rnd) - 6][ii]); *(LAS u32x4*)(LP(po_slot(rnd) + lw * 5120 + voff) + ii * 1024) = pr[(rnd) - 6][ii]; } } while (0)
#define PO_O(k) (po + (((k) + id.cb80) & 3) * 51200)
    BAR();
    {
        const unsigned op = WS_O + (id.qb * 8 + lw * 2) * 40960;
        const u32x4 o0 = *(LAS u32x4*)LP(SCR + lw * 2048 + voff), o1 = *(LAS u32x4*)LP(SCR + lw * 2048 + 1024 + voff), o2 = *(LAS u32x4*)LP(69632 + lw * 512 + (lane & 31) * 16);
        wst128(rs, op + id.hd * 1024 + voff, o0); wst128(rs, op + 40960 + id.hd * 1024 + voff, o1);
        wst128(rs, h ? OOB : op + (r >> 4) * 40960 + 32768 + id.hd * 256 + (r & 15) * 16, o2);
    }
    PO_PUT(6);
    PO_DMA(12, PO_O(0));
    LGKM0(); VM_BAR(8);
    PO_PUT(7);
    VMW(5);
    publish(flags, F_O + (id.qb * 4 + lw) * 32 + id.hd, lane == 0, tag);
    PO_PUT(8); PO_DMA(13, PO_O(1));
    BAR_L();
    PO_PUT(9); PO_PUT(10);
    BAR_L();
    PO_PUT(11); PO_DMA(14, PO_O(2)); PO_DMA(15, PO_O(3));
    PO_VOFF(20 * (4 * (qq >> 1) + 2 * (qq & 1) + u) + 16 + e);
    LGKM0(); VM_BAR(15);
    PO_DMA(16, po);
    if (lw == 1) wait_tags(flags, F_O + id.rb64 * 32 + r, tag);
    VM_BAR(0);
#undef PO_O
#undef PO_PUT
#undef KV_DMA
#undef PO_VOFF
#undef PO_DMA
#undef PO_REG
#undef PO_W
}

#define G_RDO(p, a, A, B, O) ((a) == 1 ? cat(ldstr(LP(B) + (O) + (p) * 6144), ldstr(LP(B) + (O) + (p) * 6144 + 768)) : cat(ldstr(LP(A) + (O) + (p) * 6144 + (a) * 32), ldstr(LP(A) + (O) + (p) * 6144 + (a) * 32 + 768)))
#define G_RD(p, a, A, B) G_RDO(p, a, A, B, 0)
#define G_MM(p, a) do { gacc[a] = MFMA16(gw[p][a], gx[p], gacc[a]); PIN(gacc[a]); SBAR(); } while (0)
#define G_ADDR() const int tt = w & 3, fh = w >> 2; unsigned gax = x_addr(tt * 32 + r, 0, h); \
        unsigned awA = gu_addr(lane, 3 * fh), awB = gu_addr(lane, 3 * fh + 1)
#define GAX(p, xs) ((gax ^ ((p) * 32)) + (xs))
template <bool VT>
DI void front_phase(char* ws, rsrc_t rs, int rb, int cb, int w, f32x16 (&gacc)[3], bf16x8 (&gx)[4], bf16x8 (&gw)[4][3]) {
    const int lane = lane_id(), r = lane & 31, h = lane >> 5;
    G_ADDR();
    unsigned aq = wq_addr(lane, 2 * fh, VT);
    PIN(gax); PIN(awA); PIN(awB); PIN(aq);
    f32x16 acc[2] = {};
    bf16x8 qw[4][2];
    gacc[0] = f32x16{}; gacc[1] = f32x16{}; gacc[2] = f32x16{};
#define Q_RD(p, A) cat(ldstr(LP(A) + (p) * 4096), ldstr(LP(A) + (p) * 4096 + 1024))
#define Q_MM(p, n) do { acc[n] = VT ? MFMA16(gx[p], qw[p][n], acc[n]) : MFMA16(qw[p][n], gx[p], acc[n]); PIN(acc[n]); SBAR(); } while (0)
#define QG_UNIT(X0, X1) do { BAR_L(); const unsigned q0_ = aq + wsl, q1_ = q0_ ^ 64, a_ = awA + 8192 + wsl, c_ = awB + 8192 + wsl; \
        _Pragma("unroll") for (int p = 0; p < 2; ++p) { \
            Q_MM(p, 0); qw[p][0] = Q_RD(p, q0_); SBAR(); G_MM(p, 0); gw[p][0] = G_RD(p, 0, a_, c_); SBAR(); Q_MM(p, 1); qw[p][1] = Q_RD(p, q1_); SBAR(); \
            G_MM(p, 1); gw[p][1] = G_RD(p, 1, a_, c_); SBAR(); G_MM(p, 2); gw[p][2] = G_RD(p, 2, a_, c_); gx[p] = lds128(LP(p ? (X1) : (X0))); SBAR(); } \
        wsl = NEXT_W(wsl); } while (0)
    BAR();
    STAMP(0, 1);
#pragma unroll
    for (int p = 0; p < 2; ++p) {
        gx[p] = lds128(LP(GAX(p, XB(0)))); qw[p][0] = Q_RD(p, aq); qw[p][1] = Q_RD(p, aq ^ 64);
#pragma unroll
        for (int a = 0; a < 3; ++a) gw[p][a] = G_RDO(p, a, awA, awB, 8192);
    }
    unsigned xs = XB(0), wsl = 24576;
#pragma unroll 1
    for (int u = 0; u < UQ - 2; u += 2) { QG_UNIT(GAX(2, xs), GAX(3, xs)); xs = NEXT_X(xs); QG_UNIT(GAX(0, xs), GAX(1, xs)); if (u == 2) STAMP(5, 1); }
    QG_UNIT(GAX(2, xs), GAX(3, xs));
    STAMP(5, 2);
    BAR_L();
    constexpr unsigned WQ0 = UQ % 3 * 24576, XQ0 = XB(NQG), WG0 = UG % 3 * 24576, WG1 = (UG + 1) % 3 * 24576;
#pragma unroll
    for (int p = 0; p < 2; ++p) {
        Q_MM(p, 0); qw[p][0] = Q_RD(p, aq + WQ0); SBAR(); G_MM(p, 0); qw[p + 2][0] = Q_RD(p + 2, aq + WQ0); SBAR(); Q_MM(p, 1); qw[p][1] = Q_RD(p, (aq ^ 64) + WQ0); SBAR();
        G_MM(p, 1); qw[p + 2][1] = Q_RD(p + 2, (aq ^ 64) + WQ0); SBAR(); G_MM(p, 2); gx[p] = lds128(LP(GAX(p, XQ0))); gx[p + 2] = lds128(LP(GAX(p + 2, XQ0))); SBAR();
    }
    xs = XB(NQG + 1); wsl = (UQ + 1) % 3 * 24576;
#pragma unroll 1
    for (int u = UQ; u < UG - 1; ++u) {
        BAR_L();
        const unsigned q0 = aq + wsl, q1 = q0 ^ 64;
#pragma unroll
        for (int p = 0; p < 4; ++p) { Q_MM(p, 0); qw[p][0] = Q_RD(p, q0); SBAR(); Q_MM(p, 1); qw[p][1] = Q_RD(p, q1); gx[p] = lds128(LP(GAX(p, xs))); SBAR(); }
        xs = NEXT_X(xs); wsl = NEXT_W(wsl);
    }
    STAMP(5, 3);
    BAR_L();
    awA += WG0; awB += WG0;
#pragma unroll
    for (int p = 0; p < 4; ++p) {
        Q_MM(p, 0); gw[p][0] = G_RD(p, 0, awA, awB); gw[p][1] = G_RD(p, 1, awA, awB); SBAR();
        Q_MM(p, 1); gw[p][2] = G_RD(p, 2, awA, awB); gx[p] = lds128(LP(GAX(p, XB(5)))); SBAR();
    }
#undef Q_RD
#undef Q_MM
#undef QG_UNIT
    STAMP(0, 2);
    BAR_L();
    awA += WG1 - WG0; awB += WG1 - WG0;
    PIN(gax);
    const int ln = lane_id(), r4 = (ln & 31) * 4, h16 = (ln >> 5) * 16;
    const unsigned rec = cb < 5 ? 1536 : 3072;
    const int tile = rb * 4 + tt;
    float sa, sb;
    f32x4 e0, e1;
    u32x4 ov;
    if (!VT) {
        sa = *(LAS float*)LP(SCR + tt * 128 + r4); sb = *(LAS float*)LP(SCR + 512 + tt * 128 + r4);
        if (cb < 5) sa *= 0.22360679774997896f * 1.4426950408889634f;
    }
#pragma unroll
    for (int p = 0; p < 4; ++p) {
        const int n = p >> 1, o = p & 1;
        const f32x16& c = acc[n];
#pragma unroll
        for (int a = 0; a < 3; ++a) {
            if (a < 2) {
                if (!VT) e1 = *(LAS f32x4*)(LP(SCR + 1024 + fh * 256 + h16) + p * 64 + a * 32);
                else {
                    e0 = *(LAS f32x4*)(LP(SCR + tt * 128 + h16) + o * 64 + a * 32); e1 = *(LAS f32x4*)(LP(SCR + tt * 128 + h16) + 512 + o * 64 + a * 32);
                    if (a == 0) sb = *(LAS float*)(LP(SCR + 1024 + fh * 256 + r4) + n * 128);
                }
            }
            SBAR();
            G_MM(p, a);
            if (p > 0) { gw[p - 1][a] = G_RD(p - 1, a, awA, awB); if (a == 2) gx[p - 1] = lds128(LP(GAX(p - 1, XB(6)))); }
            if (p == 3) { gw[3][a] = G_RD(3, a, awA, awB); if (a == 2) gx[3] = lds128(LP(GAX(3, XB(6)))); }
            if (a < 2) {
                float v[4];
#pragma unroll
                for (int e = 0; e < 4; ++e) v[e] = mulf(fmaf1(e1[e], sb, c[8 * o + 4 * a + e]), VT ? e0[e] : sa);
                ov[2 * a] = pk(v[0], v[1]); ov[2 * a + 1] = pk(v[2], v[3]);
                PIN(ov);
            } else if (!VT) {
                unsigned o2[2];
#pragma unroll
                for (int u = 0; u < 2; ++u) {
                    const int Gc = (cb % 5) * 16 + fh * 8 + p * 2 + u, P = Gc / 5, gp = Gc - P * 5;
                    o2[u] = (cb < 5 ? WS_Q : WS_KV) + tile * rec + (2 * P + (gp > 2)) * 64 * rec + (gp > 2 ? gp - 3 : gp) * 512 + (gp == 2);
                }
                const unsigned off = (h16 ? o2[1] : o2[0]) + r4 * 4;
                wst128(rs, off & ~1u, ov);
                if ((o2[0] | o2[1]) & 1) wst128(rs, (off & 1) ? off - 1 + 64 * rec : OOB, ov);
            } else {
                const int col = (cb - 10) * 128 + fh * 64 + n * 32 + (r4 >> 2), vh = col / 20, d = col - vh * 20;
                wst128(rs, cb < 15 ? WS_KV + (vh * 64 + tile) * 3072 + 1536 + h16 * 24 + d * 16 + o * 768 : OOB, ov);
            }
            SBAR();
        }
    }
}

extern "C" __global__ __launch_bounds__(768, 3) void k_block(const char* __restrict__ x, const char* __restrict__ wqkv, const char* __restrict__ wo,
                                                             const char* __restrict__ wg, const char* __restrict__ wu, const char* __restrict__ wd,
                                                             char* __restrict__ ws, char* __restrict__ out) {
    extern __shared__ __attribute__((aligned(16))) char smem[];
    const int b = blockIdx.x, j = b & 7, i = b >> 3;
    const int w = __builtin_amdgcn_readfirstlane(threadIdx.x >> 6);
    const rsrc_t rs = __builtin_amdgcn_make_buffer_rsrc(ws, 0, WS_END, 0x00020000);
    const int rb = 4 * (j >> 1) + (i & 3), cb = (j & 1) * 8 + (i >> 2);
    const int hd = j * 4 + (i >> 3), qb = i & 7;
    const int rb64 = j * 4 + (i & 3), cb80 = i >> 2;
    if (w == 8 || w == 9) {
        const int lane = lane_id(), lw = w - 8, rk = first_stage(j, cb);
        LOADER_SETUP();
        H_X(0); H_X(1); H_Q(0, 0);
    }
    if (w >= 8) {
        const Ids id = {rb, cb, hd, qb, rb64, cb80, first_stage(j, cb), i, cb & 3, rb & 3};
        helper_role(x, wqkv, wo, wg, wu, wd, ws, rs, (gu32*)(ws + WS_FLAG), id, w - 8);
        return;
    }
    STAMP(0, 0);
    f32x16 gacc[3];
    bf16x8 gx[4], gw[4][3];
    if (cb < 10) front_phase<false>(ws, rs, rb, cb, w, gacc, gx, gw); else front_phase<true>(ws, rs, rb, cb, w, gacc, gx, gw);
    STAMP(0, 3);

    {
        const int lane = lane_id(), r = lane & 31, h = lane >> 5;
        G_ADDR();
        STAMP(1, 0);
        unsigned xs = XB(7), wsl = (UG + 2) % 3 * 24576;
#pragma unroll 1
        for (int u = UG + 1; u < 19; ++u) {
            if (u == UG + 3) VM0();
            BAR_L();
            const unsigned a_ = awA + wsl, c_ = awB + wsl;
#pragma unroll
            for (int p = 0; p < 4; ++p) {
                G_MM(p, 0); gw[p][0] = G_RD(p, 0, a_, c_); SBAR(); G_MM(p, 1); gw[p][1] = G_RD(p, 1, a_, c_); SBAR();
                G_MM(p, 2); gw[p][2] = G_RD(p, 2, a_, c_); gx[p] = lds128(LP(GAX(p, xs))); SBAR();
            }
            xs = NEXT_X(xs); wsl = NEXT_W(wsl);
        }
        const float ra = *(LAS float*)LP(SCR + (tt * 32 + r) * 4), rm = *(LAS float*)LP(SCR + 512 + (tt * 32 + r) * 4);
        f32x4 e0[6], e1[6];
        G_MM(0, 0); G_MM(0, 1); G_MM(0, 2); G_MM(1, 0); G_MM(1, 1); G_MM(1, 2);
#pragma unroll
        for (int t = 0; t < 6; ++t) { e0[t] = *(LAS f32x4*)LP(SCR + 1536 + (fh * 96 + (t >> 1) * 32 + 8 * (t & 1) + 4 * h) * 4); e1[t] = *(LAS f32x4*)LP(SCR + 1536 + (fh * 96 + (t >> 1) * 32 + 16 + 8 * (t & 1) + 4 * h) * 4); }
        SBAR();
        G_MM(2, 0); G_MM(3, 0); G_MM(2, 1); G_MM(3, 1); G_MM(2, 2); G_MM(3, 2);
        STAMP(1, 3);
        const float a2 = ra * ra;
        const unsigned hp = WS_H + ((rb * 4 + tt) * 96 + cb * 6 + 3 * fh) * 1024 + lane * 16;
#pragma unroll
        for (int a = 0; a < 3; ++a) {
            u32x4 o;
#pragma unroll
            for (int g = 0; g < 2; ++g) {
                const f32x16& c = gacc[a];
                const f32x4 gv = f32x4{c[4 * g], c[4 * g + 1], c[4 * g + 2], c[4 * g + 3]} + e0[2 * a + g] * rm;
                const f32x4 uv = (f32x4{c[8 + 4 * g], c[8 + 4 * g + 1], c[8 + 4 * g + 2], c[8 + 4 * g + 3]} + e1[2 * a + g] * rm) * a2;
                o[2 * g] = pk(__builtin_fmaxf(gv[0], 0.f) * uv[0], __builtin_fmaxf(gv[1], 0.f) * uv[1]);
                o[2 * g + 1] = pk(__builtin_fmaxf(gv[2], 0.f) * uv[2], __builtin_fmaxf(gv[3], 0.f) * uv[3]);
            }
            wst128(rs, hp + a * 1024, o);
        }
    }
    STAMP(1, 4);

    bf16x8 hf[12][2];
    const int ptt = w & 1, pkq = w >> 1, prot = 3 * (rb64 & 3);
    unsigned ho;
#define H_REQ(HI) hf[((HI) >> 1) % 12][(HI) & 1] = __builtin_bit_cast(bf16x8, __builtin_amdgcn_raw_buffer_load_b128(rs, ho + ((HI) & 1) * 256, \
        (((HI) >> 1) + prot >= 12 ? ((HI) >> 1) + prot - 12 : ((HI) >> 1) + prot) * 2048, 16))
    {
        const int lane = lane_id(), r = lane & 31, h = lane >> 5;
        f32x16 o = {};
        float l;
        const unsigned aK = lane * 16, aK2 = h ? 1536 + 320 : 1024 + r * 16 + (hd & 1) * 8, aV = 1536 + h * 384 + (r < 21 ? r : 21) * 16;
        const f32x16 zero = {};
        BAR_L();
        STAMP(2, 0);
        const bf16x8 q1 = lds128(LP(49152 + w * 1536 + lane * 16));
        bf16x4 q2 = lds64(LP(49152 + w * 1536 + 1024 + r * 16 + (hd & 1) * 8));
        if (h) q2 = bf16x4{0, 0, 0, 0};
        constexpr unsigned B0 = KVB;
        bf16x8 k1 = lds128(LP(B0 + aK)); bf16x4 k2 = lds64(LP(B0 + aK2));
        f32x16 sA = MFMA8(k2, q2, MFMA16(k1, q1, zero));
        { const float m = max16(sA); if (h) q2[0] = (short)(pk(-m, 0.f) & 0xffffu); }
        sA = MFMA8(k2, q2, MFMA16(k1, q1, zero));
        k1 = lds128(LP(B0 + 3072 + aK)); k2 = lds64(LP(B0 + 3072 + aK2));
        bf16x8 v0 = lds128(LP(B0 + aV)), v1 = v0;
        f32x16 sB;
        u32x4 p0, p1 = {0u, 0u, 0u, 0u};
        float e12 = 0.f, e13 = 0.f, e14 = 0.f, e15 = 0.f;
#define EX(v) __builtin_amdgcn_exp2f(v)
#define ATT_STEP(SC, SN, K1A, K2A, V1A, V0A, HI) do { \
        SN = MFMA16(k1, q1, zero); SBAR(); \
        k1 = lds128(K1A); \
        p1[2] = pk(e12, e13); p1[3] = pk(e14, e15); PIN(p1); \
        float e0 = EX(SC[0]), e1 = EX(SC[1]), e2 = EX(SC[2]), e3 = EX(SC[3]); PIN(e0); PIN(e1); PIN(e2); PIN(e3); SBAR(); \
        SN = MFMA8(k2, q2, SN); SBAR(); \
        k2 = lds64(K2A); \
        if ((HI) >= 0) H_REQ(HI); \
        p0[0] = pk(e0, e1); p0[1] = pk(e2, e3); PIN(p0); \
        float e4 = EX(SC[4]), e5 = EX(SC[5]), e6 = EX(SC[6]), e7 = EX(SC[7]); PIN(e4); PIN(e5); PIN(e6); PIN(e7); SBAR(); \
        o = MFMA16(v1, __builtin_bit_cast(bf16x8, p1), o); SBAR(); \
        v1 = lds128(V1A); \
        p0[2] = pk(e4, e5); p0[3] = pk(e6, e7); PIN(p0); \
        float e8 = EX(SC[8]), e9 = EX(SC[9]), e10 = EX(SC[10]), e11 = EX(SC[11]); PIN(e8); PIN(e9); PIN(e10); PIN(e11); SBAR(); \
        o = MFMA16(v0, __builtin_bit_cast(bf16x8, p0), o); SBAR(); \
        v0 = lds128(V0A); \
        p1[0] = pk(e8, e9); p1[1] = pk(e10, e11); PIN(p1); \
        e12 = EX(SC[12]); e13 = EX(SC[13]); e14 = EX(SC[14]); e15 = EX(SC[15]); PIN(e12); PIN(e13); PIN(e14); PIN(e15); SBAR(); \
    } while (0)
#define ATT_T(SC, SN, t, HI) ATT_STEP(SC, SN, cK + ((t) + 2) * 3072, cK2 + ((t) + 2) * 3072, cV + (t) * 3072 + 768, cV + ((t) + 1) * 3072, HI)
#define ATT_STAGE(H0, WAITC) do { \
        const unsigned nxt = cur == KVB + 49152 ? KVB : cur + 24576; \
        LAS char *cK = LP(aK + cur), *cK2 = LP(aK2 + cur), *cV = LP(aV + cur); \
        ATT_T(sA, sB, 0, H0); ATT_T(sB, sA, 1, (H0) < 0 ? -1 : (H0) + 1); ATT_T(sA, sB, 2, (H0) < 0 ? -1 : (H0) + 2); ATT_T(sB, sA, 3, (H0) < 0 ? -1 : (H0) + 3); \
        ATT_T(sA, sB, 4, (H0) < 0 ? -1 : (H0) + 4); ATT_T(sB, sA, 5, (H0) < 0 ? -1 : (H0) + 5); \
        WAITC; \
        cK = LP(aK + nxt); cK2 = LP(aK2 + nxt); \
        ATT_STEP(sA, sB, cK, cK2, cV + 6 * 3072 + 768, cV + 7 * 3072, (H0) < 0 ? -1 : (H0) + 6); \
        ATT_STEP(sB, sA, cK + 3072, cK2 + 3072, cV + 7 * 3072 + 768, LP(aV + nxt), (H0) < 0 ? -1 : (H0) + 7); \
        cur = nxt; } while (0)
        unsigned cur = B0;
#pragma unroll 1
        for (int s = 0; s < 6; ++s) ATT_STAGE(-1, VM_BAR(0));
        ho = WS_H + ((rb64 * 2 + ptt) * 96 + pkq * 24 + h) * 1024 + ((lane >> 4) & 1) * 512 + (lane & 15) * 16;
        ATT_STAGE(0, BAR());
        ATT_STAGE(8, BAR());
        p1[2] = pk(e12, e13); p1[3] = pk(e14, e15);
        o = MFMA16(v1, __builtin_bit_cast(bf16x8, p1), o);
#undef ATT_STEP
#undef ATT_T
#undef ATT_STAGE
        STAMP(2, 3);
        { auto rr = __builtin_amdgcn_permlane32_swap(__float_as_uint(o[8]), __float_as_uint(o[8]), false, false); l = __uint_as_float(rr[1]); }
        float am = 0.f;
#pragma unroll
        for (int e = 0; e < 12; ++e) am = __builtin_fmaxf(am, __builtin_fabsf(o[e]));
        const bool ok = (l > 5.4e-20f) && (l < 1.8e19f) && (am < 3.0e38f);
        if (__builtin_expect(!__all(ok), 0)) {
            float m = -INFINITY;
            o = zero;
            if (h) q2[0] = 0;
#pragma unroll 1
            for (int t = 0; t < 64; ++t) {
                const unsigned rec = WS_KV + (hd * 64 + t) * 3072;
                const bf16x8 fk1 = __builtin_bit_cast(bf16x8, wld128(rs, rec + aK)); const bf16x4 fk2 = __builtin_bit_cast(bf16x4, wld64(rs, rec + aK2));
                const bf16x8 fv0 = __builtin_bit_cast(bf16x8, wld128(rs, rec + aV)), fv1 = __builtin_bit_cast(bf16x8, wld128(rs, rec + aV + 768));
                f32x16 sf = MFMA8(fk2, q2, MFMA16(fk1, q1, zero));
                const float mn = __builtin_fmaxf(m, max16(sf));
                const float al = EX(m - mn);
                m = mn;
                u32x4 f0, f1;
#pragma unroll
                for (int e = 0; e < 16; ++e) o[e] *= al;
#pragma unroll
                for (int e = 0; e < 4; ++e) { f0[e] = pk(EX(sf[2 * e] - mn), EX(sf[2 * e + 1] - mn)); f1[e] = pk(EX(sf[8 + 2 * e] - mn), EX(sf[8 + 2 * e + 1] - mn)); }
                o = MFMA16(fv0, __builtin_bit_cast(bf16x8, f0), o); o = MFMA16(fv1, __builtin_bit_cast(bf16x8, f1), o);
            }
            auto rr = __builtin_amdgcn_permlane32_swap(__float_as_uint(o[8]), __float_as_uint(o[8]), false, false); l = __uint_as_float(rr[1]);
        }
#undef EX
        const float inv = __builtin_amdgcn_rcpf(l);
        u32x4 oa;
#pragma unroll
        for (int e = 0; e < 4; ++e) oa[e] = pk(o[2 * e] * inv, o[2 * e + 1] * inv);
        *(LAS u32x4*)LP(SCR + w * 1024 + lane * 16) = oa;
        if (h == 0) *(LAS u32x2*)LP(69632 + w * 256 + r * 8) = u32x2{pk(o[8] * inv, o[9] * inv), pk(o[10] * inv, o[11] * inv)};
    }

    {
        const int lane = lane_id(), r16 = lane & 15, q4 = lane >> 4;
        const unsigned paw = pkq * 5120 + (4 * q4 + ((lane >> 2) & 3)) * 160 + (lane & 3) * 40, awH = paw + 49152, awT = paw + 114688;
        f32x4 acc[2][5] = {};
        bf16x8 pf[2][5];
        u32x4 xv[2];
#define PO_A(rnd) (po_slot(rnd) < 49152 ? LP(paw) + po_slot(rnd) : po_slot(rnd) < 114688 ? LP(awH) + (po_slot(rnd) - 49152) : LP(awT) + (po_slot(rnd) - 114688))
#define PO_RW(rnd, nt) pf[(rnd) & 1][nt] = cat(ldstr(PO_A(rnd) + (nt) * 8), ldstr(PO_A(rnd) + (nt) * 8 + 2560))
        unsigned fo[2], fl[2];
#pragma unroll
        for (int nt = 0; nt < 5; ++nt) PO_RW(0, nt);
        SFOR(q, 17)
            if constexpr (q == 0 || (q >= 4 && q <= 12 && !(q & 1))) BAR_L();
            if constexpr (q == 0) STAMP(4, 0);
            if constexpr (q == 6) STAMP(4, 1);
            if constexpr (q >= 1 && q < 5) { H_REQ(14 + 2 * q); H_REQ(15 + 2 * q); }
            if constexpr (q == 6) {
#pragma unroll
                for (int k = 0; k < 2; ++k) {
                    const int it = (k ? 512 + (w & 1) * 64 : w * 64) + lane, row = it / 10, c = it - row * 10;
                    fo[k] = (rb64 * 64 + row) * 1280 + cb80 * 160 + c * 16; fl[k] = row * 336 + c * 32;
                    xv[k] = *(const u32x4*)(x + fo[k]);
                }
            }
            if constexpr (q == 12) {
                STAMP(4, 2);
                const unsigned op = WS_O + (rb64 * 2 + ptt) * 40960;
#pragma unroll
                for (int s = 0; s < 4; ++s)
#pragma unroll
                    for (int t = 0; t < 2; ++t) hf[s][t] = __builtin_bit_cast(bf16x8, wld128(rs, op + (8 * pkq + 2 * ((s + cb80) & 3) + (q4 >> 1)) * 1024 + (q4 & 1) * 512 + (t * 16 + r16) * 16));
#pragma unroll
                for (int t = 0; t < 2; ++t) {
                    const unsigned a = op + 32768 + (8 * pkq + 2 * q4) * 256 + (t * 16 + r16) * 8;
                    hf[4][t] = cat(__builtin_bit_cast(bf16x4, wld64(rs, a)), __builtin_bit_cast(bf16x4, wld64(rs, a + 256)));
                }
            }
            SBAR();
#pragma unroll
            for (int nt = 0; nt < 5; ++nt) {
#pragma unroll
                for (int t = 0; t < 2; ++t) {
                    acc[t][nt] = MFMA16X16(pf[q & 1][nt], hf[q % 12][t], acc[t][nt]); PIN(acc[t][nt]); SBAR();
                    if constexpr (q < 16) if (2 * nt + t < 5) PO_RW(q + 1, (2 * nt + t) % 5);
                    SBAR();
                }
            }
            if constexpr (q == 15) BAR_L();
        SEND
#undef PO_A
#undef PO_RW
        STAMP(4, 3);
#pragma unroll
        for (int Q = 0; Q < 10; ++Q) *(LAS f32x4*)(LP(pkq * 21504 + (ptt * 32 + r16) * 336 + q4 * 80) + (Q / 5) * 5376 + (Q % 5) * 16) = acc[Q / 5][Q % 5];
        BAR_L();
#pragma unroll
        for (int k = 0; k < 2; ++k) {
            if (k == 0 || w < 2) {
                f32x4 v0 = {bflo(xv[k][0]), bfhi(xv[k][0]), bflo(xv[k][1]), bfhi(xv[k][1])}, v1 = {bflo(xv[k][2]), bfhi(xv[k][2]), bflo(xv[k][3]), bfhi(xv[k][3])};
#pragma unroll
                for (int p = 0; p < 4; ++p) { v0 += *(LAS f32x4*)(LP(fl[k]) + p * 21504); v1 += *(LAS f32x4*)(LP(fl[k]) + p * 21504 + 16); }
                const u32x4 ov = {pk(v0[0], v0[1]), pk(v0[2], v0[3]), pk(v1[0], v1[1]), pk(v1[2], v1[3])};
                *(u32x4*)(out + fo[k]) = ov;
            }
        }
    }
#undef H_REQ
    STAMP(4, 4);
}

extern "C" __global__ void k_end() {}
extern "C" void kernel_launch(void* const* d_in, const int* in_sizes, int n_in, void* d_out, int out_size, void* d_ws, size_t ws_size,
                              hipStream_t stream) {
    (void)hipFuncSetAttribute((const void*)k_block, hipFuncAttributeMaxDynamicSharedMemorySize, 163840);
    k_block<<<256, 768, 163840, stream>>>((const char*)d_in[0], (const char*)d_in[1], (const char*)d_in[2], (const char*)d_in[4], (const char*)d_in[5],
                                          (const char*)d_in[6], (char*)d_ws, (char*)d_out);
}
```

```cpp
#include <hip/hip_runtime.h>
#include <hip/hip_bf16.h>
#include <stdint.h>

typedef __attribute__((ext_vector_type(8))) short bf16x8;
typedef __attribute__((ext_vector_type(4))) short bf16x4;
typedef __attribute__((ext_vector_type(16))) float f32x16;
typedef __attribute__((ext_vector_type(4))) float f32x4;
typedef __attribute__((ext_vector_type(2))) float f32x2;
typedef __attribute__((ext_vector_type(4))) unsigned u32x4;
typedef __attribute__((ext_vector_type(2))) unsigned u32x2;
typedef __attribute__((ext_vector_type(2))) __bf16 bf2;
typedef __attribute__((address_space(1))) unsigned gu32;
typedef __amdgpu_buffer_rsrc_t rsrc_t;
#define LAS __attribute__((address_space(3)))
#define DI __device__ __forceinline__

constexpr unsigned WS_Q = 0;
constexpr unsigned WS_KV = WS_Q + 32 * 64 * 1536;
constexpr unsigned WS_H = WS_KV + 32 * 64 * 3072;
constexpr unsigned WS_O = WS_H + 64 * 96 * 1024;
constexpr unsigned WS_ST = WS_O + 64 * 40 * 1024;
constexpr unsigned WS_CQ = WS_ST + 16 * 10 * 128 * 16;
constexpr unsigned WS_CG = WS_CQ + 16 * 10 * 128 * 8;
constexpr unsigned WS_FLAG = WS_CG + 16 * 10 * 192 * 8;
constexpr unsigned WS_END = WS_FLAG + 65536;
constexpr unsigned OOB = 0x7fffff00u;
constexpr int F_GEN = 0;
constexpr int F_ACK = 64;
constexpr int F_H = 1024;
constexpr int F_O = 3072;
constexpr int F_QKV = 8192;
constexpr size_t WS_DBG = 64u << 20;
#define STAMP(kid, i) do {} while (0)

#define MFMA16(a, b, c) __builtin_amdgcn_mfma_f32_32x32x16_bf16((a), (b), (c), 0, 0, 0)
#define MFMA8(a, b, c) __builtin_amdgcn_mfma_f32_32x32x8bf16_1k((a), (b), (c), 0, 0, 0)
#define MFMA16X16(a, b, c) __builtin_amdgcn_mfma_f32_16x16x32_bf16((a), (b), (c), 0, 0, 0)
#define LP(a) ((LAS char*)(uintptr_t)(a))
#define SBAR() __builtin_amdgcn_sched_barrier(0)
#define PIN(x) asm volatile("" : "+v"(x))
#define LGKM0() __builtin_amdgcn_s_waitcnt(0xC07F)
#define BAR() asm volatile("s_barrier" ::: "memory")
#define BAR_L() do { asm volatile("" ::: "memory"); LGKM0(); BAR(); } while (0)
#define VM_BAR(N) asm volatile("s_waitcnt vmcnt(" #N ")\n\ts_barrier" ::: "memory")

DI void set_m0(unsigned lds) { asm volatile("s_mov_b32 m0, %0\n\ts_nop 0" :: "s"(lds) : "memory"); }
#define GLDS(voff, sbase, OFF) asm volatile("global_load_lds_dwordx4 %0, %1 offset:" #OFF :: "v"(voff), "s"(sbase) : "memory")
#define GLDS_SC1(voff, sbase, OFF) asm volatile("global_load_lds_dwordx4 %0, %1 offset:" #OFF " sc1" :: "v"(voff), "s"(sbase) : "memory")

DI int lane_id() { int l; asm volatile("v_mbcnt_lo_u32_b32 %0, -1, 0\n\tv_mbcnt_hi_u32_b32 %0, -1, %0" : "=v"(l)); return l; }
DI bf16x8 lds128(LAS char* p) { return *(LAS bf16x8*)p; }
DI bf16x4 lds64(LAS char* p) { return *(LAS bf16x4*)p; }
DI bf16x4 ldstr(LAS char* p) { return __builtin_amdgcn_ds_read_tr16_b64_v4i16((LAS bf16x4*)p); }
DI bf16x8 cat(bf16x4 a, bf16x4 b) { return __builtin_shufflevector(a, b, 0, 1, 2, 3, 4, 5, 6, 7); }
DI unsigned pk(float a, float b) { f32x2 f = {a, b}; return __builtin_bit_cast(unsigned, __builtin_convertvector(f, bf2)); }
DI float bflo(unsigned u) { return __uint_as_float(u << 16); }
DI float bfhi(unsigned u) { return __uint_as_float(u & 0xffff0000u); }
DI float dot2(unsigned a, unsigned b, float c) { return __builtin_amdgcn_fdot2_f32_bf16(__builtin_bit_cast(bf2, a), __builtin_bit_cast(bf2, b), c, false); }
DI float swap32_add(float v) { auto r = __builtin_amdgcn_permlane32_swap(__float_as_uint(v), __float_as_uint(v), false, false); return __uint_as_float(r[0]) + __uint_as_float(r[1]); }
DI float swap32_max(float v) { auto r = __builtin_amdgcn_permlane32_swap(__float_as_uint(v), __float_as_uint(v), false, false); return __builtin_fmaxf(__uint_as_float(r[0]), __uint_as_float(r[1])); }
DI float addf(float a, float b) { float r; asm("v_add_f32 %0, %1, %2" : "=v"(r) : "v"(a), "v"(b)); return r; }
DI float mulf(float a, float b) { float r; asm("v_mul_f32 %0, %1, %2" : "=v"(r) : "v"(a), "v"(b)); return r; }
DI float fmaf1(float a, float b, float c) { float r; asm("v_fma_f32 %0, %1, %2, %3" : "=v"(r) : "v"(a), "v"(b), "v"(c)); return r; }
DI void wst128(rsrc_t rs, unsigned off, u32x4 v) { __builtin_amdgcn_raw_buffer_store_b128(v, rs, off, 0, 16); }
DI void wst64(rsrc_t rs, unsigned off, u32x2 v) { __builtin_amdgcn_raw_buffer_store_b64(v, rs, off, 0, 16); }
DI u32x4 wld128(rsrc_t rs, unsigned off) { return __builtin_amdgcn_raw_buffer_load_b128(rs, off, 0, 16); }
DI u32x2 wld64(rsrc_t rs, unsigned off) { return __builtin_amdgcn_raw_buffer_load_b64(rs, off, 0, 16); }
DI unsigned wld32(rsrc_t rs, unsigned off) { return __builtin_amdgcn_raw_buffer_load_b32(rs, off, 0, 16); }
#define POLL_SLEEP() do { __builtin_amdgcn_s_sleep(4); asm volatile("" ::: "memory"); } while (0)
DI void wait_tags(gu32* flags, int word, unsigned tag) {
    for (int spin = 0; spin < (1 << 18); ++spin) {
        const unsigned v = __hip_atomic_load(flags + word, __ATOMIC_RELAXED, __HIP_MEMORY_SCOPE_AGENT);
        if (__all(v == tag)) break;
        POLL_SLEEP();
    }
    asm volatile("" ::: "memory");
}
DI void wait_tags2(gu32* flags, int word0, int word1, unsigned tag) {
    for (int spin = 0; spin < (1 << 18); ++spin) {
        const unsigned v0 = __hip_atomic_load(flags + word0, __ATOMIC_RELAXED, __HIP_MEMORY_SCOPE_AGENT);
        const unsigned v1 = __hip_atomic_load(flags + word1, __ATOMIC_RELAXED, __HIP_MEMORY_SCOPE_AGENT);
        if (__all(v0 == tag && v1 == tag)) break;
        POLL_SLEEP();
    }
    asm volatile("" ::: "memory");
}
DI void publish(gu32* flags, int word, bool on, unsigned tag) { if (on) __hip_atomic_store(flags + word, tag, __ATOMIC_RELAXED, __HIP_MEMORY_SCOPE_AGENT); }

DI float max16(const f32x16& s) {
    float a = __builtin_fmaxf(__builtin_fmaxf(s[0], s[1]), s[2]), b = __builtin_fmaxf(__builtin_fmaxf(s[3], s[4]), s[5]);
    a = __builtin_fmaxf(__builtin_fmaxf(a, s[6]), s[7]); b = __builtin_fmaxf(__builtin_fmaxf(b, s[8]), s[9]);
    a = __builtin_fmaxf(__builtin_fmaxf(a, s[10]), s[11]); b = __builtin_fmaxf(__builtin_fmaxf(b, s[12]), s[13]);
    a = __builtin_fmaxf(__builtin_fmaxf(a, s[14]), s[15]);
    return swap32_max(__builtin_fmaxf(a, b));
}

template <int I> struct IC { static constexpr int v = I; };
template <int I, int N, class F> DI void sfor(F&& f) { if constexpr (I < N) { f(IC<I>{}); sfor<I + 1, N>(f); } }
#define SFOR(var, N) sfor<0, N>([&](auto var##_) __attribute__((always_inline)) { constexpr int var = decltype(var##_)::v;
#define SEND });
#define VM0() __builtin_amdgcn_s_waitcnt(0x0F70)

constexpr unsigned WRB = 81920, SCR = 155648, KVB = 0;
constexpr unsigned po_slot_f(int r) {
    constexpr int m[17] = {0, 1, 2, 3, 4, 5, 0, 1, 2, 5, 0, 1, 6, 3, 4, 2, 5};
    return m[r] < 4 ? 73728u + m[r] * 20480u : m[r] == 4 ? 49152u : m[r] == 5 ? 0u : 24576u;
}
template <int R> struct PoSlot { static constexpr unsigned v = po_slot_f(R); };
#define po_slot(r) (PoSlot<(r)>::v)
DI int first_stage(int j, int cb) { return ((((j >> 1) + 2 * (j & 1) + ((cb >> 2) & 1)) & 3) * 5 + 1) >> 1; }
DI int kstage(int kt, int rk) { const int k = kt + rk; return k >= 10 ? k - 10 : k; }
#define GATHER2(S0, S1, G, OFF, STRIDE) do { unsigned bad_ = 0; \
        _Pragma("unroll") for (int k_ = 0; k_ < 5; ++k_) { PIN(G[k_]); bad_ |= (G[k_][1] ^ tag) | (G[k_][3] ^ tag); } \
        S0 = addf(addf(addf(addf(__uint_as_float(G[0][0]), __uint_as_float(G[1][0])), __uint_as_float(G[2][0])), __uint_as_float(G[3][0])), __uint_as_float(G[4][0])); \
        S1 = addf(addf(addf(addf(__uint_as_float(G[0][2]), __uint_as_float(G[1][2])), __uint_as_float(G[2][2])), __uint_as_float(G[3][2])), __uint_as_float(G[4][2])); \
        bool ok_ = bad_ == 0; \
        for (int spin_ = 0; spin_ < (1 << 18) && __builtin_expect(!__all(ok_), 0); ++spin_) { \
            POLL_SLEEP(); S0 = 0.f; S1 = 0.f; ok_ = true; \
            _Pragma("unroll") for (int k_ = 0; k_ < 5; ++k_) { const u32x4 g_ = wld128(rs, (OFF) + k_ * (STRIDE)); \
                S0 += __uint_as_float(g_[0]); S1 += __uint_as_float(g_[2]); ok_ = ok_ && g_[1] == tag && g_[3] == tag; } } \
        S0 = swap32_add(S0); S1 = swap32_add(S1); } while (0)
#define GATHER_REQ(G, OFF, STRIDE) do { _Pragma("unroll") for (int k_ = 0; k_ < 5; ++k_) \
        asm volatile("global_load_dwordx4 %0, %1, %2 sc1" : "=v"(G[k_]) : "v"(OFF), "s"(ws + k_ * (STRIDE)) : "memory"); } while (0)
DI unsigned x_addr(int row, int p, int h) { return row * 128 + (((2 * p + h) ^ ((row >> 1) & 7)) << 4); }
DI unsigned wq_addr(int lane, int nt, bool vt) {
    const int c = lane & 3;
    return WRB + (lane >> 5) * 2048 + (nt >> 1) * 512 + ((lane >> 2) & 3) * 128 + (((nt ^ (lane >> 3)) & 1) << 6) + ((lane >> 4) & 1) * 32 + (vt ? c : ((c & 1) << 1) | (c >> 1)) * 8;
}
DI unsigned gu_addr(int lane, int nt) {
    const int c = lane & 3, gi = (lane >> 4) & 1;
    return WRB + gi * 3072 + (lane >> 5) * 1536 + ((lane >> 2) & 3) * 192 + (nt >> 1) * 64 + (((nt ^ gi) & 1) << 5) + (((c & 1) << 1) | (c >> 1)) * 8;
}
#define VMW(N) asm volatile("s_waitcnt vmcnt(" #N ")" ::: "memory")
#define NQG 3
constexpr int UQ = 2 * NQG, UG = NQG + 10;
constexpr int g_stage(int u) { return u - UG < 5 ? 5 + u - UG : NQG + u - UG - 5; }
#define XB(L) (((L) + NQG) % 5 * 16384)
constexpr int w_count(int u) { return u < UQ ? 10 : u < UG ? 8 : u < 20 ? 12 : 0; }
constexpr int x_lo(int g) { return g < UQ - 4 ? g + 4 : g <= UG - 4 ? 2 * (g + 4 - NQG) : g < UG + 2 ? 20 : 20 + 2 * (g - UG - 2) < 30 - 2 * NQG ? 20 + 2 * (g - UG - 2) : 30 - 2 * NQG; }
constexpr int req_count(int g) { return g < 0 ? 18 : 4 * (x_lo(g + 1) - x_lo(g)) + w_count(g + 3); }
template <int N> DI void vm_bar() { asm volatile("s_waitcnt vmcnt(%0)\n\ts_barrier" :: "n"(N) : "memory"); }
struct Ids { int rb, cb, hd, qb, rb64, cb80, rk, i, sx, sw; };

#define LOADER_SETUP() \
        const unsigned vA0 = (lane >> 3) * 1280 + (((lane & 7) ^ (lane >> 4)) << 4), vA1 = (vA0 ^ 64) + 10240 - 1024; \
        const unsigned vW0 = ((lane >> 3) & 3) * 3840 + (lane >> 5) * 128 + (((lane & 7) ^ ((lane >> 2) & 4)) << 4), vW1 = vW0 + 15360 - 1024; \
        const char* xa = x + (size_t)(rb * 128 + lw * 32) * 1280; \
        const char* wb = wqkv + (size_t)lw * 16 * 3840 + (cb < 15 ? cb : 14) * 256
#define H_4(V0, V1, P, STEP) do { GLDS(V0, P, 0); GLDS(V1, P, 1024); GLDS(V0, (P) + (STEP) - 2048, 2048); GLDS(V1, (P) + (STEP) - 2048, 3072); } while (0)
#define H_X(v) do { set_m0(XB((v) >> 1) + ((v) & 1) * 8192 + lw * 4096); H_4(vA0, vA1, xa + kstage((v) < 20 ? (v) >> 1 : ((v) >> 1) - 10 + NQG, rk) * 128 + ((v) & 1) * 81920, 20480); } while (0)
#define H_Q(v, dst) do { set_m0(WRB + (dst) + lw * 4096); H_4(vW0, vW1, wb + (kstage((v) >> 1, rk) * 2 + ((v) & 1)) * 122880, 30720); } while (0)
#define NEXT_X(v) ((v) == 65536 ? 0u : (v) + 16384)
#define NEXT_W(v) ((v) == 49152 ? 0u : (v) + 24576)
DI void helper_role(const char* x, const char* wqkv, const char* wo, const char* wg, const char* wu, const char* wd, char* ws, rsrc_t rs, gu32* flags,
                    const Ids id, int lw) {
    const int w = lw;
    __builtin_amdgcn_s_setprio(3);
    unsigned gen, tag;
    asm volatile("global_load_dword %0, %1, %2 sc1" : "=v"(gen) : "v"(0), "s"(flags + F_GEN) : "memory");
    const int lane = lane_id(), r = lane & 31, h = lane >> 5;
    const int rb = id.rb, cb = id.cb, rk = id.rk, cbl = cb < 15 ? cb : 14;
    const unsigned voff = lane * 16;
    if (lw < 2) {
        LOADER_SETUP();
        unsigned gv[3];
#pragma unroll
        for (int p = 0; p < 3; ++p) { const int P = 64 * p + lane, row = P / 12; gv[p] = row * 3072 + (((P - row * 12) ^ (2 * lw)) << 4) + 2048 - p * 1024; }
        const char* gb = (lw ? wu : wg) + cb * 192 - 2048;
#define H_G(v, dst) do { const char* q_ = gb + (size_t)(kstage((v) >> 1, rk) * 2 + ((v) & 1)) * 98304; _Pragma("unroll") for (int st_ = 0; st_ < 2; ++st_) { \
        set_m0(WRB + (dst) + st_ * 6144 + lw * 3072); GLDS(gv[0], q_ + st_ * 49152, 0); GLDS(gv[1], q_ + st_ * 49152, 1024); GLDS(gv[2], q_ + st_ * 49152, 2048); } } while (0)
#define H_W(u) do { constexpr unsigned d_ = (u) % 3 * 24576; \
        if constexpr ((u) < UQ) { H_Q(u, d_); H_G(u, d_ + 8192); } else if constexpr ((u) < UG) { H_Q(2 * ((u) - NQG), d_); H_Q(2 * ((u) - NQG) + 1, d_ + 8192); } \
        else { H_G(2 * g_stage(u), d_); H_G(2 * g_stage(u) + 1, d_ + 12288); } } while (0)
        H_G(0, 8192); H_W(1);
        VM_BAR(10);
        H_X(2); H_X(3); H_W(2);
        SFOR(g, 19)
            vm_bar<req_count(g - 1)>();
            if constexpr (x_lo(g + 1) > x_lo(g)) H_X(x_lo(g));
            if constexpr (x_lo(g + 1) > x_lo(g) + 1) H_X(x_lo(g) + 1);
            if constexpr (g < 17) H_W(g + 3);
        SEND
        asm volatile("v_readfirstlane_b32 %0, %1" : "=s"(tag) : "v"(gen));
        tag = tag * 2654435761u + 0x9E3779B9u;
#undef H_G
#undef H_W
    } else {
        const int sv = lw - 2;
        unsigned dummy;
        {
            const char* pc; int o0, sz;
            asm volatile("s_getpc_b64 %0\n1:\n\ts_mov_b32 %1, k_block-1b\n\ts_mov_b32 %2, k_end-k_block" : "=s"(pc), "=s"(o0), "=s"(sz));
            const int o = ((id.i * 2 + sv) * 6 + (lane < 6 ? lane : 5)) * 128;
            asm volatile("global_load_dword %0, %1, %2" : "=v"(dummy) : "v"(o < sz ? o : sz), "s"(pc + o0) : "memory");
        }
        VM_BAR(1);
        asm volatile("v_readfirstlane_b32 %0, %1" : "=s"(tag) : "v"(gen));
        tag = tag * 2654435761u + 0x9E3779B9u;
        unsigned wsl = 0;
        bf16x8 fw[5][2];
        float cs[5] = {0.f, 0.f, 0.f, 0.f, 0.f};
#pragma unroll 1
        for (int g = 0; g < 7; ++g) {
            if (g == 2 * id.sw + 1 || g == 2 * id.sw + 2) {
#pragma unroll
                for (int p = 0; p < 2; ++p)
#pragma unroll
                    for (int e = 0; e < 4; ++e)
#pragma unroll
                        for (int t = 0; t < 5; ++t) cs[t] = dot2(__builtin_bit_cast(u32x4, fw[t][p])[e], 0x3F803F80u, cs[t]);
                if (g == 2 * id.sw + 2) {
#pragma unroll
                    for (int t = 0; t < 5; ++t) cs[t] = swap32_add(cs[t]);
                    const int ka = kstage(id.sw, rk);
                    const unsigned o = WS_CG + ((cb * 10 + ka) * 192 + sv * 96 + lane) * 8;
                    wst64(rs, cb < 15 ? WS_CQ + ((cb * 10 + ka) * 128 + sv * 64 + lane) * 8 : OOB, u32x2{__float_as_uint(h ? cs[1] : cs[0]), tag});
                    wst64(rs, o, u32x2{__float_as_uint(h ? cs[3] : cs[2]), tag});
                    wst64(rs, h == 0 ? o + 512 : OOB, u32x2{__float_as_uint(cs[4]), tag});
                }
            }
            if ((g >> 1) == id.sx && id.sx < 3) {
                const int row = sv * 64 + (g & 1) * 32 + r;
                float sx = 0.f, sq = 0.f;
#pragma unroll
                for (int p = 0; p < 4; ++p) {
                    const u32x4 f = __builtin_bit_cast(u32x4, lds128(LP(x_addr(row, p, h) + XB(id.sx))));
#pragma unroll
                    for (int e = 0; e < 4; ++e) { sx = dot2(f[e], 0x3F803F80u, sx); sq = dot2(f[e], f[e], sq); }
                }
                sx = swap32_add(sx); sq = swap32_add(sq);
                wst128(rs, h == 0 ? WS_ST + ((rb * 10 + kstage(id.sx, rk)) * 128 + row) * 16 : OOB, u32x4{__float_as_uint(sx), tag, __float_as_uint(sq), tag});
            }
            if ((g >> 1) == id.sw) {
#pragma unroll
                for (int t = 0; t < 5; ++t) {
                    const unsigned a = (t < 2 ? wq_addr(lane, sv * 2 + t, cb >= 10) : gu_addr(lane, 3 * sv + t - 2) + 8192) + wsl;
#pragma unroll
                    for (int p = 0; p < 2; ++p) fw[t][p] = t < 2 ? cat(ldstr(LP(a) + p * 4096), ldstr(LP(a) + p * 4096 + 1024)) : cat(ldstr(LP(a) + p * 6144), ldstr(LP(a) + p * 6144 + 768));
                }
            }
            BAR_L();
            wsl = NEXT_W(wsl);
        }
        {
            const int vh = ((cb - 10) * 128 + 19) / 20 + (lane >> 3);
            const unsigned v = (lane & 1) ? 0u : 0x3F803F80u;
            const unsigned o = (cb >= 10 && cb < 15 && vh * 20 < (cb - 9) * 128) ? WS_KV + (vh * 64 + rb * 4 + sv * 2) * 3072 + 1536 + ((lane >> 1) & 3) * 384 + 320 + (lane & 1) * 16 : OOB;
            wst128(rs, o, u32x4{v, v, v, v}); wst128(rs, o + 3072, u32x4{v, v, v, v});
        }
        u32x4 gs[2][5], gc[5];
        const unsigned so = WS_ST + ((rb * 10 + h * 5) * 128 + sv * 64 + r) * 16, co = WS_CQ + ((cbl * 10 + h * 5) * 128 + sv * 64 + 2 * r) * 8;
        const unsigned cg = WS_CG + ((cb * 10 + h * 5) * 192 + sv * 64 + 2 * r) * 8, cg2 = cg - sv * 512 + 1024;
        float s0, s1;
        SFOR(g_, 12)
            constexpr int g = g_ + 7;
            BAR_L();
            if constexpr (g == UG - 6) GATHER_REQ(gs[0], so, 2048);
            if constexpr (g == UG - 5) GATHER_REQ(gs[1], so + 512, 2048);
            if constexpr (g == UG - 4) GATHER_REQ(gc, co, 1024);
            if constexpr (g == UG - 3 || g == UG - 2) {
                constexpr int t = g - (UG - 3);
                if (t) VMW(5); else { VMW(10); PIN(dummy); }
                GATHER2(s0, s1, gs[t], so + t * 512, 2048);
                const float mu = s0 * (1.f / 640.f);
                *(LAS float*)LP(SCR + h * 512 + (sv * 64 + t * 32 + r) * 4) = h ? -mu : __builtin_amdgcn_rsqf(__builtin_fmaxf(__builtin_fmaf(-mu, mu, s1 * (1.f / 640.f)), 0.f) + 1e-5f);
            }
            if constexpr (g == UG - 1) {
                VMW(0);
                GATHER2(s0, s1, gc, co, 1024);
                *(LAS f32x2*)LP(SCR + 1024 + (sv * 64 + 2 * r) * 4) = f32x2{s0, s1};
            }
            if constexpr (g == 14) { GATHER_REQ(gc, cg, 1536); GATHER_REQ(gs[0], cg2, 1536); }
            if constexpr (g == 16) {
                VMW(0);
                GATHER2(s0, s1, gc, cg, 1536);
                *(LAS f32x2*)LP(SCR + 1536 + (sv * 64 + 2 * r) * 4) = f32x2{s0, s1};
            }
            if constexpr (g == 17) if (sv == 0) { GATHER2(s0, s1, gs[0], cg2, 1536); *(LAS f32x2*)LP(SCR + 1536 + (128 + 2 * r) * 4) = f32x2{s0, s1}; }
            if constexpr (g == UG + 3) if (sv == 1) {
                const int ty = cb / 5, cc = cb - ty * 5, gg = (r >> 1) * 5 + (r & 1) * 2 + 2 * h;
                const int blk = ty == 2 ? (r * 20 + 19 * h) >> 7 : gg >> 4;
                publish(flags, F_QKV + r * 128 + ((ty == 0 ? 4 : ty == 1 ? 0 : 2) + h) * 16 + rb, ty < 3 && blk == cc, tag);
                publish(flags, F_ACK + blockIdx.x, lane == 0, tag);
            }
        SEND
    }
    wait_tags2(flags, F_QKV + id.hd * 128 + lane, F_QKV + id.hd * 128 + 64 + (lane & 1) * 16 + id.qb * 2 + ((lane >> 1) & 1), tag);
    { const char* p_ = ws + WS_Q + (id.hd * 64 + id.qb * 8 + lw * 2) * 1536; set_m0(49152 + lw * 3072); GLDS_SC1(voff, p_, 0); GLDS_SC1(voff, p_, 1024); GLDS_SC1(voff, p_, 2048); }
    const char* kv = ws + WS_KV + (size_t)id.hd * 196608 + lw * 6144;
#define KV_DMA(s) do { const char* p_ = kv + (s) * 24576; set_m0(KVB + (s) % 3 * 24576 + lw * 6144); GLDS_SC1(voff, p_, 0); GLDS_SC1(voff, p_, 1024); GLDS_SC1(voff, p_, 2048); \
        set_m0(KVB + (s) % 3 * 24576 + lw * 6144 + 3072); GLDS_SC1(voff, p_ + 3072, 0); GLDS_SC1(voff, p_ + 3072, 1024); GLDS_SC1(voff, p_ + 3072, 2048); } while (0)
    KV_DMA(0);
    unsigned pv[5], pa[5], pb[5];
#define PO_VOFF(pv, ROW) do { _Pragma("unroll") for (int ii = 0; ii < 5; ++ii) { \
        const int P = ii * 64 + lane, rho = P / 10, c = P - rho * 10, u = rho >> 4, qq = (rho >> 2) & 3, e = rho & 3; pv[ii] = (ROW) * 1280 + c * 16 + 3072 - (ii & 3) * 1024; PIN(pv[ii]); } } while (0)
#define PO_DMA(rnd, src) PO_DMAV(pv, rnd, src)
#define PO_DMAV(pv, rnd, src) do { const char* p_ = (src); set_m0(po_slot(rnd) + lw * 5120); GLDS(pv[0], p_, 0); GLDS(pv[1], p_, 1024); GLDS(pv[2], p_, 2048); GLDS(pv[3], p_, 3072); \
        set_m0(po_slot(rnd) + lw * 5120 + 4096); GLDS(pv[4], p_, 0); } while (0)
#define PO_REG(k, src) do { const char* p_ = (src); _Pragma("unroll") for (int ii = 0; ii < 5; ++ii) \
        asm volatile("global_load_dwordx4 %0, %1, %2" : "=v"(pr[k][ii]) : "v"(pv[ii]), "s"(p_ + (ii & 3) * 1024) : "memory"); } while (0)
    PO_VOFF(pv, 8 * qq + 4 * u + e);
    const char* pw = wd + (size_t)lw * 491520 + id.cb80 * 160 - 3072;
    const char* po = wo + (size_t)lw * 204800 + id.cb80 * 160 - 3072;
    const int prot = 3 * (id.rb64 & 3);
#define PO_W(rnd) (pw + ((rnd) + prot >= 12 ? (rnd) + prot - 12 : (rnd) + prot) * 40960)
    u32x4 pr[6][5];
    VM_BAR(0);
    KV_DMA(1);
    SFOR(s, 8)
        if constexpr (s == 1) if (lw == 0) publish(flags, F_H + (rb * 2 + lane) * 32 + cb, lane < 2, tag);
        if constexpr (s < 2) { PO_DMA(2 * s, PO_W(2 * s)); PO_DMA(2 * s + 1, PO_W(2 * s + 1)); }
        if constexpr (s >= 2 && s < 5) { PO_REG(2 * s - 4, PO_W(2 * s + 2)); PO_REG(2 * s - 3, PO_W(2 * s + 3)); }
        if constexpr (s == 3) {
            if (lw == 1) wait_tags(flags, F_H + id.rb64 * 32 + (lane & 15), tag);
            if (lw == 2 && blockIdx.x == 0) {
                for (int spin = 0; spin < (1 << 18); ++spin) {
                    const u32x4 a = wld128(rs, WS_FLAG + F_ACK * 4 + lane * 16);
                    if (__all(a[0] == tag && a[1] == tag && a[2] == tag && a[3] == tag)) break;
                    POLL_SLEEP();
                }
                publish(flags, F_GEN, lane == 0, tag);
            }
        }
        if constexpr (s == 5) {
            unsigned dummy;
            asm volatile("global_load_dword %0, %1, %2\n\ts_waitcnt vmcnt(0)" : "=&v"(dummy) : "v"((lane < 50 ? lane : 49) * 128), "s"(wo + (size_t)(id.i * 4 + lw) * 6400) : "memory");
        }
        if constexpr (s == 5) if (lw == 3) {
            const int k = 8 + (lane >> 4) + prot;
            unsigned d = wld32(rs, WS_H + ((id.rb64 * 2 + (id.cb80 & 1)) * 96 + (id.cb80 >> 1) * 24 + (k >= 12 ? k - 12 : k) * 2) * 1024 + (lane & 15) * 128);
            PIN(d);
        }
        if constexpr (s == 6) { PO_VOFF(pa, 20 * (qq >> 1) + 4 * (qq & 1) + 8 * u + e); PO_VOFF(pb, 20 * (4 * (qq >> 1) + 2 * (qq & 1) + u) + 16 + e); }
        if constexpr (s == 7) PO_DMA(4, PO_W(4));
        VM_BAR(0);
        if constexpr (s < 6) KV_DMA(s + 2);
    SEND
    PO_DMA(5, PO_W(5));
#define PO_PUT(rnd) do { _Pragma("unroll") for (int ii = 0; ii < 5; ++ii) { PIN(pr[(rnd) - 6][ii]); *(LAS u32x4*)(LP(po_slot(rnd) + lw * 5120 + voff) + ii * 1024) = pr[(rnd) - 6][ii]; } } while (0)
#define PO_O(k) (po + (((k) + id.cb80) & 3) * 51200)
    BAR();
    {
        const unsigned op = WS_O + (id.qb * 8 + lw * 2) * 40960;
        const u32x4 o0 = *(LAS u32x4*)LP(SCR + lw * 2048 + voff), o1 = *(LAS u32x4*)LP(SCR + lw * 2048 + 1024 + voff), o2 = *(LAS u32x4*)LP(69632 + lw * 512 + (lane & 31) * 16);
        wst128(rs, op + id.hd * 1024 + voff, o0); wst128(rs, op + 40960 + id.hd * 1024 + voff, o1);
        wst128(rs, h ? OOB : op + (r >> 4) * 40960 + 32768 + id.hd * 256 + (r & 15) * 16, o2);
    }
    PO_PUT(6);
    PO_DMAV(pa, 12, PO_O(0));
    LGKM0(); VM_BAR(8);
    PO_PUT(7);
    VMW(5);
    publish(flags, F_O + (id.qb * 4 + lw) * 32 + id.hd, lane == 0, tag);
    PO_PUT(8); PO_DMAV(pa, 13, PO_O(1));
    BAR_L();
    PO_PUT(9); PO_PUT(10);
    BAR_L();
    PO_PUT(11); PO_DMAV(pa, 14, PO_O(2)); PO_DMAV(pa, 15, PO_O(3));
    LGKM0(); VM_BAR(15);
    PO_DMAV(pb, 16, po);
    if (lw == 1) wait_tags(flags, F_O + id.rb64 * 32 + r, tag);
    VM_BAR(0);
#undef PO_O
#undef PO_PUT
#undef KV_DMA
#undef PO_VOFF
#undef PO_DMA
#undef PO_DMAV
#undef PO_REG
#undef PO_W
}

#define G_RDO(p, a, A, B, O) ((a) == 1 ? cat(ldstr(LP(B) + (O) + (p) * 6144), ldstr(LP(B) + (O) + (p) * 6144 + 768)) : cat(ldstr(LP(A) + (O) + (p) * 6144 + (a) * 32), ldstr(LP(A) + (O) + (p) * 6144 + (a) * 32 + 768)))
#define G_RD(p, a, A, B) G_RDO(p, a, A, B, 0)
#define G_MM(p, a) do { gacc[a] = MFMA16(gw[p][a], gx[p], gacc[a]); PIN(gacc[a]); SBAR(); } while (0)
#define G_ADDR() const int tt = w & 3, fh = w >> 2; unsigned gax = x_addr(tt * 32 + r, 0, h); \
        unsigned awA = gu_addr(lane, 3 * fh), awB = gu_addr(lane, 3 * fh + 1)
#define GAX(p, xs) ((gax ^ ((p) * 32)) + (xs))
template <bool VT>
DI void front_phase(char* ws, rsrc_t rs, int rb, int cb, int w, f32x16 (&gacc)[3], bf16x8 (&gx)[4], bf16x8 (&gw)[4][3]) {
    const int lane = lane_id(), r = lane & 31, h = lane >> 5;
    G_ADDR();
    unsigned aq = wq_addr(lane, 2 * fh, VT);
    PIN(gax); PIN(awA); PIN(awB); PIN(aq);
    f32x16 acc[2] = {};
    bf16x8 qw[4][2];
    gacc[0] = f32x16{}; gacc[1] = f32x16{}; gacc[2] = f32x16{};
#define Q_RD(p, A) cat(ldstr(LP(A) + (p) * 4096), ldstr(LP(A) + (p) * 4096 + 1024))
#define Q_MM(p, n) do { acc[n] = VT ? MFMA16(gx[p], qw[p][n], acc[n]) : MFMA16(qw[p][n], gx[p], acc[n]); PIN(acc[n]); SBAR(); } while (0)
#define QG_UNIT(X0, X1) do { BAR_L(); const unsigned q0_ = aq + wsl, q1_ = q0_ ^ 64, a_ = awA + 8192 + wsl, c_ = awB + 8192 + wsl; \
        _Pragma("unroll") for (int p = 0; p < 2; ++p) { \
            Q_MM(p, 0); qw[p][0] = Q_RD(p, q0_); SBAR(); G_MM(p, 0); gw[p][0] = G_RD(p, 0, a_, c_); SBAR(); Q_MM(p, 1); qw[p][1] = Q_RD(p, q1_); SBAR(); \
            G_MM(p, 1); gw[p][1] = G_RD(p, 1, a_, c_); SBAR(); G_MM(p, 2); gw[p][2] = G_RD(p, 2, a_, c_); gx[p] = lds128(LP(p ? (X1) : (X0))); SBAR(); } \
        wsl = NEXT_W(wsl); } while (0)
    BAR();
    STAMP(0, 1);
#pragma unroll
    for (int p = 0; p < 2; ++p) {
        gx[p] = lds128(LP(GAX(p, XB(0)))); qw[p][0] = Q_RD(p, aq); qw[p][1] = Q_RD(p, aq ^ 64);
#pragma unroll
        for (int a = 0; a < 3; ++a) gw[p][a] = G_RDO(p, a, awA, awB, 8192);
    }
    unsigned xs = XB(0), wsl = 24576;
#pragma unroll 1
    for (int u = 0; u < UQ - 2; u += 2) { QG_UNIT(GAX(2, xs), GAX(3, xs)); xs = NEXT_X(xs); QG_UNIT(GAX(0, xs), GAX(1, xs)); if (u == 2) STAMP(5, 1); }
    QG_UNIT(GAX(2, xs), GAX(3, xs));
    STAMP(5, 2);
    BAR_L();
    constexpr unsigned WQ0 = UQ % 3 * 24576, XQ0 = XB(NQG), WG0 = UG % 3 * 24576, WG1 = (UG + 1) % 3 * 24576;
#pragma unroll
    for (int p = 0; p < 2; ++p) {
        Q_MM(p, 0); qw[p][0] = Q_RD(p, aq + WQ0); SBAR(); G_MM(p, 0); qw[p + 2][0] = Q_RD(p + 2, aq + WQ0); SBAR(); Q_MM(p, 1); qw[p][1] = Q_RD(p, (aq ^ 64) + WQ0); SBAR();
        G_MM(p, 1); qw[p + 2][1] = Q_RD(p + 2, (aq ^ 64) + WQ0); SBAR(); G_MM(p, 2); gx[p] = lds128(LP(GAX(p, XQ0))); gx[p + 2] = lds128(LP(GAX(p + 2, XQ0))); SBAR();
    }
    xs = XB(NQG + 1); wsl = (UQ + 1) % 3 * 24576;
#pragma unroll 1
    for (int u = UQ; u < UG - 1; ++u) {
        BAR_L();
        const unsigned q0 = aq + wsl, q1 = q0 ^ 64;
#pragma unroll
        for (int p = 0; p < 4; ++p) { Q_MM(p, 0); qw[p][0] = Q_RD(p, q0); SBAR(); Q_MM(p, 1); qw[p][1] = Q_RD(p, q1); gx[p] = lds128(LP(GAX(p, xs))); SBAR(); }
        xs = NEXT_X(xs); wsl = NEXT_W(wsl);
    }
    STAMP(5, 3);
    BAR_L();
    awA += WG0; awB += WG0;
#pragma unroll
    for (int p = 0; p < 4; ++p) {
        Q_MM(p, 0); gw[p][0] = G_RD(p, 0, awA, awB); gw[p][1] = G_RD(p, 1, awA, awB); SBAR();
        Q_MM(p, 1); gw[p][2] = G_RD(p, 2, awA, awB); gx[p] = lds128(LP(GAX(p, XB(5)))); SBAR();
    }
#undef Q_RD
#undef Q_MM
#undef QG_UNIT
    STAMP(0, 2);
    BAR_L();
    awA += WG1 - WG0; awB += WG1 - WG0;
    PIN(gax);
    const int ln = lane_id(), r4 = (ln & 31) * 4, h16 = (ln >> 5) * 16;
    const unsigned rec = cb < 5 ? 1536 : 3072;
    const int tile = rb * 4 + tt;
    float sa, sb;
    f32x4 e0, e1;
    u32x4 ov;
    if (!VT) {
        sa = *(LAS float*)LP(SCR + tt * 128 + r4); sb = *(LAS float*)LP(SCR + 512 + tt * 128 + r4);
        if (cb < 5) sa *= 0.22360679774997896f * 1.4426950408889634f;
    }
#pragma unroll
    for (int p = 0; p < 4; ++p) {
        const int n = p >> 1, o = p & 1;
        const f32x16& c = acc[n];
#pragma unroll
        for (int a = 0; a < 3; ++a) {
            if (a < 2) {
                if (!VT) e1 = *(LAS f32x4*)(LP(SCR + 1024 + fh * 256 + h16) + p * 64 + a * 32);
                else {
                    e0 = *(LAS f32x4*)(LP(SCR + tt * 128 + h16) + o * 64 + a * 32); e1 = *(LAS f32x4*)(LP(SCR + tt * 128 + h16) + 512 + o * 64 + a * 32);
                    if (a == 0) sb = *(LAS float*)(LP(SCR + 1024 + fh * 256 + r4) + n * 128);
                }
            }
            SBAR();
            G_MM(p, a);
            if (p > 0) { gw[p - 1][a] = G_RD(p - 1, a, awA, awB); if (a == 2) gx[p - 1] = lds128(LP(GAX(p - 1, XB(6)))); }
            if (p == 3) { gw[3][a] = G_RD(3, a, awA, awB); if (a == 2) gx[3] = lds128(LP(GAX(3, XB(6)))); }
            if (a < 2) {
                float v[4];
#pragma unroll
                for (int e = 0; e < 4; ++e) v[e] = mulf(fmaf1(e1[e], sb, c[8 * o + 4 * a + e]), VT ? e0[e] : sa);
                ov[2 * a] = pk(v[0], v[1]); ov[2 * a + 1] = pk(v[2], v[3]);
                PIN(ov);
            } else if (!VT) {
                unsigned o2[2];
#pragma unroll
                for (int u = 0; u < 2; ++u) {
                    const int Gc = (cb % 5) * 16 + fh * 8 + p * 2 + u, P = Gc / 5, gp = Gc - P * 5;
                    o2[u] = (cb < 5 ? WS_Q : WS_KV) + tile * rec + (2 * P + (gp > 2)) * 64 * rec + (gp > 2 ? gp - 3 : gp) * 512 + (gp == 2);
                }
                const unsigned off = (h16 ? o2[1] : o2[0]) + r4 * 4;
                wst128(rs, off & ~1u, ov);
                if ((o2[0] | o2[1]) & 1) wst128(rs, (off & 1) ? off - 1 + 64 * rec : OOB, ov);
            } else {
                const int col = (cb - 10) * 128 + fh * 64 + n * 32 + (r4 >> 2), vh = col / 20, d = col - vh * 20;
                wst128(rs, cb < 15 ? WS_KV + (vh * 64 + tile) * 3072 + 1536 + h16 * 24 + d * 16 + o * 768 : OOB, ov);
            }
            SBAR();
        }
    }
}

extern "C" __global__ __launch_bounds__(768, 3) void k_block(const char* __restrict__ x, const char* __restrict__ wqkv, const char* __restrict__ wo,
                                                             const char* __restrict__ wg, const char* __restrict__ wu, const char* __restrict__ wd,
                                                             char* __restrict__ ws, char* __restrict__ out) {
    extern __shared__ __attribute__((aligned(16))) char smem[];
    const int b = blockIdx.x, j = b & 7, i = b >> 3;
    const int w = __builtin_amdgcn_readfirstlane(threadIdx.x >> 6);
    const rsrc_t rs = __builtin_amdgcn_make_buffer_rsrc(ws, 0, WS_END, 0x00020000);
    const int rb = 4 * (j >> 1) + (i & 3), cb = (j & 1) * 8 + (i >> 2);
    const int hd = j * 4 + (i >> 3), qb = i & 7;
    const int rb64 = j * 4 + (i & 3), cb80 = i >> 2;
    if (w == 8 || w == 9) {
        const int lane = lane_id(), lw = w - 8, rk = first_stage(j, cb);
        LOADER_SETUP();
        H_X(0); H_X(1); H_Q(0, 0);
    }
    if (w >= 8) {
        const Ids id = {rb, cb, hd, qb, rb64, cb80, first_stage(j, cb), i, cb & 3, rb & 3};
        helper_role(x, wqkv, wo, wg, wu, wd, ws, rs, (gu32*)(ws + WS_FLAG), id, w - 8);
        return;
    }
    STAMP(0, 0);
    f32x16 gacc[3];
    bf16x8 gx[4], gw[4][3];
    if (cb < 10) front_phase<false>(ws, rs, rb, cb, w, gacc, gx, gw); else front_phase<true>(ws, rs, rb, cb, w, gacc, gx, gw);
    STAMP(0, 3);

    {
        const int lane = lane_id(), r = lane & 31, h = lane >> 5;
        G_ADDR();
        STAMP(1, 0);
        unsigned xs = XB(7), wsl = (UG + 2) % 3 * 24576;
#pragma unroll 1
        for (int u = UG + 1; u < 19; ++u) {
            if (u == UG + 3) VM0();
            BAR_L();
            const unsigned a_ = awA + wsl, c_ = awB + wsl;
#pragma unroll
            for (int p = 0; p < 4; ++p) {
                G_MM(p, 0); gw[p][0] = G_RD(p, 0, a_, c_); SBAR(); G_MM(p, 1); gw[p][1] = G_RD(p, 1, a_, c_); SBAR();
                G_MM(p, 2); gw[p][2] = G_RD(p, 2, a_, c_); gx[p] = lds128(LP(GAX(p, xs))); SBAR();
            }
            xs = NEXT_X(xs); wsl = NEXT_W(wsl);
        }
        const float ra = *(LAS float*)LP(SCR + (tt * 32 + r) * 4), rm = *(LAS float*)LP(SCR + 512 + (tt * 32 + r) * 4);
        f32x4 e0[6], e1[6];
        G_MM(0, 0); G_MM(0, 1); G_MM(0, 2); G_MM(1, 0); G_MM(1, 1); G_MM(1, 2);
#pragma unroll
        for (int t = 0; t < 6; ++t) { e0[t] = *(LAS f32x4*)LP(SCR + 1536 + (fh * 96 + (t >> 1) * 32 + 8 * (t & 1) + 4 * h) * 4); e1[t] = *(LAS f32x4*)LP(SCR + 1536 + (fh * 96 + (t >> 1) * 32 + 16 + 8 * (t & 1) + 4 * h) * 4); }
        SBAR();
        G_MM(2, 0); G_MM(3, 0); G_MM(2, 1); G_MM(3, 1); G_MM(2, 2); G_MM(3, 2);
        STAMP(1, 3);
        const float a2 = ra * ra;
        const unsigned hp = WS_H + ((rb * 4 + tt) * 96 + cb * 6 + 3 * fh) * 1024 + lane * 16;
#pragma unroll
        for (int a = 0; a < 3; ++a) {
            u32x4 o;
#pragma unroll
            for (int g = 0; g < 2; ++g) {
                const f32x16& c = gacc[a];
                const f32x4 gv = f32x4{c[4 * g], c[4 * g + 1], c[4 * g + 2], c[4 * g + 3]} + e0[2 * a + g] * rm;
                const f32x4 uv = (f32x4{c[8 + 4 * g], c[8 + 4 * g + 1], c[8 + 4 * g + 2], c[8 + 4 * g + 3]} + e1[2 * a + g] * rm) * a2;
                o[2 * g] = pk(__builtin_fmaxf(gv[0], 0.f) * uv[0], __builtin_fmaxf(gv[1], 0.f) * uv[1]);
                o[2 * g + 1] = pk(__builtin_fmaxf(gv[2], 0.f) * uv[2], __builtin_fmaxf(gv[3], 0.f) * uv[3]);
            }
            wst128(rs, hp + a * 1024, o);
        }
    }
    STAMP(1, 4);

    bf16x8 hf[12][2];
    const int ptt = w & 1, pkq = w >> 1, prot = 3 * (rb64 & 3);
    unsigned ho;
#define H_REQ(HI) hf[((HI) >> 1) % 12][(HI) & 1] = __builtin_bit_cast(bf16x8, __builtin_amdgcn_raw_buffer_load_b128(rs, ho + ((HI) & 1) * 256, \
        (((HI) >> 1) + prot >= 12 ? ((HI) >> 1) + prot - 12 : ((HI) >> 1) + prot) * 2048, 16))
    {
        const int lane = lane_id(), r = lane & 31, h = lane >> 5;
        f32x16 o = {};
        float l;
        const unsigned aK = lane * 16, aK2 = h ? 1536 + 320 : 1024 + r * 16 + (hd & 1) * 8, aV = 1536 + h * 384 + (r < 21 ? r : 21) * 16;
        const f32x16 zero = {};
        BAR_L();
        STAMP(2, 0);
        const bf16x8 q1 = lds128(LP(49152 + w * 1536 + lane * 16));
        bf16x4 q2 = lds64(LP(49152 + w * 1536 + 1024 + r * 16 + (hd & 1) * 8));
        if (h) q2 = bf16x4{0, 0, 0, 0};
        constexpr unsigned B0 = KVB;
        bf16x8 k1 = lds128(LP(B0 + aK)); bf16x4 k2 = lds64(LP(B0 + aK2));
        f32x16 sA = MFMA8(k2, q2, MFMA16(k1, q1, zero));
        { const float m = max16(sA); if (h) q2[0] = (short)(pk(-m, 0.f) & 0xffffu); }
        sA = MFMA8(k2, q2, MFMA16(k1, q1, zero));
        k1 = lds128(LP(B0 + 3072 + aK)); k2 = lds64(LP(B0 + 3072 + aK2));
        bf16x8 v0 = lds128(LP(B0 + aV)), v1 = v0;
        f32x16 sB;
        u32x4 p0, p1 = {0u, 0u, 0u, 0u};
        float e12 = 0.f, e13 = 0.f, e14 = 0.f, e15 = 0.f;
#define EX(v) __builtin_amdgcn_exp2f(v)
#define ATT_STEP(SC, SN, K1A, K2A, V1A, V0A, HI) do { \
        SN = MFMA16(k1, q1, zero); SBAR(); \
        k1 = lds128(K1A); \
        p1[2] = pk(e12, e13); p1[3] = pk(e14, e15); PIN(p1); \
        float e0 = EX(SC[0]), e1 = EX(SC[1]), e2 = EX(SC[2]), e3 = EX(SC[3]); PIN(e0); PIN(e1); PIN(e2); PIN(e3); SBAR(); \
        SN = MFMA8(k2, q2, SN); SBAR(); \
        k2 = lds64(K2A); \
        if ((HI) >= 0) H_REQ(HI); \
        p0[0] = pk(e0, e1); p0[1] = pk(e2, e3); PIN(p0); \
        float e4 = EX(SC[4]), e5 = EX(SC[5]), e6 = EX(SC[6]), e7 = EX(SC[7]); PIN(e4); PIN(e5); PIN(e6); PIN(e7); SBAR(); \
        o = MFMA16(v1, __builtin_bit_cast(bf16x8, p1), o); SBAR(); \
        v1 = lds128(V1A); \
        p0[2] = pk(e4, e5); p0[3] = pk(e6, e7); PIN(p0); \
        float e8 = EX(SC[8]), e9 = EX(SC[9]), e10 = EX(SC[10]), e11 = EX(SC[11]); PIN(e8); PIN(e9); PIN(e10); PIN(e11); SBAR(); \
        o = MFMA16(v0, __builtin_bit_cast(bf16x8, p0), o); SBAR(); \
        v0 = lds128(V0A); \
        p1[0] = pk(e8, e9); p1[1] = pk(e10, e11); PIN(p1); \
        e12 = EX(SC[12]); e13 = EX(SC[13]); e14 = EX(SC[14]); e15 = EX(SC[15]); PIN(e12); PIN(e13); PIN(e14); PIN(e15); SBAR(); \
    } while (0)
#define ATT_T(SC, SN, t, HI) ATT_STEP(SC, SN, cK + ((t) + 2) * 3072, cK2 + ((t) + 2) * 3072, cV + (t) * 3072 + 768, cV + ((t) + 1) * 3072, HI)
#define ATT_STAGE(H0, WAITC) do { \
        const unsigned nxt = cur == KVB + 49152 ? KVB : cur + 24576; \
        LAS char *cK = LP(aK + cur), *cK2 = LP(aK2 + cur), *cV = LP(aV + cur); \
        ATT_T(sA, sB, 0, H0); ATT_T(sB, sA, 1, (H0) < 0 ? -1 : (H0) + 1); ATT_T(sA, sB, 2, (H0) < 0 ? -1 : (H0) + 2); ATT_T(sB, sA, 3, (H0) < 0 ? -1 : (H0) + 3); \
        ATT_T(sA, sB, 4, (H0) < 0 ? -1 : (H0) + 4); ATT_T(sB, sA, 5, (H0) < 0 ? -1 : (H0) + 5); \
        WAITC; \
        cK = LP(aK + nxt); cK2 = LP(aK2 + nxt); \
        ATT_STEP(sA, sB, cK, cK2, cV + 6 * 3072 + 768, cV + 7 * 3072, (H0) < 0 ? -1 : (H0) + 6); \
        ATT_STEP(sB, sA, cK + 3072, cK2 + 3072, cV + 7 * 3072 + 768, LP(aV + nxt), (H0) < 0 ? -1 : (H0) + 7); \
        cur = nxt; } while (0)
        unsigned cur = B0;
#pragma unroll 1
        for (int s = 0; s < 6; ++s) ATT_STAGE(-1, VM_BAR(0));
        ho = WS_H + ((rb64 * 2 + ptt) * 96 + pkq * 24 + h) * 1024 + ((lane >> 4) & 1) * 512 + (lane & 15) * 16;
        ATT_STAGE(0, BAR());
        ATT_STAGE(8, BAR());
        p1[2] = pk(e12, e13); p1[3] = pk(e14, e15);
        o = MFMA16(v1, __builtin_bit_cast(bf16x8, p1), o);
#undef ATT_STEP
#undef ATT_T
#undef ATT_STAGE
        STAMP(2, 3);
        { auto rr = __builtin_amdgcn_permlane32_swap(__float_as_uint(o[8]), __float_as_uint(o[8]), false, false); l = __uint_as_float(rr[1]); }
        float am = 0.f;
#pragma unroll
        for (int e = 0; e < 12; ++e) am = __builtin_fmaxf(am, __builtin_fabsf(o[e]));
        const bool ok = (l > 5.4e-20f) && (l < 1.8e19f) && (am < 3.0e38f);
        if (__builtin_expect(!__all(ok), 0)) {
            float m = -INFINITY;
            o = zero;
            if (h) q2[0] = 0;
#pragma unroll 1
            for (int t = 0; t < 64; ++t) {
                const unsigned rec = WS_KV + (hd * 64 + t) * 3072;
                const bf16x8 fk1 = __builtin_bit_cast(bf16x8, wld128(rs, rec + aK)); const bf16x4 fk2 = __builtin_bit_cast(bf16x4, wld64(rs, rec + aK2));
                const bf16x8 fv0 = __builtin_bit_cast(bf16x8, wld128(rs, rec + aV)), fv1 = __builtin_bit_cast(bf16x8, wld128(rs, rec + aV + 768));
                f32x16 sf = MFMA8(fk2, q2, MFMA16(fk1, q1, zero));
                const float mn = __builtin_fmaxf(m, max16(sf));
                const float al = EX(m - mn);
                m = mn;
                u32x4 f0, f1;
#pragma unroll
                for (int e = 0; e < 16; ++e) o[e] *= al;
#pragma unroll
                for (int e = 0; e < 4; ++e) { f0[e] = pk(EX(sf[2 * e] - mn), EX(sf[2 * e + 1] - mn)); f1[e] = pk(EX(sf[8 + 2 * e] - mn), EX(sf[8 + 2 * e + 1] - mn)); }
                o = MFMA16(fv0, __builtin_bit_cast(bf16x8, f0), o); o = MFMA16(fv1, __builtin_bit_cast(bf16x8, f1), o);
            }
            auto rr = __builtin_amdgcn_permlane32_swap(__float_as_uint(o[8]), __float_as_uint(o[8]), false, false); l = __uint_as_float(rr[1]);
        }
#undef EX
        const float inv = __builtin_amdgcn_rcpf(l);
        u32x4 oa;
#pragma unroll
        for (int e = 0; e < 4; ++e) oa[e] = pk(o[2 * e] * inv, o[2 * e + 1] * inv);
        *(LAS u32x4*)LP(SCR + w * 1024 + lane * 16) = oa;
        if (h == 0) *(LAS u32x2*)LP(69632 + w * 256 + r * 8) = u32x2{pk(o[8] * inv, o[9] * inv), pk(o[10] * inv, o[11] * inv)};
    }

    {
        const int lane = lane_id(), r16 = lane & 15, q4 = lane >> 4;
        const unsigned paw = pkq * 5120 + (4 * q4 + ((lane >> 2) & 3)) * 160 + (lane & 3) * 40, awH = paw + 49152, awT = paw + 114688;
        f32x4 acc[2][5] = {};
        bf16x8 pf[2][5];
        u32x4 xv[2];
#define PO_A(rnd) (po_slot(rnd) < 49152 ? LP(paw) + po_slot(rnd) : po_slot(rnd) < 114688 ? LP(awH) + (po_slot(rnd) - 49152) : LP(awT) + (po_slot(rnd) - 114688))
#define PO_RW(rnd, nt) pf[(rnd) & 1][nt] = cat(ldstr(PO_A(rnd) + (nt) * 8), ldstr(PO_A(rnd) + (nt) * 8 + 2560))
#define PO_ST(t, nt) *(LAS f32x4*)(LP(pkq * 21504 + (ptt * 32 + r16) * 336 + q4 * 80) + (t) * 5376 + (nt) * 16) = acc[t][nt]
        unsigned fo[2], fl[2];
#pragma unroll
        for (int nt = 0; nt < 5; ++nt) PO_RW(0, nt);
        SFOR(q, 17)
            if constexpr (q == 0 || (q >= 4 && q <= 12 && !(q & 1))) BAR_L();
            if constexpr (q == 0) STAMP(4, 0);
            if constexpr (q == 6) STAMP(4, 1);
            if constexpr (q >= 1 && q < 5) { H_REQ(14 + 2 * q); H_REQ(15 + 2 * q); }
            if constexpr (q == 6) {
#pragma unroll
                for (int k = 0; k < 2; ++k) {
                    const int it = (k ? 512 + (w & 1) * 64 : w * 64) + lane, row = it / 10, c = it - row * 10;
                    fo[k] = (rb64 * 64 + row) * 1280 + cb80 * 160 + c * 16; fl[k] = row * 336 + c * 32;
                    xv[k] = *(const u32x4*)(x + fo[k]);
                }
            }
            if constexpr (q == 12) {
                STAMP(4, 2);
                const unsigned op = WS_O + (rb64 * 2 + ptt) * 40960;
#pragma unroll
                for (int s = 0; s < 4; ++s)
#pragma unroll
                    for (int t = 0; t < 2; ++t) hf[s][t] = __builtin_bit_cast(bf16x8, wld128(rs, op + (8 * pkq + 2 * ((s + cb80) & 3) + (q4 >> 1)) * 1024 + (q4 & 1) * 512 + (t * 16 + r16) * 16));
#pragma unroll
                for (int t = 0; t < 2; ++t) {
                    const unsigned a = op + 32768 + (8 * pkq + 2 * q4) * 256 + (t * 16 + r16) * 8;
                    hf[4][t] = cat(__builtin_bit_cast(bf16x4, wld64(rs, a)), __builtin_bit_cast(bf16x4, wld64(rs, a + 256)));
                }
            }
            SBAR();
#pragma unroll
            for (int nt = 0; nt < 5; ++nt) {
#pragma unroll
                for (int t = 0; t < 2; ++t) {
                    acc[t][nt] = MFMA16X16(pf[q & 1][nt], hf[q % 12][t], acc[t][nt]); PIN(acc[t][nt]); SBAR();
                    if constexpr (q < 16) if (2 * nt + t < 5) PO_RW(q + 1, (2 * nt + t) % 5);
                    if constexpr (q == 16) if (nt > 0) PO_ST(t, nt - 1);
                    SBAR();
                }
            }
            if constexpr (q == 15) BAR_L();
        SEND
#undef PO_A
#undef PO_RW
        STAMP(4, 3);
        PO_ST(0, 4); PO_ST(1, 4);
#undef PO_ST
        BAR_L();
#pragma unroll
        for (int k = 0; k < 2; ++k) {
            if (k == 0 || w < 2) {
                f32x4 v0 = {bflo(xv[k][0]), bfhi(xv[k][0]), bflo(xv[k][1]), bfhi(xv[k][1])}, v1 = {bflo(xv[k][2]), bfhi(xv[k][2]), bflo(xv[k][3]), bfhi(xv[k][3])};
#pragma unroll
                for (int p = 0; p < 4; ++p) { v0 += *(LAS f32x4*)(LP(fl[k]) + p * 21504); v1 += *(LAS f32x4*)(LP(fl[k]) + p * 21504 + 16); }
                const u32x4 ov = {pk(v0[0], v0[1]), pk(v0[2], v0[3]), pk(v1[0], v1[1]), pk(v1[2], v1[3])};
                *(u32x4*)(out + fo[k]) = ov;
            }
        }
    }
#undef H_REQ
    STAMP(4, 4);
}

extern "C" __global__ void k_end() {}
extern "C" void kernel_launch(void* const* d_in, const int* in_sizes, int n_in, void* d_out, int out_size, void* d_ws, size_t ws_size,
                              hipStream_t stream) {
    (void)hipFuncSetAttribute((const void*)k_block, hipFuncAttributeMaxDynamicSharedMemorySize, 163840);
    k_block<<<256, 768, 163840, stream>>>((const char*)d_in[0], (const char*)d_in[1], (const char*)d_in[2], (const char*)d_in[4], (const char*)d_in[5],
                                          (const char*)d_in[6], (char*)d_ws, (char*)d_out);
}
```
